```python
import torch

def _softmax(x, axis=-1):
    return torch.softmax(x, dim=axis)
def _gelu(x):
    return torch.nn.functional.gelu(x, approximate="tanh")
def _silu(x):
    return torch.nn.functional.silu(x)
def _sigmoid(x):
    return torch.sigmoid(x)
def _layernorm(x, axis=-1, eps=1e-5):
    m = x.mean(dim=axis, keepdim=True)
    v = x.var(dim=axis, keepdim=True, unbiased=False)
    return (x - m) / torch.sqrt(v + eps)
def _scatter_add(tgt, idx, src, axis=0):
    return tgt.clone().index_add_(axis, idx, src)
def _segment_sum(src, seg, n):
    out = torch.zeros((n,) + src.shape[1:], dtype=src.dtype)
    return out.index_add_(0, seg, src)
def _pad(x, pw):
    flat = tuple(v for pair in reversed(pw) for v in pair)
    return torch.nn.functional.pad(x, flat)

kernel_name = 'gated_mix__linear_attn_block__mixer_token_mlp__prompt'

def setup_inputs():

    _w = (lambda a: (lambda t: t.to(torch.bfloat16) if t.is_floating_point() else t)(torch.as_tensor(a)))
    def _A_setup():
        B, S, D = 1, 2048, 512
        return {
            "x": _w((lambda *s: torch.randn(*s, dtype=torch.bfloat16) * 0.05)(B, S, D)),
            "wqkv": _w((lambda *s: torch.randn(*s, dtype=torch.bfloat16) * 0.4)(D, 3 * D)),
            "wo": _w((lambda *s: torch.randn(*s, dtype=torch.bfloat16) * 0.05)(D, D)),
        }
    _A_in = _A_setup()

    def _B_setup():
        B, S, D, F = 1, 2048, 512, 1376
        return {
            "x": _w((lambda *s: torch.randn(*s, dtype=torch.bfloat16) * 0.05)(B, S, D)),
            "w1": _w((lambda *s: torch.randn(*s, dtype=torch.bfloat16) * 0.4)(S, F) * (S ** -0.5)),
            "b1": _w((lambda *s: torch.randn(*s, dtype=torch.bfloat16) * 3.2)(F) * 0.02),
            "w2": _w((lambda *s: torch.randn(*s, dtype=torch.bfloat16) * 0.05)(F, S) * (F ** -0.5)),
            "b2": _w((lambda *s: torch.randn(*s, dtype=torch.bfloat16) * 0.1)(S) * 0.02),
        }
    _B_in = _B_setup()

    return {
        "A_x": _A_in["x"],
        "A_wqkv": _A_in["wqkv"],
        "A_wo": _A_in["wo"],
        "B_x": _B_in["x"],
        "B_w1": _B_in["w1"],
        "B_b1": _B_in["b1"],
        "B_w2": _B_in["w2"],
        "B_b2": _B_in["b2"],
        "gate_w": _w((lambda *s: torch.randn(*s, dtype=torch.bfloat16) * 0.05)(512) * 16.0),
    }

def reference(A_x, A_wqkv, A_wo, B_x, B_w1, B_b1, B_w2, B_b2, gate_w):
    def _A_ref(x, wqkv, wo):
        B, S, D = x.shape
        H = 32; Dh = D // H
        qkv = (lambda x, *d: x.permute(*d))((x @ wqkv).reshape(B, S, 3, H, Dh), 2, 0, 3, 1, 4)
        q, k, v = qkv[0], qkv[1], qkv[2]
        phi = lambda t: torch.where(t > 0, t + 1, torch.exp(t)) + 1e-6
        pq, pk = phi(q), phi(k)
        kv = torch.einsum("bhsd,bhse->bhde", pk, v)
        z = torch.einsum("bhsd->bhd", pk)
        num = torch.einsum("bhqd,bhde->bhqe", pq, kv)
        den = torch.einsum("bhqd,bhd->bhq", pq, z)[..., None] + 1e-6
        o = num / den
        return (lambda x, *d: x.permute(*d))(o, 0, 2, 1, 3).reshape(B, S, D) @ wo

    def _B_ref(x, w1, b1, w2, b2):
        B, S, D = x.shape
        h = _layernorm(x, axis=-1)
        u = torch.einsum("bsd,sf->bdf", h, w1) + b1
        u = _gelu(u)
        y = torch.einsum("bdf,fs->bds", u, w2) + b2
        return (lambda x, *d: x.permute(*d))(y, 0, 2, 1)

    x = A_x
    g = _sigmoid((x * gate_w).sum(-1, keepdims=True))
    a = _A_ref(x, A_wqkv, A_wo)
    b = _B_ref(x, B_w1, B_b1, B_w2, B_b2)
    return g * a + (1 - g) * b
```

```cpp
#include <hip/hip_runtime.h>
#include <hip/hip_bf16.h>
typedef __hip_bfloat16 bf16;
#define S 2048
#define D 512
#define F 1376
__device__ inline float b2f(bf16 x){return __bfloat162float(x);}
__global__ void k_stats(const bf16* x,const bf16* gw,float* h,float* g){
  int s=blockIdx.x; int t=threadIdx.x;
  float v[8]; float sum=0,dot=0;
  for(int i=0;i<8;i++){v[i]=b2f(x[s*D+t*8+i]); sum+=v[i]; dot+=v[i]*b2f(gw[t*8+i]);}
  for(int o=32;o;o>>=1){sum+=__shfl_xor(sum,o);dot+=__shfl_xor(dot,o);}
  float m=sum/D; float q=0; for(int i=0;i<8;i++){q+=(v[i]-m)*(v[i]-m);}
  for(int o=32;o;o>>=1){q+=__shfl_xor(q,o);}
  float r=1.f/sqrtf(q/D+1e-5f);
  for(int i=0;i<8;i++)h[s*D+t*8+i]=(v[i]-m)*r;
  if(t==0)g[s]=1.f/(1.f+expf(-dot));
}
__global__ void k_qkv(const bf16* x,const bf16* w,float* qkv){
  int n=blockIdx.x*256+threadIdx.x; int s=blockIdx.y;
  float a=0; for(int k=0;k<D;k++)a+=b2f(x[s*D+k])*b2f(w[k*1536+n]);
  if(n<1024){a=(a>0?a+1.f:expf(a))+1e-6f;}
  qkv[s*1536+n]=a;
}
__global__ void k_kv(const float* qkv,float* kv,float* z){
  int h=blockIdx.x; int d=threadIdx.x/16,e=threadIdx.x%16;
  float a=0,zz=0; for(int s=0;s<S;s++){float pk=qkv[s*1536+512+h*16+d]; a+=pk*qkv[s*1536+1024+h*16+e]; zz+=pk;}
  kv[h*256+d*16+e]=a; if(e==0)z[h*16+d]=zz;
}
__global__ void k_o(const float* qkv,const float* kv,const float* z,float* o){
  int s=blockIdx.x; int c=threadIdx.x; int h=c/16,e=c%16;
  float num=0,den=0; for(int d=0;d<16;d++){float pq=qkv[s*1536+h*16+d]; num+=pq*kv[h*256+d*16+e]; den+=pq*z[h*16+d];}
  o[s*D+c]=num/(den+1e-6f);
}
__global__ void k_u(const float* h,const bf16* w1,const bf16* b1,float* u){
  int f=blockIdx.x*32+threadIdx.x%32; int d=blockIdx.y*8+threadIdx.x/32;
  float a=0; for(int s=0;s<S;s++)a+=h[s*D+d]*b2f(w1[s*F+f]);
  a+=b2f(b1[f]);
  float t=0.7978845608028654f*(a+0.044715f*a*a*a);
  u[d*F+f]=0.5f*a*(1.f+tanhf(t));
}
__global__ void k_out(const float* u,const bf16* w2,const bf16* b2,const float* o,const bf16* wo,const float* g,bf16* out){
  int s=blockIdx.x*32+threadIdx.x%32; int d=blockIdx.y*8+threadIdx.x/32;
  float y=0; for(int f=0;f<F;f++)y+=u[d*F+f]*b2f(w2[f*S+s]);
  y+=b2f(b2[s]);
  float a=0; for(int k=0;k<D;k++)a+=o[s*D+k]*b2f(wo[k*D+d]);
  float gg=g[s];
  out[s*D+d]=__float2bfloat16(gg*a+(1.f-gg)*y);
}
extern "C" void kernel_launch(void* const* d_in, const int* in_sizes, int n_in,
                              void* d_out, int out_size, void* d_ws, size_t ws_size,
                              hipStream_t stream) {
  const bf16* x=(const bf16*)d_in[0]; const bf16* wqkv=(const bf16*)d_in[1]; const bf16* wo=(const bf16*)d_in[2];
  const bf16* w1=(const bf16*)d_in[4]; const bf16* b1=(const bf16*)d_in[5]; const bf16* w2=(const bf16*)d_in[6]; const bf16* b2=(const bf16*)d_in[7]; const bf16* gw=(const bf16*)d_in[8];
  float* ws=(float*)d_ws;
  float* h=ws; float* g=h+S*D; float* qkv=g+S; float* kv=qkv+S*1536; float* z=kv+32*256; float* o=z+512; float* u=o+S*D;
  k_stats<<<S,64,0,stream>>>(x,gw,h,g);
  k_qkv<<<dim3(6,S),256,0,stream>>>(x,wqkv,qkv);
  k_kv<<<32,256,0,stream>>>(qkv,kv,z);
  k_o<<<S,512,0,stream>>>(qkv,kv,z,o);
  k_u<<<dim3(F/32,D/8),256,0,stream>>>(h,w1,b1,u);
  k_out<<<dim3(S/32,D/8),256,0,stream>>>(u,w2,b2,o,wo,g,(bf16*)d_out);
}
```

```cpp
#include <hip/hip_runtime.h>
#include <hip/hip_bf16.h>

#pragma clang diagnostic ignored "-Wint-to-pointer-cast"
#define DI __device__ __forceinline__
#define LAS __attribute__((address_space(3)))
typedef unsigned u32;
typedef u32 u32x4 __attribute__((ext_vector_type(4)));
typedef u32 u32x2 __attribute__((ext_vector_type(2)));
typedef float f32x2 __attribute__((ext_vector_type(2)));
typedef float f32x4 __attribute__((ext_vector_type(4)));
typedef float f32x16 __attribute__((ext_vector_type(16)));
typedef __bf16 bf16x8 __attribute__((ext_vector_type(8)));
typedef __bf16 bf16x2 __attribute__((ext_vector_type(2)));
typedef short s16x4 __attribute__((ext_vector_type(4)));

constexpr u32 WS_E = 0, WS_HF = 4096, WS_UF = 8192, WS_KF = 12288, WS_OF = 16384, WS_G = 32768;
constexpr u32 WS_KV = 65536, WS_Z = WS_KV + 262144, WS_H = 1u << 20, WS_U = 3u << 20, WS_O = 5u << 20, WS_END = 7u << 20;
constexpr u32 UDB = 88 * 1024;
constexpr int SPIN_MAX = 20000;

#define TS(i)

DI u32x4 srd(const void* p, u32 bytes) { const unsigned long long a = (unsigned long long)p; return u32x4{(u32)a, (u32)(a >> 32), bytes, 0x00020000u}; }
DI f32x16 mfma(u32x4 a, u32x4 b, f32x16 c) { return __builtin_amdgcn_mfma_f32_32x32x16_bf16(__builtin_bit_cast(bf16x8, a), __builtin_bit_cast(bf16x8, b), c, 0, 0, 0); }
DI u32x2 trd(u32 a) { return __builtin_bit_cast(u32x2, __builtin_amdgcn_ds_read_tr16_b64_v4i16((LAS s16x4*)a)); }
DI u32x4 trd2(u32 a, u32 b) { const u32x2 x = trd(a), y = trd(b); return u32x4{x[0], x[1], y[0], y[1]}; }
DI u32x4 lds128(u32 a) { return *(LAS u32x4*)a; }
DI f32x4 ldsf4(u32 a) { return *(LAS f32x4*)a; }
DI u32 pk(float a, float b) { return __builtin_bit_cast(u32, __builtin_convertvector(f32x2{a, b}, bf16x2)); }
DI float blo(u32 v) { return __uint_as_float(v << 16); }
DI float bhi(u32 v) { return __uint_as_float(v & 0xffff0000u); }
DI u32x4 pack8(const f32x16& x, int o) { return u32x4{pk(x[o], x[o + 1]), pk(x[o + 2], x[o + 3]), pk(x[o + 4], x[o + 5]), pk(x[o + 6], x[o + 7])}; }

DI void glds(u32 lds, u32 voff, u32x4 rs, u32 soff) {
  asm volatile("s_mov_b32 m0, %0\n\ts_nop 0\n\tbuffer_load_dwordx4 %1, %2, %3 offen lds" ::"s"(lds), "v"(voff), "s"(rs), "s"(soff) : "memory");
}
template <int OFF> DI void ld128c(u32x4& d, u32 voff, u32x4 rs, u32 soff) {
  asm volatile("buffer_load_dwordx4 %0, %1, %2, %3 offen offset:%4 sc1" : "=v"(d) : "v"(voff), "s"(rs), "s"(soff), "i"(OFF) : "memory");
}
DI void ld128(u32x4& d, u32 voff, u32x4 rs, u32 soff) { asm volatile("buffer_load_dwordx4 %0, %1, %2, %3 offen" : "=v"(d) : "v"(voff), "s"(rs), "s"(soff) : "memory"); }
DI void ld32c(u32& d, u32 voff, u32x4 rs, u32 soff) { asm volatile("buffer_load_dword %0, %1, %2, %3 offen sc1" : "=v"(d) : "v"(voff), "s"(rs), "s"(soff) : "memory"); }
DI void st128c(u32x4 v, u32 voff, u32x4 rs, u32 soff) { asm volatile("buffer_store_dwordx4 %0, %1, %2, %3 offen sc1\n\ts_nop 1" ::"v"(v), "v"(voff), "s"(rs), "s"(soff) : "memory"); }
DI void st32c(u32 v, u32 voff, u32x4 rs, u32 soff) { asm volatile("buffer_store_dword %0, %1, %2, %3 offen sc1" ::"v"(v), "v"(voff), "s"(rs), "s"(soff) : "memory"); }
template <int N> DI void vmwait() { asm volatile("s_waitcnt vmcnt(%0)" ::"i"(N) : "memory"); }
template <int N> DI void vmwait(u32x4& a) { asm volatile("s_waitcnt vmcnt(%1)" : "+v"(a) : "i"(N) : "memory"); }
DI void bar() { asm volatile("s_waitcnt lgkmcnt(0)\n\ts_barrier" ::: "memory"); }
template <int N> DI void vmbar() { asm volatile("s_waitcnt vmcnt(%0) lgkmcnt(0)\n\ts_barrier" ::"i"(N) : "memory"); }

DI void poll(u32 voff, u32x4 ws, u32 tag) {
  for (int it = 0; it < SPIN_MAX; ++it) {
    u32 f;
    asm volatile("buffer_load_dword %0, %1, %2, 0 offen sc1\n\ts_waitcnt vmcnt(0)" : "=&v"(f) : "v"(voff), "s"(ws) : "memory");
    if (__builtin_amdgcn_ballot_w64(f != tag) == 0) break;
    __builtin_amdgcn_s_sleep(4);
  }
}
template <int C> DI float dppadd(float v) { return v + __int_as_float(__builtin_amdgcn_update_dpp(0, __float_as_int(v), C, 0xf, 0xf, true)); }
DI float wsum(float v) {
  v = dppadd<0xB1>(v); v = dppadd<0x4E>(v); v = dppadd<0x141>(v); v = dppadd<0x140>(v);
  const int i = __float_as_int(v);
  return (__int_as_float(__builtin_amdgcn_readlane(i, 0)) + __int_as_float(__builtin_amdgcn_readlane(i, 16))) +
         (__int_as_float(__builtin_amdgcn_readlane(i, 32)) + __int_as_float(__builtin_amdgcn_readlane(i, 48)));
}
DI float phi(float t) { return fmaxf(t, 0.f) + __builtin_amdgcn_exp2f(fminf(t, 0.f) * 1.4426950408889634f) + 1e-6f; }
DI float gelu(float v) {
  const float t = v * (-2.3022081981f - 0.1029432397f * v * v);
  return v * __builtin_amdgcn_rcpf(1.f + __builtin_amdgcn_exp2f(t));
}

struct WSrc { u32x4 rs; u32 voff, sbase, stride, lds; };
DI void w_issue(const WSrc& s, int c) {
#pragma unroll
  for (int i = 0; i < 4; ++i) glds(s.lds + (c & 3) * 32768 + i * 1024, s.voff, s.rs, s.sbase + (64 * c + 8 * i) * s.stride);
}
struct WA4 { u32 a[2][2]; };
DI WA4 w_addr(u32 a0) { WA4 r; r.a[0][0] = a0; r.a[0][1] = a0 ^ 64; r.a[1][0] = a0 + 65536; r.a[1][1] = (a0 ^ 64) + 65536;
  asm volatile("" : "+v"(r.a[0][0]), "+v"(r.a[0][1]), "+v"(r.a[1][0]), "+v"(r.a[1][1])); return r; }
DI u32x4 w_frag(const WA4& r, int b, int slot, int ks) { const u32 a = r.a[slot >> 1][b] + (slot & 1) * 32768 + ks * 2048; return trd2(a, a + 1024); }
template <int C, int N> DI void w_step(const WSrc& s, const WA4& a0, u32x4 (&wr)[16][2]) {
  vmbar<N>();
  if (C > 0) w_issue(s, C + 3);
#pragma unroll
  for (int k = 0; k < 4; ++k) { wr[4 * C + k][0] = w_frag(a0, 0, C, k); wr[4 * C + k][1] = w_frag(a0, 1, C, k); }
}
template <int I> DI void d_issue(u32x4 (&d)[32], u32 voff, u32x4 rs, u32 sb) { ld128c<(I & 3) * 1024>(d[I], voff, rs, sb + (I >> 2) * 4096); }
template <bool WA, int K> DI void wr_steps(u32x4 (&d)[32], u32x4 (&wr)[16][2], const WA4& a0, u32 voff, u32x4 rs, u32 sb, f32x16& c0, f32x16& c1) {
  if constexpr (K < 32) {
    constexpr int issued = K < 8 ? 16 + 2 * K : 32;
    vmwait<issued - 1 - K>(d[K]);
    if constexpr (WA) { c0 = mfma(wr[K & 15][0], d[K], c0); c1 = mfma(wr[K & 15][1], d[K], c1); }
    else { c0 = mfma(d[K], wr[K & 15][0], c0); c1 = mfma(d[K], wr[K & 15][1], c1); }
    asm volatile("" : "+v"(c0), "+v"(c1));
    if constexpr (K < 8) { d_issue<16 + 2 * K>(d, voff, rs, sb); d_issue<17 + 2 * K>(d, voff, rs, sb); }
    else if constexpr (K < 24) { constexpr int J = K - 8; wr[J][0] = w_frag(a0, 0, J >> 2, J & 3); wr[J][1] = w_frag(a0, 1, J >> 2, J & 3); }
    wr_steps<WA, K + 1>(d, wr, a0, voff, rs, sb, c0, c1);
  }
}
template <int I> DI void d_first(u32x4 (&d)[32], u32 voff, u32x4 rs, u32 sb) { if constexpr (I < 16) { d_issue<I>(d, voff, rs, sb); d_first<I + 1>(d, voff, rs, sb); } }
template <bool WA> DI void wr_gemm(u32x4 (&wr)[16][2], const WA4& a0, u32 voff, u32x4 rs, u32 sb, f32x16& c0, f32x16& c1) {
  u32x4 d[32];
  d_first<0>(d, voff, rs, sb);
#pragma unroll
  for (int i = 0; i < 16; ++i) { c0[i] = 0.f; c1[i] = 0.f; }
  wr_steps<WA, 0>(d, wr, a0, voff, rs, sb, c0, c1);
}
constexpr u32 RSL = 1088;
DI void red_write(u32 base, int w, int lane, const f32x16& c0, const f32x16& c1) {
  const u32 a = base + w * 8 * RSL + lane * 16;
#pragma unroll
  for (int q = 0; q < 4; ++q) {
    *(LAS f32x4*)(a + q * RSL) = f32x4{c0[4 * q], c0[4 * q + 1], c0[4 * q + 2], c0[4 * q + 3]};
    *(LAS f32x4*)(a + (4 + q) * RSL) = f32x4{c1[4 * q], c1[4 * q + 1], c1[4 * q + 2], c1[4 * q + 3]};
  }
}

constexpr u32 AST = 53248;
struct ASrc { u32x4 rx, rw; u32 vx, vw, vq, sx, s0, s1, sq; };
template <bool KV> DI void a_issue(const ASrc& s, u32 st, int w, int c) {
  if (KV) { if (w < 4) glds(st + 49152 + w * 1024, s.vq, s.rw, s.sq + c * 64 * 3072); }
#pragma unroll
  for (int i = 0; i < 4; ++i) glds(st + w * 4096 + i * 1024, s.vx ^ ((i & 1) * 64), s.rx, s.sx + i * 8192 + c * 128);
  glds(st + 32768 + w * 1024, s.vw, s.rw, s.s0 + c * 64 * 3072);
  glds(st + 40960 + w * 1024, s.vw, s.rw, s.s1 + c * 64 * 3072);
}

__global__ __launch_bounds__(512, 2) void fused_kernel(const char* __restrict__ x, const char* __restrict__ wqkv, const char* __restrict__ wo,
                                                        const char* __restrict__ w1, const char* __restrict__ b1, const char* __restrict__ w2,
                                                        const char* __restrict__ b2, const char* __restrict__ gw, char* __restrict__ out, char* ws
) {
  __shared__ __attribute__((aligned(1024))) char sm[163840];
  const u32 L = (u32)(size_t)(LAS char*)sm;
  const int lane = threadIdx.x & 63, w = __builtin_amdgcn_readfirstlane(threadIdx.x >> 6);
  const int xcd = blockIdx.x & 7, slot = blockIdx.x >> 3;
  const int hh = lane >> 5, col = lane & 31;
  const u32x4 rws = srd(ws, WS_END), rx = srd(x, 2048 * 1024);
  TS(0);

  const int p = (xcd & 1) * 128 + (xcd >> 1) * 32 + slot;
  const int row0 = 16 * (p >> 1) + 4 * (p & 1) + (w & 3) + 8 * (w >> 2);
  u32x4 xr, gr; u32 ev;
  asm volatile("s_nop 4" ::: "memory");
  ld128(xr, lane * 16, rx, row0 * 1024);
  ld128(gr, lane * 16, srd(gw, 1024), 0);
  ld32c(ev, 0, rws, WS_E);

  const int g = w & 3, hf = w >> 2;
  const int q4 = (lane & 15) >> 2;
  const WA4 a0 = w_addr(L + g * 8192 + (4 * hh + q4) * 128 + 64 * (q4 >> 1) + 32 * ((lane >> 4) & 1) + 8 * (lane & 3));
  const u32 wch = (lane & 7) ^ ((lane >> 2) & 4);
  const bool isG1 = slot < 22, isKV = slot < 30;

  const int gi = (xcd >> 1) * 22 + slot, ft = gi >> 2, dt1 = (xcd & 1) * 4 + (gi & 3);
  WSrc s1;
  s1.rs = srd(w1, 2048 * 2752); s1.stride = 2752; s1.lds = L + g * 8192 + hf * 4096;
  s1.voff = (lane >> 3) * 2752 + (ft == 21 ? (wch & 3) : wch) * 16;
  s1.sbase = (g * 512 + hf * 32) * 2752 + ft * 128;

  const int ai = slot - 22;
  const int rc = isKV ? (xcd & 1) * 4 + (ai & 3) : (xcd & 1) * 4 + (xcd >> 1);
  const int hg = (xcd >> 1) * 2 + (ai >> 2), qj = slot - 30;
  ASrc sa;
  sa.rx = rx; sa.rw = srd(wqkv, 512 * 3072);
  sa.vx = (lane >> 3) * 1024 + ((lane & 7) ^ (lane >> 4)) * 16;
  sa.vw = (lane >> 3) * 3072 + wch * 16;
  sa.vq = (lane >> 2) * 3072 + (lane & 3) * 16;
  sa.sx = (rc * 256 + w * 32) * 1024;
  sa.s0 = w * 8 * 3072 + (isKV ? 1024 + hg * 128 : 512 + qj * 256);
  sa.s1 = w * 8 * 3072 + (isKV ? 2048 + hg * 128 : 640 + qj * 256);
  sa.sq = w * 16 * 3072 + hg * 64;

  if (isG1) { w_issue(s1, 0); w_issue(s1, 1); vmwait<8>(); }
  else if (isKV) { a_issue<true>(sa, L, w, 0); vmwait<6>(); }
  else { a_issue<false>(sa, L, w, 0); vmwait<6>(); }
  asm volatile("" : "+v"(xr), "+v"(gr), "+v"(ev)::"memory");
  TS(1);
  const u32 tag = __builtin_amdgcn_readfirstlane(ev) * 1664525u + 1013904223u;

  {
    float v[8], s = 0.f, gd = 0.f;
#pragma unroll
    for (int i = 0; i < 4; ++i) {
      v[2 * i] = blo(xr[i]); v[2 * i + 1] = bhi(xr[i]);
      s += v[2 * i] + v[2 * i + 1]; gd += v[2 * i] * blo(gr[i]) + v[2 * i + 1] * bhi(gr[i]);
    }
    const float m = wsum(s) * (1.f / 512.f);
    gd = wsum(gd);
    float q = 0.f;
#pragma unroll
    for (int i = 0; i < 8; ++i) { v[i] -= m; q += v[i] * v[i]; }
    const float r = __builtin_amdgcn_rsqf(wsum(q) * (1.f / 512.f) + 1e-5f);
    const u32 st = L + 131072;
    *(LAS u32x4*)(st + w * 1088 + lane * 16) = u32x4{pk(v[0] * r, v[1] * r), pk(v[2] * r, v[3] * r), pk(v[4] * r, v[5] * r), pk(v[6] * r, v[7] * r)};
    bar();
    const u32 ta = st + q4 * 1088 + (w * 64 + (lane >> 4) * 16 + (lane & 3) * 4) * 2;
    const u32x4 hv = trd2(ta, ta + 4 * 1088);
    st128c(hv, hh * 131072 + col * 16, rws, WS_H + w * 262144 + (p >> 1) * 1024 + (p & 1) * 512);
    const float gt = __builtin_amdgcn_rcpf(1.f + __builtin_amdgcn_exp2f(gd * -1.4426950408889634f));
    if (lane == 0) st32c(__float_as_uint(gt), 0, rws, WS_G + row0 * 4);
  }
  if (isG1) { w_issue(s1, 2); w_issue(s1, 3); vmbar<8>(); }
  else if (isKV) { a_issue<true>(sa, L + AST, w, 1); vmbar<6>(); }
  else { a_issue<false>(sa, L + AST, w, 1); vmbar<6>(); }
  if (threadIdx.x == 0) st32c(tag, 0, rws, WS_HF + p * 4);
  TS(2);

  const int dt = (xcd & 1) * 4 + (slot & 3), stl = (xcd >> 1) * 8 + (slot >> 2);
  WSrc s2;
  s2.rs = g < 3 ? srd(w2, 1376 * 4096) : srd(wo, 512 * 1024); s2.stride = g < 3 ? 4096 : 1024; s2.lds = s1.lds;
  s2.voff = (lane >> 3) * s2.stride + wch * 16;
  s2.sbase = g < 3 ? (g * 512 + hf * 32) * 4096 + stl * 128 : hf * 32 * 1024 + dt * 128;

  if (isG1) {
    u32x4 wr[16][2];
    w_step<0, 9>(s1, a0, wr); w_step<1, 9>(s1, a0, wr); w_step<2, 8>(s1, a0, wr); w_step<3, 8>(s1, a0, wr);
    bar(); w_issue(s1, 7);
    const int fb = w >> 2, dh = (w >> 1) & 1, hs = w & 1;
    u32x2 bb0, bb1;
    {
      const u32x4 rb = srd(b1, 2752); const u32 o = ft * 128 + fb * 64 + hs * 32;
      asm volatile("buffer_load_dwordx2 %0, %2, %3, %4 offen\n\tbuffer_load_dwordx2 %1, %2, %3, %4 offen offset:16" : "=&v"(bb0), "=&v"(bb1) : "v"(hh * 8), "s"(rb), "s"(o) : "memory");
    }
    asm volatile("s_waitcnt vmcnt(0) lgkmcnt(0)\n\ts_barrier" : "+v"(bb0), "+v"(bb1)::"memory");
    TS(3);
    poll(WS_HF + (g * 64 + lane) * 4, rws, tag);
    TS(4);
    f32x16 c0, c1;
    wr_gemm<true>(wr, a0, lane * 16, rws, WS_H + (dt1 * 2 + hf) * 131072 + g * 32768, c0, c1);
    TS(5);
    bar();
    if (threadIdx.x == 0 && blockIdx.x == 0) st32c(tag, 0, rws, WS_E);
    const u32 R = L + 163840 - 64 * RSL;
    red_write(R, w, lane, c0, c1);
    w_issue(s2, 0); w_issue(s2, 1);
    bar();
    f32x4 lo = {blo(bb0[0]), bhi(bb0[0]), blo(bb0[1]), bhi(bb0[1])}, hi = {blo(bb1[0]), bhi(bb1[0]), blo(bb1[1]), bhi(bb1[1])};
#pragma unroll
    for (int k = 0; k < 4; ++k) {
      const u32 a = R + ((dh * 4 + k) * 8 + fb * 4 + hs * 2) * RSL + lane * 16;
      lo += ldsf4(a); hi += ldsf4(a + RSL);
    }
    const u32x4 uv = {pk(gelu(lo[0]), gelu(lo[1])), pk(gelu(lo[2]), gelu(lo[3])), pk(gelu(hi[0]), gelu(hi[1])), pk(gelu(hi[2]), gelu(hi[3]))};
    st128c(uv, lane * 16, rws, WS_U + (dt1 * 2 + dh) * UDB + (ft * 4 + fb * 2 + hs) * 1024);
    vmbar<0>();
    if (threadIdx.x == 0) st32c(tag, 0, rws, WS_UF + dt1 * 128 + ft * 4);
    TS(6);
    w_issue(s2, 2); w_issue(s2, 3);
  } else {
    const int mi = w >> 1, ni = w & 1;
    f32x16 c[5];
#pragma unroll
    for (int j = 0; j < 5; ++j)
#pragma unroll
      for (int i = 0; i < 16; ++i) c[j][i] = 0.f;
    const u32 xa0 = L + (mi * 64 + ni * 32 + col) * 128, xa1 = L + (mi * 64 + (ni ^ 1) * 32 + col) * 128;
    const u32 xs = (col >> 1) & 7;
    const u32 wa = L + 32768 + (8 * hh + q4) * 128 + 64 * ((q4 >> 1) ^ ni) + 32 * ((lane >> 4) & 1) + 8 * (lane & 3);
    const u32 qa = L + 49152 + (8 * hh + q4) * 64 + 32 * ((lane >> 4) & 1) + 8 * (lane & 3);
    u32 xo[4];
#pragma unroll
    for (int k = 0; k < 4; ++k) xo[k] = ((2 * k + hh) ^ xs) * 16;
    if (isKV) {
      u32 so = 0;
#pragma unroll 1
      for (int cc = 0; cc < 8; ++cc, so = so == 2 * AST ? 0 : so + AST) {
        if (cc < 7) vmbar<6>(); else vmbar<0>();
        if (cc < 6) a_issue<true>(sa, L + (so == 0 ? 2 * AST : so - AST), w, cc + 2);
#pragma unroll
        for (int k = 0; k < 4; ++k) {
          const u32x4 x0 = lds128(xa0 + xo[k] + so), x1 = lds128(xa1 + xo[k] + so);
          const u32x4 wk = trd2(wa + so + k * 2048, wa + so + k * 2048 + 512), wv = trd2(wa + so + 8192 + k * 2048, wa + so + 8192 + k * 2048 + 512);
          const u32x4 wq = trd2(qa + so + k * 1024, qa + so + k * 1024 + 256);
          c[0] = mfma(x0, wk, c[0]); c[1] = mfma(x1, wk, c[1]); c[2] = mfma(x0, wv, c[2]); c[3] = mfma(x1, wv, c[3]); c[4] = mfma(wq, x0, c[4]);
        }
      }
    } else {
      u32 so = 0;
#pragma unroll 1
      for (int cc = 0; cc < 8; ++cc, so = so == 2 * AST ? 0 : so + AST) {
        if (cc < 7) vmbar<6>(); else vmbar<0>();
        if (cc < 6) a_issue<false>(sa, L + (so == 0 ? 2 * AST : so - AST), w, cc + 2);
#pragma unroll
        for (int k = 0; k < 4; ++k) {
          const u32x4 x0 = lds128(xa0 + xo[k] + so), x1 = lds128(xa1 + xo[k] + so);
          const u32x4 wk = trd2(wa + so + k * 2048, wa + so + k * 2048 + 512), wv = trd2(wa + so + 8192 + k * 2048, wa + so + 8192 + k * 2048 + 512);
          c[0] = mfma(wk, x0, c[0]); c[1] = mfma(wk, x1, c[1]); c[2] = mfma(wv, x0, c[2]); c[3] = mfma(wv, x1, c[3]);
        }
      }
    }
    TS(3);
    bar();
    w_issue(s2, 0); w_issue(s2, 1);
    const u32 E = L + 131072;
    const u32 KT = E + 20480, ZT = E + 20480 + 4096;
    if (isKV) {
      f32x16 d1, d2;
#pragma unroll
      for (int i = 0; i < 16; ++i) { d1[i] = 0.f; d2[i] = 0.f; }
      const u32x4 ones = {0x3f803f80u, 0x3f803f80u, 0x3f803f80u, 0x3f803f80u};
#pragma unroll
      for (int b = 0; b < 2; ++b) {
#pragma unroll
        for (int i = 0; i < 16; ++i) c[b][i] = phi(c[b][i]);
#pragma unroll
        for (int s = 0; s < 2; ++s) { const u32x4 pa = pack8(c[b], 8 * s); d1 = mfma(pa, pack8(c[2 + b], 8 * s), d1); d2 = mfma(pa, ones, d2); }
      }
      const bool lo = col < 16;
      const u32 ka = E + (ni * 4 + mi) * 2048 + lane * 16;
      *(LAS f32x4*)ka = f32x4{lo ? d1[0] : d1[8], lo ? d1[1] : d1[9], lo ? d1[2] : d1[10], lo ? d1[3] : d1[11]};
      *(LAS f32x4*)(ka + 1024) = f32x4{lo ? d1[4] : d1[12], lo ? d1[5] : d1[13], lo ? d1[6] : d1[14], lo ? d1[7] : d1[15]};
      if (col == 0) {
#pragma unroll
        for (int q = 0; q < 4; ++q) *(LAS f32x4*)(E + 16384 + (ni * 4 + mi) * 128 + q * 32 + hh * 16) = f32x4{d2[4 * q], d2[4 * q + 1], d2[4 * q + 2], d2[4 * q + 3]};
      }
      bar();
      if (w < 4) {
        const u32 a = E + (w >> 1) * 8192 + (w & 1) * 1024 + lane * 16;
        const f32x4 t = (ldsf4(a) + ldsf4(a + 2048)) + (ldsf4(a + 4096) + ldsf4(a + 6144));
        st128c(__builtin_bit_cast(u32x4, t), lane * 16, rws, WS_KV + (rc * 16 + hg * 2) * 2048 + w * 1024);
      } else if (w == 4) {
        const u32 a = E + 16384 + hh * 512 + col * 4;
        const float t = (*(LAS float*)a + *(LAS float*)(a + 128)) + (*(LAS float*)(a + 256) + *(LAS float*)(a + 384));
        st32c(__float_as_uint(t), lane * 4, rws, WS_Z + rc * 2048 + hg * 256);
      }
      vmbar<0>();
      if (threadIdx.x == 0) st32c(tag, 0, rws, WS_KF + hg * 128 + rc * 4);
#pragma unroll
      for (int i = 0; i < 16; ++i) c[0][i] = c[4][i];
    }
    TS(4);
    u32x4 pq[4][2];
#pragma unroll
    for (int j = 0; j < 4; ++j) {
      if (j == 0 || !isKV) {
#pragma unroll
        for (int i = 0; i < 16; ++i) c[j][i] = phi(c[j][i]);
      }
      pq[j][0] = pack8(c[j], 0); pq[j][1] = pack8(c[j], 8);
    }
    const int pair0 = isKV ? hg : 8 + 4 * qj;
    if (w < 2 || !isKV) {
      const int pr = pair0 + (w >> 1);
      poll(WS_KF + (pr >> 1) * 128 + (lane & 7) * 4, rws, tag);
      TS(5);
      u32x4 t[8]; u32 z[8];
#pragma unroll
      for (int r = 0; r < 8; ++r) { ld128c<0>(t[r], lane * 16, rws, WS_KV + (r * 16 + pr) * 2048 + (w & 1) * 1024); ld32c(z[r], (lane & 15) * 4, rws, WS_Z + r * 2048 + pr * 128 + (w & 1) * 64); }
      asm volatile("s_waitcnt vmcnt(0)" : "+v"(t[0]), "+v"(t[1]), "+v"(t[2]), "+v"(t[3]), "+v"(t[4]), "+v"(t[5]), "+v"(t[6]), "+v"(t[7]),
                   "+v"(z[0]), "+v"(z[1]), "+v"(z[2]), "+v"(z[3]), "+v"(z[4]), "+v"(z[5]), "+v"(z[6]), "+v"(z[7])::"memory");
      f32x4 a = __builtin_bit_cast(f32x4, t[0]); float zs = __uint_as_float(z[0]);
#pragma unroll
      for (int r = 1; r < 8; ++r) { a += __builtin_bit_cast(f32x4, t[r]); zs += __uint_as_float(z[r]); }
      *(LAS u32x2*)(KT + (w >> 1) * 1024 + lane * 16 + (w & 1) * 8) = u32x2{pk(a[0], a[1]), pk(a[2], a[3])};
      if (lane < 16) *(LAS unsigned short*)(ZT + w * 32 + ((lane >> 2) & 1) * 16 + ((lane & 3) + 4 * (lane >> 3)) * 2) = (unsigned short)pk(zs, zs);
    }
    bar();
    f32x16 zero;
#pragma unroll
    for (int i = 0; i < 16; ++i) zero[i] = 0.f;
#pragma unroll
    for (int j = 0; j < 4; ++j) {
      if (j == 0 || !isKV) {
        const int pl = isKV ? 0 : (j >> 1) * 2 + ni;
        const int sb = rc * 8 + mi * 2 + (ni ^ (j & 1));
#pragma unroll
        for (int h = 0; h < 2; ++h) {
          const u32x4 af = lds128(((col < 16) == (h == 0)) ? KT + pl * 1024 + lane * 16 : ZT + pl * 64 + h * 32 + hh * 16);
          const f32x16 dd = mfma(af, pq[j][h], zero);
          const float rd = __builtin_amdgcn_rcpf(dd[8 - 8 * h] + 1e-6f);
          const int o = 8 * h;
          const u32x4 ov = {pk(dd[o] * rd, dd[o + 1] * rd), pk(dd[o + 2] * rd, dd[o + 3] * rd), pk(dd[o + 4] * rd, dd[o + 5] * rd), pk(dd[o + 6] * rd, dd[o + 7] * rd)};
          st128c(ov, lane * 16, rws, WS_O + sb * 32768 + ((pair0 + pl) * 2 + h) * 1024);
        }
      }
    }
    vmbar<0>();
    if (threadIdx.x == 0) st32c(tag, 0, rws, WS_OF + rc * 128 + (isKV ? hg : 8 + qj) * 4);
    TS(6);
    w_issue(s2, 2); w_issue(s2, 3);
  }

  {
    const int sr = w * 8 + (lane >> 3), cq = lane & 7;
    u32 b2v;
    asm volatile("buffer_load_ushort %0, %1, %2, %3 offen" : "=v"(b2v) : "v"(sr * 2), "s"(srd(b2, 4096)), "s"(stl * 128) : "memory");
    const u32 GT = L + 131072;
    if (g < 3) {
      u32x4 wr[16][2]; f32x16 c0, c1;
      w_step<0, 9>(s2, a0, wr); w_step<1, 9>(s2, a0, wr); w_step<2, 9>(s2, a0, wr); w_step<3, 8>(s2, a0, wr);
      bar(); w_issue(s2, 7); vmbar<0>();
      TS(7);
      const int nf = g < 2 ? 8 : 6;
      poll(WS_UF + dt * 128 + (g * 8 + (lane < nf ? lane : 0)) * 4, rws, tag);
      TS(8);
      u32 gv;
      ld32c(gv, lane * 4, rws, WS_G + stl * 256);
      const u32 ub = WS_U + (dt * 2 + hf) * UDB;
      wr_gemm<false>(wr, a0, lane * 16, srd(ws, ub + 86 * 1024), ub + g * 32768, c0, c1);
      asm volatile("" : "+v"(gv));
      *(LAS u32*)(GT + lane * 4) = gv;
      TS(9);
      bar();
      red_write(L, w, lane, c0, c1);
    } else {
      u32x4 wr[16][2]; f32x16 c0, c1;
      w_step<0, 9>(s2, a0, wr); w_step<1, 9>(s2, a0, wr); w_step<2, 9>(s2, a0, wr); w_step<3, 8>(s2, a0, wr);
      bar(); w_issue(s2, 7); vmbar<0>();
      TS(7);
      poll(WS_OF + (stl >> 2) * 128 + (lane < 10 ? lane : 0) * 4, rws, tag);
      TS(8);
      wr_gemm<true>(wr, a0, lane * 16, rws, WS_O + (stl * 2 + hf) * 32768, c0, c1);
      TS(9);
      bar();
      red_write(L, w, lane, c0, c1);
    }
    asm volatile("s_waitcnt lgkmcnt(0)\n\ts_barrier" : "+v"(b2v)::"memory");
    const int db = cq >> 2, sb = w >> 2;
    const u32 lo = (cq & 3) * RSL + (sr & 31) * 16;
    f32x4 y0 = {0.f, 0.f, 0.f, 0.f}, y1 = y0;
#pragma unroll
    for (int k = 0; k < 3; ++k) { const u32 a = L + ((db * 4 + k) * 8 + sb * 4) * RSL + lo; y0 += ldsf4(a); y1 += ldsf4(a + 512); }
    const u32 aa = L + ((sb * 4 + 3) * 8 + db * 4) * RSL + lo;
    const f32x4 a0v = ldsf4(aa), a1v = ldsf4(aa + 512);
    const float gt = *(LAS float*)(GT + sr * 4), bb = blo(b2v), og = 1.f - gt;
    u32x4 ov;
    ov[0] = pk(gt * a0v[0] + og * (y0[0] + bb), gt * a0v[1] + og * (y0[1] + bb));
    ov[1] = pk(gt * a0v[2] + og * (y0[2] + bb), gt * a0v[3] + og * (y0[3] + bb));
    ov[2] = pk(gt * a1v[0] + og * (y1[0] + bb), gt * a1v[1] + og * (y1[1] + bb));
    ov[3] = pk(gt * a1v[2] + og * (y1[2] + bb), gt * a1v[3] + og * (y1[3] + bb));
    *(u32x4*)(out + (size_t)(stl * 64 + sr) * 1024 + dt * 128 + cq * 16) = ov;
    TS(10);
  }
}

extern "C" void kernel_launch(void* const* d_in, const int* in_sizes, int n_in, void* d_out, int out_size, void* d_ws, size_t ws_size, hipStream_t stream) {
  fused_kernel<<<256, 512, 0, stream>>>((const char*)d_in[0], (const char*)d_in[1], (const char*)d_in[2], (const char*)d_in[4], (const char*)d_in[5],
                                        (const char*)d_in[6], (const char*)d_in[7], (const char*)d_in[8], (char*)d_out, (char*)d_ws);
}
```

```cpp
#define PROBE 1
#define PROLE (true)
#include <hip/hip_runtime.h>
#include <hip/hip_bf16.h>

#pragma clang diagnostic ignored "-Wint-to-pointer-cast"
#define DI __device__ __forceinline__
#define LAS __attribute__((address_space(3)))
typedef unsigned u32;
typedef u32 u32x4 __attribute__((ext_vector_type(4)));
typedef u32 u32x2 __attribute__((ext_vector_type(2)));
typedef float f32x2 __attribute__((ext_vector_type(2)));
typedef float f32x4 __attribute__((ext_vector_type(4)));
typedef float f32x16 __attribute__((ext_vector_type(16)));
typedef __bf16 bf16x8 __attribute__((ext_vector_type(8)));
typedef __bf16 bf16x2 __attribute__((ext_vector_type(2)));
typedef short s16x4 __attribute__((ext_vector_type(4)));

constexpr u32 WS_E = 0, WS_HF = 4096, WS_UF = 8192, WS_KF = 12288, WS_OF = 16384, WS_G = 32768;
constexpr u32 WS_KV = 65536, WS_Z = WS_KV + 262144, WS_H = 1u << 20, WS_U = 3u << 20, WS_O = 5u << 20, WS_END = 7u << 20;
constexpr u32 UDB = 88 * 1024;
constexpr int SPIN_MAX = 20000;

#define TS(i) do { if ((i) == 0) pt0 = __builtin_amdgcn_s_memrealtime(); if ((i) == PROBE) pt1 = __builtin_amdgcn_s_memrealtime(); \
  if ((i) == 10 && PROLE) { const unsigned long long e = __builtin_amdgcn_s_memrealtime() + 20 * (pt1 - pt0); for (int z = 0; z < 200000 && __builtin_amdgcn_s_memrealtime() < e; ++z) __builtin_amdgcn_s_sleep(8); } } while (0)

DI u32x4 srd(const void* p, u32 bytes) { const unsigned long long a = (unsigned long long)p; return u32x4{(u32)a, (u32)(a >> 32), bytes, 0x00020000u}; }
DI f32x16 mfma(u32x4 a, u32x4 b, f32x16 c) { return __builtin_amdgcn_mfma_f32_32x32x16_bf16(__builtin_bit_cast(bf16x8, a), __builtin_bit_cast(bf16x8, b), c, 0, 0, 0); }
DI u32x2 trd(u32 a) { return __builtin_bit_cast(u32x2, __builtin_amdgcn_ds_read_tr16_b64_v4i16((LAS s16x4*)a)); }
DI u32x4 trd2(u32 a, u32 b) { const u32x2 x = trd(a), y = trd(b); return u32x4{x[0], x[1], y[0], y[1]}; }
DI u32x4 lds128(u32 a) { return *(LAS u32x4*)a; }
DI f32x4 ldsf4(u32 a) { return *(LAS f32x4*)a; }
DI u32 pk(float a, float b) { return __builtin_bit_cast(u32, __builtin_convertvector(f32x2{a, b}, bf16x2)); }
DI float blo(u32 v) { return __uint_as_float(v << 16); }
DI float bhi(u32 v) { return __uint_as_float(v & 0xffff0000u); }
DI u32x4 pack8(const f32x16& x, int o) { return u32x4{pk(x[o], x[o + 1]), pk(x[o + 2], x[o + 3]), pk(x[o + 4], x[o + 5]), pk(x[o + 6], x[o + 7])}; }

DI void glds(u32 lds, u32 voff, u32x4 rs, u32 soff) {
  asm volatile("s_mov_b32 m0, %0\n\ts_nop 0\n\tbuffer_load_dwordx4 %1, %2, %3 offen lds" ::"s"(lds), "v"(voff), "s"(rs), "s"(soff) : "memory");
}
template <int OFF> DI void ld128c(u32x4& d, u32 voff, u32x4 rs, u32 soff) {
  asm volatile("buffer_load_dwordx4 %0, %1, %2, %3 offen offset:%4 sc1" : "=v"(d) : "v"(voff), "s"(rs), "s"(soff), "i"(OFF) : "memory");
}
DI void ld128(u32x4& d, u32 voff, u32x4 rs, u32 soff) { asm volatile("buffer_load_dwordx4 %0, %1, %2, %3 offen" : "=v"(d) : "v"(voff), "s"(rs), "s"(soff) : "memory"); }
DI void ld32c(u32& d, u32 voff, u32x4 rs, u32 soff) { asm volatile("buffer_load_dword %0, %1, %2, %3 offen sc1" : "=v"(d) : "v"(voff), "s"(rs), "s"(soff) : "memory"); }
DI void st128c(u32x4 v, u32 voff, u32x4 rs, u32 soff) { asm volatile("buffer_store_dwordx4 %0, %1, %2, %3 offen sc1\n\ts_nop 1" ::"v"(v), "v"(voff), "s"(rs), "s"(soff) : "memory"); }
DI void st32c(u32 v, u32 voff, u32x4 rs, u32 soff) { asm volatile("buffer_store_dword %0, %1, %2, %3 offen sc1" ::"v"(v), "v"(voff), "s"(rs), "s"(soff) : "memory"); }
template <int N> DI void vmwait() { asm volatile("s_waitcnt vmcnt(%0)" ::"i"(N) : "memory"); }
template <int N> DI void vmwait(u32x4& a) { asm volatile("s_waitcnt vmcnt(%1)" : "+v"(a) : "i"(N) : "memory"); }
DI void bar() { asm volatile("s_waitcnt lgkmcnt(0)\n\ts_barrier" ::: "memory"); }
template <int N> DI void vmbar() { asm volatile("s_waitcnt vmcnt(%0) lgkmcnt(0)\n\ts_barrier" ::"i"(N) : "memory"); }

DI void poll(u32 voff, u32x4 ws, u32 tag) {
  for (int it = 0; it < SPIN_MAX; ++it) {
    u32 f;
    asm volatile("buffer_load_dword %0, %1, %2, 0 offen sc1\n\ts_waitcnt vmcnt(0)" : "=&v"(f) : "v"(voff), "s"(ws) : "memory");
    if (__builtin_amdgcn_ballot_w64(f != tag) == 0) break;
    __builtin_amdgcn_s_sleep(4);
  }
}
template <int C> DI float dppadd(float v) { return v + __int_as_float(__builtin_amdgcn_update_dpp(0, __float_as_int(v), C, 0xf, 0xf, true)); }
DI float wsum(float v) {
  v = dppadd<0xB1>(v); v = dppadd<0x4E>(v); v = dppadd<0x141>(v); v = dppadd<0x140>(v);
  const int i = __float_as_int(v);
  return (__int_as_float(__builtin_amdgcn_readlane(i, 0)) + __int_as_float(__builtin_amdgcn_readlane(i, 16))) +
         (__int_as_float(__builtin_amdgcn_readlane(i, 32)) + __int_as_float(__builtin_amdgcn_readlane(i, 48)));
}
DI float phi(float t) { return fmaxf(t, 0.f) + __builtin_amdgcn_exp2f(fminf(t, 0.f) * 1.4426950408889634f) + 1e-6f; }
DI float gelu(float v) {
  const float t = v * (-2.3022081981f - 0.1029432397f * v * v);
  return v * __builtin_amdgcn_rcpf(1.f + __builtin_amdgcn_exp2f(t));
}

struct WSrc { u32x4 rs; u32 voff, sbase, stride, lds; };
DI void w_issue(const WSrc& s, int c) {
#pragma unroll
  for (int i = 0; i < 4; ++i) glds(s.lds + (c & 3) * 32768 + i * 1024, s.voff, s.rs, s.sbase + (64 * c + 8 * i) * s.stride);
}
struct WA4 { u32 a[2][2]; };
DI WA4 w_addr(u32 a0) { WA4 r; r.a[0][0] = a0; r.a[0][1] = a0 ^ 64; r.a[1][0] = a0 + 65536; r.a[1][1] = (a0 ^ 64) + 65536;
  asm volatile("" : "+v"(r.a[0][0]), "+v"(r.a[0][1]), "+v"(r.a[1][0]), "+v"(r.a[1][1])); return r; }
DI u32x4 w_frag(const WA4& r, int b, int slot, int ks) { const u32 a = r.a[slot >> 1][b] + (slot & 1) * 32768 + ks * 2048; return trd2(a, a + 1024); }
template <int C, int N> DI void w_step(const WSrc& s, const WA4& a0, u32x4 (&wr)[16][2]) {
  vmbar<N>();
  if (C > 0) w_issue(s, C + 3);
#pragma unroll
  for (int k = 0; k < 4; ++k) { wr[4 * C + k][0] = w_frag(a0, 0, C, k); wr[4 * C + k][1] = w_frag(a0, 1, C, k); }
}
template <int I> DI void d_issue(u32x4 (&d)[32], u32 voff, u32x4 rs, u32 sb) { ld128c<(I & 3) * 1024>(d[I], voff, rs, sb + (I >> 2) * 4096); }
template <bool WA, int K> DI void wr_steps(u32x4 (&d)[32], u32x4 (&wr)[16][2], const WA4& a0, u32 voff, u32x4 rs, u32 sb, f32x16& c0, f32x16& c1) {
  if constexpr (K < 32) {
    constexpr int issued = K < 8 ? 16 + 2 * K : 32;
    vmwait<issued - 1 - K>(d[K]);
    if constexpr (WA) { c0 = mfma(wr[K & 15][0], d[K], c0); c1 = mfma(wr[K & 15][1], d[K], c1); }
    else { c0 = mfma(d[K], wr[K & 15][0], c0); c1 = mfma(d[K], wr[K & 15][1], c1); }
    asm volatile("" : "+v"(c0), "+v"(c1));
    if constexpr (K < 8) { d_issue<16 + 2 * K>(d, voff, rs, sb); d_issue<17 + 2 * K>(d, voff, rs, sb); }
    else if constexpr (K < 24) { constexpr int J = K - 8; wr[J][0] = w_frag(a0, 0, J >> 2, J & 3); wr[J][1] = w_frag(a0, 1, J >> 2, J & 3); }
    wr_steps<WA, K + 1>(d, wr, a0, voff, rs, sb, c0, c1);
  }
}
template <int I> DI void d_first(u32x4 (&d)[32], u32 voff, u32x4 rs, u32 sb) { if constexpr (I < 16) { d_issue<I>(d, voff, rs, sb); d_first<I + 1>(d, voff, rs, sb); } }
template <bool WA> DI void wr_gemm(u32x4 (&wr)[16][2], const WA4& a0, u32 voff, u32x4 rs, u32 sb, f32x16& c0, f32x16& c1) {
  u32x4 d[32];
  d_first<0>(d, voff, rs, sb);
#pragma unroll
  for (int i = 0; i < 16; ++i) { c0[i] = 0.f; c1[i] = 0.f; }
  wr_steps<WA, 0>(d, wr, a0, voff, rs, sb, c0, c1);
}
constexpr u32 RSL = 1088;
DI void red_write(u32 base, int w, int lane, const f32x16& c0, const f32x16& c1) {
  const u32 a = base + w * 8 * RSL + lane * 16;
#pragma unroll
  for (int q = 0; q < 4; ++q) {
    *(LAS f32x4*)(a + q * RSL) = f32x4{c0[4 * q], c0[4 * q + 1], c0[4 * q + 2], c0[4 * q + 3]};
    *(LAS f32x4*)(a + (4 + q) * RSL) = f32x4{c1[4 * q], c1[4 * q + 1], c1[4 * q + 2], c1[4 * q + 3]};
  }
}

constexpr u32 AST = 53248;
struct ASrc { u32x4 rx, rw; u32 vx, vw, vq, sx, s0, s1, sq; };
template <bool KV> DI void a_issue(const ASrc& s, u32 st, int w, int c) {
  if (KV) { if (w < 4) glds(st + 49152 + w * 1024, s.vq, s.rw, s.sq + c * 64 * 3072); }
#pragma unroll
  for (int i = 0; i < 4; ++i) glds(st + w * 4096 + i * 1024, s.vx ^ ((i & 1) * 64), s.rx, s.sx + i * 8192 + c * 128);
  glds(st + 32768 + w * 1024, s.vw, s.rw, s.s0 + c * 64 * 3072);
  glds(st + 40960 + w * 1024, s.vw, s.rw, s.s1 + c * 64 * 3072);
}

__global__ __launch_bounds__(512, 2) void fused_kernel(const char* __restrict__ x, const char* __restrict__ wqkv, const char* __restrict__ wo,
                                                        const char* __restrict__ w1, const char* __restrict__ b1, const char* __restrict__ w2,
                                                        const char* __restrict__ b2, const char* __restrict__ gw, char* __restrict__ out, char* ws
) {
  __shared__ __attribute__((aligned(1024))) char sm[163840];
  const u32 L = (u32)(size_t)(LAS char*)sm;
  const int lane = threadIdx.x & 63, w = __builtin_amdgcn_readfirstlane(threadIdx.x >> 6);
  const int xcd = blockIdx.x & 7, slot = blockIdx.x >> 3;
  const int hh = lane >> 5, col = lane & 31;
  const u32x4 rws = srd(ws, WS_END), rx = srd(x, 2048 * 1024);
  unsigned long long pt0 = 0, pt1 = 0;
  TS(0);

  const int p = (xcd & 1) * 128 + (xcd >> 1) * 32 + slot;
  const int row0 = 16 * (p >> 1) + 4 * (p & 1) + (w & 3) + 8 * (w >> 2);
  u32x4 xr, gr; u32 ev;
  asm volatile("s_nop 4" ::: "memory");
  ld128(xr, lane * 16, rx, row0 * 1024);
  ld128(gr, lane * 16, srd(gw, 1024), 0);
  ld32c(ev, 0, rws, WS_E);

  const int g = w & 3, hf = w >> 2;
  const int q4 = (lane & 15) >> 2;
  const WA4 a0 = w_addr(L + g * 8192 + (4 * hh + q4) * 128 + 64 * (q4 >> 1) + 32 * ((lane >> 4) & 1) + 8 * (lane & 3));
  const u32 wch = (lane & 7) ^ ((lane >> 2) & 4);
  const bool isG1 = slot < 22, isKV = slot < 30;

  const int gi = (xcd >> 1) * 22 + slot, ft = gi >> 2, dt1 = (xcd & 1) * 4 + (gi & 3);
  WSrc s1;
  s1.rs = srd(w1, 2048 * 2752); s1.stride = 2752; s1.lds = L + g * 8192 + hf * 4096;
  s1.voff = (lane >> 3) * 2752 + (ft == 21 ? (wch & 3) : wch) * 16;
  s1.sbase = (g * 512 + hf * 32) * 2752 + ft * 128;

  const int ai = slot - 22;
  const int rc = isKV ? (xcd & 1) * 4 + (ai & 3) : (xcd & 1) * 4 + (xcd >> 1);
  const int hg = (xcd >> 1) * 2 + (ai >> 2), qj = slot - 30;
  ASrc sa;
  sa.rx = rx; sa.rw = srd(wqkv, 512 * 3072);
  sa.vx = (lane >> 3) * 1024 + ((lane & 7) ^ (lane >> 4)) * 16;
  sa.vw = (lane >> 3) * 3072 + wch * 16;
  sa.vq = (lane >> 2) * 3072 + (lane & 3) * 16;
  sa.sx = (rc * 256 + w * 32) * 1024;
  sa.s0 = w * 8 * 3072 + (isKV ? 1024 + hg * 128 : 512 + qj * 256);
  sa.s1 = w * 8 * 3072 + (isKV ? 2048 + hg * 128 : 640 + qj * 256);
  sa.sq = w * 16 * 3072 + hg * 64;

  if (isG1) { w_issue(s1, 0); w_issue(s1, 1); vmwait<8>(); }
  else if (isKV) { a_issue<true>(sa, L, w, 0); vmwait<6>(); }
  else { a_issue<false>(sa, L, w, 0); vmwait<6>(); }
  asm volatile("" : "+v"(xr), "+v"(gr), "+v"(ev)::"memory");
  TS(1);
  const u32 tag = __builtin_amdgcn_readfirstlane(ev) * 1664525u + 1013904223u;

  {
    float v[8], s = 0.f, gd = 0.f;
#pragma unroll
    for (int i = 0; i < 4; ++i) {
      v[2 * i] = blo(xr[i]); v[2 * i + 1] = bhi(xr[i]);
      s += v[2 * i] + v[2 * i + 1]; gd += v[2 * i] * blo(gr[i]) + v[2 * i + 1] * bhi(gr[i]);
    }
    const float m = wsum(s) * (1.f / 512.f);
    gd = wsum(gd);
    float q = 0.f;
#pragma unroll
    for (int i = 0; i < 8; ++i) { v[i] -= m; q += v[i] * v[i]; }
    const float r = __builtin_amdgcn_rsqf(wsum(q) * (1.f / 512.f) + 1e-5f);
    const u32 st = L + 131072;
    *(LAS u32x4*)(st + w * 1088 + lane * 16) = u32x4{pk(v[0] * r, v[1] * r), pk(v[2] * r, v[3] * r), pk(v[4] * r, v[5] * r), pk(v[6] * r, v[7] * r)};
    bar();
    const u32 ta = st + q4 * 1088 + (w * 64 + (lane >> 4) * 16 + (lane & 3) * 4) * 2;
    const u32x4 hv = trd2(ta, ta + 4 * 1088);
    st128c(hv, hh * 131072 + col * 16, rws, WS_H + w * 262144 + (p >> 1) * 1024 + (p & 1) * 512);
    const float gt = __builtin_amdgcn_rcpf(1.f + __builtin_amdgcn_exp2f(gd * -1.4426950408889634f));
    if (lane == 0) st32c(__float_as_uint(gt), 0, rws, WS_G + row0 * 4);
  }
  if (isG1) { w_issue(s1, 2); w_issue(s1, 3); vmbar<8>(); }
  else if (isKV) { a_issue<true>(sa, L + AST, w, 1); vmbar<6>(); }
  else { a_issue<false>(sa, L + AST, w, 1); vmbar<6>(); }
  if (threadIdx.x == 0) st32c(tag, 0, rws, WS_HF + p * 4);
  TS(2);

  const int dt = (xcd & 1) * 4 + (slot & 3), stl = (xcd >> 1) * 8 + (slot >> 2);
  WSrc s2;
  s2.rs = g < 3 ? srd(w2, 1376 * 4096) : srd(wo, 512 * 1024); s2.stride = g < 3 ? 4096 : 1024; s2.lds = s1.lds;
  s2.voff = (lane >> 3) * s2.stride + wch * 16;
  s2.sbase = g < 3 ? (g * 512 + hf * 32) * 4096 + stl * 128 : hf * 32 * 1024 + dt * 128;

  if (isG1) {
    u32x4 wr[16][2];
    w_step<0, 9>(s1, a0, wr); w_step<1, 9>(s1, a0, wr); w_step<2, 8>(s1, a0, wr); w_step<3, 8>(s1, a0, wr);
    bar(); w_issue(s1, 7);
    const int fb = w >> 2, dh = (w >> 1) & 1, hs = w & 1;
    u32x2 bb0, bb1;
    {
      const u32x4 rb = srd(b1, 2752); const u32 o = ft * 128 + fb * 64 + hs * 32;
      asm volatile("buffer_load_dwordx2 %0, %2, %3, %4 offen\n\tbuffer_load_dwordx2 %1, %2, %3, %4 offen offset:16" : "=&v"(bb0), "=&v"(bb1) : "v"(hh * 8), "s"(rb), "s"(o) : "memory");
    }
    asm volatile("s_waitcnt vmcnt(0) lgkmcnt(0)\n\ts_barrier" : "+v"(bb0), "+v"(bb1)::"memory");
    TS(3);
    poll(WS_HF + (g * 64 + lane) * 4, rws, tag);
    TS(4);
    f32x16 c0, c1;
    wr_gemm<true>(wr, a0, lane * 16, rws, WS_H + (dt1 * 2 + hf) * 131072 + g * 32768, c0, c1);
    TS(5);
    bar();
    if (threadIdx.x == 0 && blockIdx.x == 0) st32c(tag, 0, rws, WS_E);
    const u32 R = L + 163840 - 64 * RSL;
    red_write(R, w, lane, c0, c1);
    w_issue(s2, 0); w_issue(s2, 1);
    bar();
    f32x4 lo = {blo(bb0[0]), bhi(bb0[0]), blo(bb0[1]), bhi(bb0[1])}, hi = {blo(bb1[0]), bhi(bb1[0]), blo(bb1[1]), bhi(bb1[1])};
#pragma unroll
    for (int k = 0; k < 4; ++k) {
      const u32 a = R + ((dh * 4 + k) * 8 + fb * 4 + hs * 2) * RSL + lane * 16;
      lo += ldsf4(a); hi += ldsf4(a + RSL);
    }
    const u32x4 uv = {pk(gelu(lo[0]), gelu(lo[1])), pk(gelu(lo[2]), gelu(lo[3])), pk(gelu(hi[0]), gelu(hi[1])), pk(gelu(hi[2]), gelu(hi[3]))};
    st128c(uv, lane * 16, rws, WS_U + (dt1 * 2 + dh) * UDB + (ft * 4 + fb * 2 + hs) * 1024);
    vmbar<0>();
    if (threadIdx.x == 0) st32c(tag, 0, rws, WS_UF + dt1 * 128 + ft * 4);
    TS(6);
    w_issue(s2, 2); w_issue(s2, 3);
  } else {
    const int mi = w >> 1, ni = w & 1;
    f32x16 c[5];
#pragma unroll
    for (int j = 0; j < 5; ++j)
#pragma unroll
      for (int i = 0; i < 16; ++i) c[j][i] = 0.f;
    const u32 xa0 = L + (mi * 64 + ni * 32 + col) * 128, xa1 = L + (mi * 64 + (ni ^ 1) * 32 + col) * 128;
    const u32 xs = (col >> 1) & 7;
    const u32 wa = L + 32768 + (8 * hh + q4) * 128 + 64 * ((q4 >> 1) ^ ni) + 32 * ((lane >> 4) & 1) + 8 * (lane & 3);
    const u32 qa = L + 49152 + (8 * hh + q4) * 64 + 32 * ((lane >> 4) & 1) + 8 * (lane & 3);
    u32 xo[4];
#pragma unroll
    for (int k = 0; k < 4; ++k) xo[k] = ((2 * k + hh) ^ xs) * 16;
    if (isKV) {
      u32 so = 0;
#pragma unroll 1
      for (int cc = 0; cc < 8; ++cc, so = so == 2 * AST ? 0 : so + AST) {
        if (cc < 7) vmbar<6>(); else vmbar<0>();
        if (cc < 6) a_issue<true>(sa, L + (so == 0 ? 2 * AST : so - AST), w, cc + 2);
#pragma unroll
        for (int k = 0; k < 4; ++k) {
          const u32x4 x0 = lds128(xa0 + xo[k] + so), x1 = lds128(xa1 + xo[k] + so);
          const u32x4 wk = trd2(wa + so + k * 2048, wa + so + k * 2048 + 512), wv = trd2(wa + so + 8192 + k * 2048, wa + so + 8192 + k * 2048 + 512);
          const u32x4 wq = trd2(qa + so + k * 1024, qa + so + k * 1024 + 256);
          c[0] = mfma(x0, wk, c[0]); c[1] = mfma(x1, wk, c[1]); c[2] = mfma(x0, wv, c[2]); c[3] = mfma(x1, wv, c[3]); c[4] = mfma(wq, x0, c[4]);
        }
      }
    } else {
      u32 so = 0;
#pragma unroll 1
      for (int cc = 0; cc < 8; ++cc, so = so == 2 * AST ? 0 : so + AST) {
        if (cc < 7) vmbar<6>(); else vmbar<0>();
        if (cc < 6) a_issue<false>(sa, L + (so == 0 ? 2 * AST : so - AST), w, cc + 2);
#pragma unroll
        for (int k = 0; k < 4; ++k) {
          const u32x4 x0 = lds128(xa0 + xo[k] + so), x1 = lds128(xa1 + xo[k] + so);
          const u32x4 wk = trd2(wa + so + k * 2048, wa + so + k * 2048 + 512), wv = trd2(wa + so + 8192 + k * 2048, wa + so + 8192 + k * 2048 + 512);
          c[0] = mfma(wk, x0, c[0]); c[1] = mfma(wk, x1, c[1]); c[2] = mfma(wv, x0, c[2]); c[3] = mfma(wv, x1, c[3]);
        }
      }
    }
    TS(3);
    bar();
    w_issue(s2, 0); w_issue(s2, 1);
    const u32 E = L + 131072;
    const u32 KT = E + 20480, ZT = E + 20480 + 4096;
    if (isKV) {
      f32x16 d1, d2;
#pragma unroll
      for (int i = 0; i < 16; ++i) { d1[i] = 0.f; d2[i] = 0.f; }
      const u32x4 ones = {0x3f803f80u, 0x3f803f80u, 0x3f803f80u, 0x3f803f80u};
#pragma unroll
      for (int b = 0; b < 2; ++b) {
#pragma unroll
        for (int i = 0; i < 16; ++i) c[b][i] = phi(c[b][i]);
#pragma unroll
        for (int s = 0; s < 2; ++s) { const u32x4 pa = pack8(c[b], 8 * s); d1 = mfma(pa, pack8(c[2 + b], 8 * s), d1); d2 = mfma(pa, ones, d2); }
      }
      const bool lo = col < 16;
      const u32 ka = E + (ni * 4 + mi) * 2048 + lane * 16;
      *(LAS f32x4*)ka = f32x4{lo ? d1[0] : d1[8], lo ? d1[1] : d1[9], lo ? d1[2] : d1[10], lo ? d1[3] : d1[11]};
      *(LAS f32x4*)(ka + 1024) = f32x4{lo ? d1[4] : d1[12], lo ? d1[5] : d1[13], lo ? d1[6] : d1[14], lo ? d1[7] : d1[15]};
      if (col == 0) {
#pragma unroll
        for (int q = 0; q < 4; ++q) *(LAS f32x4*)(E + 16384 + (ni * 4 + mi) * 128 + q * 32 + hh * 16) = f32x4{d2[4 * q], d2[4 * q + 1], d2[4 * q + 2], d2[4 * q + 3]};
      }
      bar();
      if (w < 4) {
        const u32 a = E + (w >> 1) * 8192 + (w & 1) * 1024 + lane * 16;
        const f32x4 t = (ldsf4(a) + ldsf4(a + 2048)) + (ldsf4(a + 4096) + ldsf4(a + 6144));
        st128c(__builtin_bit_cast(u32x4, t), lane * 16, rws, WS_KV + (rc * 16 + hg * 2) * 2048 + w * 1024);
      } else if (w == 4) {
        const u32 a = E + 16384 + hh * 512 + col * 4;
        const float t = (*(LAS float*)a + *(LAS float*)(a + 128)) + (*(LAS float*)(a + 256) + *(LAS float*)(a + 384));
        st32c(__float_as_uint(t), lane * 4, rws, WS_Z + rc * 2048 + hg * 256);
      }
      vmbar<0>();
      if (threadIdx.x == 0) st32c(tag, 0, rws, WS_KF + hg * 128 + rc * 4);
#pragma unroll
      for (int i = 0; i < 16; ++i) c[0][i] = c[4][i];
    }
    TS(4);
    u32x4 pq[4][2];
#pragma unroll
    for (int j = 0; j < 4; ++j) {
      if (j == 0 || !isKV) {
#pragma unroll
        for (int i = 0; i < 16; ++i) c[j][i] = phi(c[j][i]);
      }
      pq[j][0] = pack8(c[j], 0); pq[j][1] = pack8(c[j], 8);
    }
    const int pair0 = isKV ? hg : 8 + 4 * qj;
    if (w < 2 || !isKV) {
      const int pr = pair0 + (w >> 1);
      poll(WS_KF + (pr >> 1) * 128 + (lane & 7) * 4, rws, tag);
      TS(5);
      u32x4 t[8]; u32 z[8];
#pragma unroll
      for (int r = 0; r < 8; ++r) { ld128c<0>(t[r], lane * 16, rws, WS_KV + (r * 16 + pr) * 2048 + (w & 1) * 1024); ld32c(z[r], (lane & 15) * 4, rws, WS_Z + r * 2048 + pr * 128 + (w & 1) * 64); }
      asm volatile("s_waitcnt vmcnt(0)" : "+v"(t[0]), "+v"(t[1]), "+v"(t[2]), "+v"(t[3]), "+v"(t[4]), "+v"(t[5]), "+v"(t[6]), "+v"(t[7]),
                   "+v"(z[0]), "+v"(z[1]), "+v"(z[2]), "+v"(z[3]), "+v"(z[4]), "+v"(z[5]), "+v"(z[6]), "+v"(z[7])::"memory");
      f32x4 a = __builtin_bit_cast(f32x4, t[0]); float zs = __uint_as_float(z[0]);
#pragma unroll
      for (int r = 1; r < 8; ++r) { a += __builtin_bit_cast(f32x4, t[r]); zs += __uint_as_float(z[r]); }
      *(LAS u32x2*)(KT + (w >> 1) * 1024 + lane * 16 + (w & 1) * 8) = u32x2{pk(a[0], a[1]), pk(a[2], a[3])};
      if (lane < 16) *(LAS unsigned short*)(ZT + w * 32 + ((lane >> 2) & 1) * 16 + ((lane & 3) + 4 * (lane >> 3)) * 2) = (unsigned short)pk(zs, zs);
    }
    bar();
    f32x16 zero;
#pragma unroll
    for (int i = 0; i < 16; ++i) zero[i] = 0.f;
#pragma unroll
    for (int j = 0; j < 4; ++j) {
      if (j == 0 || !isKV) {
        const int pl = isKV ? 0 : (j >> 1) * 2 + ni;
        const int sb = rc * 8 + mi * 2 + (ni ^ (j & 1));
#pragma unroll
        for (int h = 0; h < 2; ++h) {
          const u32x4 af = lds128(((col < 16) == (h == 0)) ? KT + pl * 1024 + lane * 16 : ZT + pl * 64 + h * 32 + hh * 16);
          const f32x16 dd = mfma(af, pq[j][h], zero);
          const float rd = __builtin_amdgcn_rcpf(dd[8 - 8 * h] + 1e-6f);
          const int o = 8 * h;
          const u32x4 ov = {pk(dd[o] * rd, dd[o + 1] * rd), pk(dd[o + 2] * rd, dd[o + 3] * rd), pk(dd[o + 4] * rd, dd[o + 5] * rd), pk(dd[o + 6] * rd, dd[o + 7] * rd)};
          st128c(ov, lane * 16, rws, WS_O + sb * 32768 + ((pair0 + pl) * 2 + h) * 1024);
        }
      }
    }
    vmbar<0>();
    if (threadIdx.x == 0) st32c(tag, 0, rws, WS_OF + rc * 128 + (isKV ? hg : 8 + qj) * 4);
    TS(6);
    w_issue(s2, 2); w_issue(s2, 3);
  }

  {
    const int sr = w * 8 + (lane >> 3), cq = lane & 7;
    u32 b2v;
    asm volatile("buffer_load_ushort %0, %1, %2, %3 offen" : "=v"(b2v) : "v"(sr * 2), "s"(srd(b2, 4096)), "s"(stl * 128) : "memory");
    const u32 GT = L + 131072;
    if (g < 3) {
      u32x4 wr[16][2]; f32x16 c0, c1;
      w_step<0, 9>(s2, a0, wr); w_step<1, 9>(s2, a0, wr); w_step<2, 9>(s2, a0, wr); w_step<3, 8>(s2, a0, wr);
      bar(); w_issue(s2, 7); vmbar<0>();
      TS(7);
      const int nf = g < 2 ? 8 : 6;
      poll(WS_UF + dt * 128 + (g * 8 + (lane < nf ? lane : 0)) * 4, rws, tag);
      TS(8);
      u32 gv;
      ld32c(gv, lane * 4, rws, WS_G + stl * 256);
      const u32 ub = WS_U + (dt * 2 + hf) * UDB;
      wr_gemm<false>(wr, a0, lane * 16, srd(ws, ub + 86 * 1024), ub + g * 32768, c0, c1);
      asm volatile("" : "+v"(gv));
      *(LAS u32*)(GT + lane * 4) = gv;
      TS(9);
      bar();
      red_write(L, w, lane, c0, c1);
    } else {
      u32x4 wr[16][2]; f32x16 c0, c1;
      w_step<0, 9>(s2, a0, wr); w_step<1, 9>(s2, a0, wr); w_step<2, 9>(s2, a0, wr); w_step<3, 8>(s2, a0, wr);
      bar(); w_issue(s2, 7); vmbar<0>();
      TS(7);
      poll(WS_OF + (stl >> 2) * 128 + (lane < 10 ? lane : 0) * 4, rws, tag);
      TS(8);
      wr_gemm<true>(wr, a0, lane * 16, rws, WS_O + (stl * 2 + hf) * 32768, c0, c1);
      TS(9);
      bar();
      red_write(L, w, lane, c0, c1);
    }
    asm volatile("s_waitcnt lgkmcnt(0)\n\ts_barrier" : "+v"(b2v)::"memory");
    const int db = cq >> 2, sb = w >> 2;
    const u32 lo = (cq & 3) * RSL + (sr & 31) * 16;
    f32x4 y0 = {0.f, 0.f, 0.f, 0.f}, y1 = y0;
#pragma unroll
    for (int k = 0; k < 3; ++k) { const u32 a = L + ((db * 4 + k) * 8 + sb * 4) * RSL + lo; y0 += ldsf4(a); y1 += ldsf4(a + 512); }
    const u32 aa = L + ((sb * 4 + 3) * 8 + db * 4) * RSL + lo;
    const f32x4 a0v = ldsf4(aa), a1v = ldsf4(aa + 512);
    const float gt = *(LAS float*)(GT + sr * 4), bb = blo(b2v), og = 1.f - gt;
    u32x4 ov;
    ov[0] = pk(gt * a0v[0] + og * (y0[0] + bb), gt * a0v[1] + og * (y0[1] + bb));
    ov[1] = pk(gt * a0v[2] + og * (y0[2] + bb), gt * a0v[3] + og * (y0[3] + bb));
    ov[2] = pk(gt * a1v[0] + og * (y1[0] + bb), gt * a1v[1] + og * (y1[1] + bb));
    ov[3] = pk(gt * a1v[2] + og * (y1[2] + bb), gt * a1v[3] + og * (y1[3] + bb));
    *(u32x4*)(out + (size_t)(stl * 64 + sr) * 1024 + dt * 128 + cq * 16) = ov;
    TS(10);
  }
}

extern "C" void kernel_launch(void* const* d_in, const int* in_sizes, int n_in, void* d_out, int out_size, void* d_ws, size_t ws_size, hipStream_t stream) {
  fused_kernel<<<256, 512, 0, stream>>>((const char*)d_in[0], (const char*)d_in[1], (const char*)d_in[2], (const char*)d_in[4], (const char*)d_in[5],
                                        (const char*)d_in[6], (const char*)d_in[7], (const char*)d_in[8], (char*)d_out, (char*)d_ws);
}
```

```cpp
#define XP 'G'
#include <hip/hip_runtime.h>
#include <hip/hip_bf16.h>

#pragma clang diagnostic ignored "-Wint-to-pointer-cast"
#define DI __device__ __forceinline__
#define LAS __attribute__((address_space(3)))
typedef unsigned u32;
typedef u32 u32x4 __attribute__((ext_vector_type(4)));
typedef u32 u32x2 __attribute__((ext_vector_type(2)));
typedef float f32x2 __attribute__((ext_vector_type(2)));
typedef float f32x4 __attribute__((ext_vector_type(4)));
typedef float f32x16 __attribute__((ext_vector_type(16)));
typedef __bf16 bf16x8 __attribute__((ext_vector_type(8)));
typedef __bf16 bf16x2 __attribute__((ext_vector_type(2)));
typedef short s16x4 __attribute__((ext_vector_type(4)));

constexpr u32 WS_E = 0, WS_HF = 4096, WS_UF = 8192, WS_KF = 12288, WS_OF = 16384, WS_G = 32768;
constexpr u32 WS_KV = 65536, WS_Z = WS_KV + 262144, WS_H = 1u << 20, WS_U = 3u << 20, WS_O = 5u << 20, WS_END = 7u << 20;
constexpr u32 UDB = 88 * 1024;
constexpr int SPIN_MAX = 20000;

#define WS_T (8u << 20)
DI void spin_to(unsigned long long e) { for (int z = 0; z < 200000 && __builtin_amdgcn_s_memrealtime() < e; ++z) __builtin_amdgcn_s_sleep(2); }
__global__ void tiny_first(char* ws) { if (threadIdx.x == 0) *(unsigned long long*)(ws + WS_T) = __builtin_amdgcn_s_memrealtime(); }
__global__ void tiny_last(char* ws) { spin_to(*(volatile unsigned long long*)(ws + WS_T) + 6000); }
#define TS(i) do { if ((i) == 0) { pt0 = __builtin_amdgcn_s_memrealtime(); if (XP == 'G' && blockIdx.x == 0 && threadIdx.x == 0) *(unsigned long long*)(ws + WS_T) = pt0; } \
  if ((i) == 10) { if (XP == 'A' || XP == 'B' || XP == 'I') spin_to(pt0 + 6000); if (XP == 'C') spin_to(*(volatile unsigned long long*)(ws + WS_T) + 6000); } } while (0)

DI u32x4 srd(const void* p, u32 bytes) { const unsigned long long a = (unsigned long long)p; return u32x4{(u32)a, (u32)(a >> 32), bytes, 0x00020000u}; }
DI f32x16 mfma(u32x4 a, u32x4 b, f32x16 c) { return __builtin_amdgcn_mfma_f32_32x32x16_bf16(__builtin_bit_cast(bf16x8, a), __builtin_bit_cast(bf16x8, b), c, 0, 0, 0); }
DI u32x2 trd(u32 a) { return __builtin_bit_cast(u32x2, __builtin_amdgcn_ds_read_tr16_b64_v4i16((LAS s16x4*)a)); }
DI u32x4 trd2(u32 a, u32 b) { const u32x2 x = trd(a), y = trd(b); return u32x4{x[0], x[1], y[0], y[1]}; }
DI u32x4 lds128(u32 a) { return *(LAS u32x4*)a; }
DI f32x4 ldsf4(u32 a) { return *(LAS f32x4*)a; }
DI u32 pk(float a, float b) { return __builtin_bit_cast(u32, __builtin_convertvector(f32x2{a, b}, bf16x2)); }
DI float blo(u32 v) { return __uint_as_float(v << 16); }
DI float bhi(u32 v) { return __uint_as_float(v & 0xffff0000u); }
DI u32x4 pack8(const f32x16& x, int o) { return u32x4{pk(x[o], x[o + 1]), pk(x[o + 2], x[o + 3]), pk(x[o + 4], x[o + 5]), pk(x[o + 6], x[o + 7])}; }

DI void glds(u32 lds, u32 voff, u32x4 rs, u32 soff) {
  asm volatile("s_mov_b32 m0, %0\n\ts_nop 0\n\tbuffer_load_dwordx4 %1, %2, %3 offen lds" ::"s"(lds), "v"(voff), "s"(rs), "s"(soff) : "memory");
}
template <int OFF> DI void ld128c(u32x4& d, u32 voff, u32x4 rs, u32 soff) {
  asm volatile("buffer_load_dwordx4 %0, %1, %2, %3 offen offset:%4 sc1" : "=v"(d) : "v"(voff), "s"(rs), "s"(soff), "i"(OFF) : "memory");
}
DI void ld128(u32x4& d, u32 voff, u32x4 rs, u32 soff) { asm volatile("buffer_load_dwordx4 %0, %1, %2, %3 offen" : "=v"(d) : "v"(voff), "s"(rs), "s"(soff) : "memory"); }
DI void ld32c(u32& d, u32 voff, u32x4 rs, u32 soff) { asm volatile("buffer_load_dword %0, %1, %2, %3 offen sc1" : "=v"(d) : "v"(voff), "s"(rs), "s"(soff) : "memory"); }
DI void st128c(u32x4 v, u32 voff, u32x4 rs, u32 soff) { asm volatile("buffer_store_dwordx4 %0, %1, %2, %3 offen sc1\n\ts_nop 1" ::"v"(v), "v"(voff), "s"(rs), "s"(soff) : "memory"); }
DI void st32c(u32 v, u32 voff, u32x4 rs, u32 soff) { asm volatile("buffer_store_dword %0, %1, %2, %3 offen sc1" ::"v"(v), "v"(voff), "s"(rs), "s"(soff) : "memory"); }
template <int N> DI void vmwait() { asm volatile("s_waitcnt vmcnt(%0)" ::"i"(N) : "memory"); }
template <int N> DI void vmwait(u32x4& a) { asm volatile("s_waitcnt vmcnt(%1)" : "+v"(a) : "i"(N) : "memory"); }
DI void bar() { asm volatile("s_waitcnt lgkmcnt(0)\n\ts_barrier" ::: "memory"); }
template <int N> DI void vmbar() { asm volatile("s_waitcnt vmcnt(%0) lgkmcnt(0)\n\ts_barrier" ::"i"(N) : "memory"); }

DI void poll(u32 voff, u32x4 ws, u32 tag) {
  for (int it = 0; it < SPIN_MAX; ++it) {
    u32 f;
    asm volatile("buffer_load_dword %0, %1, %2, 0 offen sc1\n\ts_waitcnt vmcnt(0)" : "=&v"(f) : "v"(voff), "s"(ws) : "memory");
    if (__builtin_amdgcn_ballot_w64(f != tag) == 0) break;
    __builtin_amdgcn_s_sleep(4);
  }
}
template <int C> DI float dppadd(float v) { return v + __int_as_float(__builtin_amdgcn_update_dpp(0, __float_as_int(v), C, 0xf, 0xf, true)); }
DI float wsum(float v) {
  v = dppadd<0xB1>(v); v = dppadd<0x4E>(v); v = dppadd<0x141>(v); v = dppadd<0x140>(v);
  const int i = __float_as_int(v);
  return (__int_as_float(__builtin_amdgcn_readlane(i, 0)) + __int_as_float(__builtin_amdgcn_readlane(i, 16))) +
         (__int_as_float(__builtin_amdgcn_readlane(i, 32)) + __int_as_float(__builtin_amdgcn_readlane(i, 48)));
}
DI float phi(float t) { return fmaxf(t, 0.f) + __builtin_amdgcn_exp2f(fminf(t, 0.f) * 1.4426950408889634f) + 1e-6f; }
DI float gelu(float v) {
  const float t = v * (-2.3022081981f - 0.1029432397f * v * v);
  return v * __builtin_amdgcn_rcpf(1.f + __builtin_amdgcn_exp2f(t));
}

struct WSrc { u32x4 rs; u32 voff, sbase, stride, lds; };
DI void w_issue(const WSrc& s, int c) {
#pragma unroll
  for (int i = 0; i < 4; ++i) glds(s.lds + (c & 3) * 32768 + i * 1024, s.voff, s.rs, s.sbase + (64 * c + 8 * i) * s.stride);
}
struct WA4 { u32 a[2][2]; };
DI WA4 w_addr(u32 a0) { WA4 r; r.a[0][0] = a0; r.a[0][1] = a0 ^ 64; r.a[1][0] = a0 + 65536; r.a[1][1] = (a0 ^ 64) + 65536;
  asm volatile("" : "+v"(r.a[0][0]), "+v"(r.a[0][1]), "+v"(r.a[1][0]), "+v"(r.a[1][1])); return r; }
DI u32x4 w_frag(const WA4& r, int b, int slot, int ks) { const u32 a = r.a[slot >> 1][b] + (slot & 1) * 32768 + ks * 2048; return trd2(a, a + 1024); }
template <int C, int N> DI void w_step(const WSrc& s, const WA4& a0, u32x4 (&wr)[16][2]) {
  vmbar<N>();
  if (C > 0) w_issue(s, C + 3);
#pragma unroll
  for (int k = 0; k < 4; ++k) { wr[4 * C + k][0] = w_frag(a0, 0, C, k); wr[4 * C + k][1] = w_frag(a0, 1, C, k); }
}
template <int I> DI void d_issue(u32x4 (&d)[32], u32 voff, u32x4 rs, u32 sb) { ld128c<(I & 3) * 1024>(d[I], voff, rs, sb + (I >> 2) * 4096); }
template <bool WA, int K> DI void wr_steps(u32x4 (&d)[32], u32x4 (&wr)[16][2], const WA4& a0, u32 voff, u32x4 rs, u32 sb, f32x16& c0, f32x16& c1) {
  if constexpr (K < 32) {
    constexpr int issued = K < 8 ? 16 + 2 * K : 32;
    vmwait<issued - 1 - K>(d[K]);
    if constexpr (WA) { c0 = mfma(wr[K & 15][0], d[K], c0); c1 = mfma(wr[K & 15][1], d[K], c1); }
    else { c0 = mfma(d[K], wr[K & 15][0], c0); c1 = mfma(d[K], wr[K & 15][1], c1); }
    asm volatile("" : "+v"(c0), "+v"(c1));
    if constexpr (K < 8) { d_issue<16 + 2 * K>(d, voff, rs, sb); d_issue<17 + 2 * K>(d, voff, rs, sb); }
    else if constexpr (K < 24) { constexpr int J = K - 8; wr[J][0] = w_frag(a0, 0, J >> 2, J & 3); wr[J][1] = w_frag(a0, 1, J >> 2, J & 3); }
    wr_steps<WA, K + 1>(d, wr, a0, voff, rs, sb, c0, c1);
  }
}
template <int I> DI void d_first(u32x4 (&d)[32], u32 voff, u32x4 rs, u32 sb) { if constexpr (I < 16) { d_issue<I>(d, voff, rs, sb); d_first<I + 1>(d, voff, rs, sb); } }
template <bool WA> DI void wr_gemm(u32x4 (&wr)[16][2], const WA4& a0, u32 voff, u32x4 rs, u32 sb, f32x16& c0, f32x16& c1) {
  u32x4 d[32];
  d_first<0>(d, voff, rs, sb);
#pragma unroll
  for (int i = 0; i < 16; ++i) { c0[i] = 0.f; c1[i] = 0.f; }
  wr_steps<WA, 0>(d, wr, a0, voff, rs, sb, c0, c1);
}
constexpr u32 RSL = 1088;
DI void red_write(u32 base, int w, int lane, const f32x16& c0, const f32x16& c1) {
  const u32 a = base + w * 8 * RSL + lane * 16;
#pragma unroll
  for (int q = 0; q < 4; ++q) {
    *(LAS f32x4*)(a + q * RSL) = f32x4{c0[4 * q], c0[4 * q + 1], c0[4 * q + 2], c0[4 * q + 3]};
    *(LAS f32x4*)(a + (4 + q) * RSL) = f32x4{c1[4 * q], c1[4 * q + 1], c1[4 * q + 2], c1[4 * q + 3]};
  }
}

constexpr u32 AST = 53248;
struct ASrc { u32x4 rx, rw; u32 vx, vw, vq, sx, s0, s1, sq; };
template <bool KV> DI void a_issue(const ASrc& s, u32 st, int w, int c) {
  if (KV) { if (w < 4) glds(st + 49152 + w * 1024, s.vq, s.rw, s.sq + c * 64 * 3072); }
#pragma unroll
  for (int i = 0; i < 4; ++i) glds(st + w * 4096 + i * 1024, s.vx ^ ((i & 1) * 64), s.rx, s.sx + i * 8192 + c * 128);
  glds(st + 32768 + w * 1024, s.vw, s.rw, s.s0 + c * 64 * 3072);
  glds(st + 40960 + w * 1024, s.vw, s.rw, s.s1 + c * 64 * 3072);
}

__global__ __launch_bounds__(512, 2) void fused_kernel(const char* __restrict__ x, const char* __restrict__ wqkv, const char* __restrict__ wo,
                                                        const char* __restrict__ w1, const char* __restrict__ b1, const char* __restrict__ w2,
                                                        const char* __restrict__ b2, const char* __restrict__ gw, char* __restrict__ out, char* ws
) {
  __shared__ __attribute__((aligned(1024))) char sm[163840];
  const u32 L = (u32)(size_t)(LAS char*)sm;
  const int lane = threadIdx.x & 63, w = __builtin_amdgcn_readfirstlane(threadIdx.x >> 6);
  const int xcd = blockIdx.x & 7, slot = blockIdx.x >> 3;
  const int hh = lane >> 5, col = lane & 31;
  const u32x4 rws = srd(ws, WS_END), rx = srd(x, 2048 * 1024);
  unsigned long long pt0 = 0, pt1 = 0;
  TS(0);

  const int p = (xcd & 1) * 128 + (xcd >> 1) * 32 + slot;
  const int row0 = 16 * (p >> 1) + 4 * (p & 1) + (w & 3) + 8 * (w >> 2);
  u32x4 xr, gr; u32 ev;
  asm volatile("s_nop 4" ::: "memory");
  ld128(xr, lane * 16, rx, row0 * 1024);
  ld128(gr, lane * 16, srd(gw, 1024), 0);
  ld32c(ev, 0, rws, WS_E);

  const int g = w & 3, hf = w >> 2;
  const int q4 = (lane & 15) >> 2;
  const WA4 a0 = w_addr(L + g * 8192 + (4 * hh + q4) * 128 + 64 * (q4 >> 1) + 32 * ((lane >> 4) & 1) + 8 * (lane & 3));
  const u32 wch = (lane & 7) ^ ((lane >> 2) & 4);
  const bool isG1 = slot < 22, isKV = slot < 30;

  const int gi = (xcd >> 1) * 22 + slot, ft = gi >> 2, dt1 = (xcd & 1) * 4 + (gi & 3);
  WSrc s1;
  s1.rs = srd(w1, 2048 * 2752); s1.stride = 2752; s1.lds = L + g * 8192 + hf * 4096;
  s1.voff = (lane >> 3) * 2752 + (ft == 21 ? (wch & 3) : wch) * 16;
  s1.sbase = (g * 512 + hf * 32) * 2752 + ft * 128;

  const int ai = slot - 22;
  const int rc = isKV ? (xcd & 1) * 4 + (ai & 3) : (xcd & 1) * 4 + (xcd >> 1);
  const int hg = (xcd >> 1) * 2 + (ai >> 2), qj = slot - 30;
  ASrc sa;
  sa.rx = rx; sa.rw = srd(wqkv, 512 * 3072);
  sa.vx = (lane >> 3) * 1024 + ((lane & 7) ^ (lane >> 4)) * 16;
  sa.vw = (lane >> 3) * 3072 + wch * 16;
  sa.vq = (lane >> 2) * 3072 + (lane & 3) * 16;
  sa.sx = (rc * 256 + w * 32) * 1024;
  sa.s0 = w * 8 * 3072 + (isKV ? 1024 + hg * 128 : 512 + qj * 256);
  sa.s1 = w * 8 * 3072 + (isKV ? 2048 + hg * 128 : 640 + qj * 256);
  sa.sq = w * 16 * 3072 + hg * 64;

  if (isG1) { w_issue(s1, 0); w_issue(s1, 1); vmwait<8>(); }
  else if (isKV) { a_issue<true>(sa, L, w, 0); vmwait<6>(); }
  else { a_issue<false>(sa, L, w, 0); vmwait<6>(); }
  asm volatile("" : "+v"(xr), "+v"(gr), "+v"(ev)::"memory");
  TS(1);
  const u32 tag = __builtin_amdgcn_readfirstlane(ev) * 1664525u + 1013904223u;

  {
    float v[8], s = 0.f, gd = 0.f;
#pragma unroll
    for (int i = 0; i < 4; ++i) {
      v[2 * i] = blo(xr[i]); v[2 * i + 1] = bhi(xr[i]);
      s += v[2 * i] + v[2 * i + 1]; gd += v[2 * i] * blo(gr[i]) + v[2 * i + 1] * bhi(gr[i]);
    }
    const float m = wsum(s) * (1.f / 512.f);
    gd = wsum(gd);
    float q = 0.f;
#pragma unroll
    for (int i = 0; i < 8; ++i) { v[i] -= m; q += v[i] * v[i]; }
    const float r = __builtin_amdgcn_rsqf(wsum(q) * (1.f / 512.f) + 1e-5f);
    const u32 st = L + 131072;
    *(LAS u32x4*)(st + w * 1088 + lane * 16) = u32x4{pk(v[0] * r, v[1] * r), pk(v[2] * r, v[3] * r), pk(v[4] * r, v[5] * r), pk(v[6] * r, v[7] * r)};
    bar();
    const u32 ta = st + q4 * 1088 + (w * 64 + (lane >> 4) * 16 + (lane & 3) * 4) * 2;
    const u32x4 hv = trd2(ta, ta + 4 * 1088);
    st128c(hv, hh * 131072 + col * 16, rws, WS_H + w * 262144 + (p >> 1) * 1024 + (p & 1) * 512);
    const float gt = __builtin_amdgcn_rcpf(1.f + __builtin_amdgcn_exp2f(gd * -1.4426950408889634f));
    if (lane == 0) st32c(__float_as_uint(gt), 0, rws, WS_G + row0 * 4);
  }
  if (isG1) { w_issue(s1, 2); w_issue(s1, 3); vmbar<8>(); }
  else if (isKV) { a_issue<true>(sa, L + AST, w, 1); vmbar<6>(); }
  else { a_issue<false>(sa, L + AST, w, 1); vmbar<6>(); }
  if (threadIdx.x == 0) st32c(tag, 0, rws, WS_HF + p * 4);
  TS(2);

  const int dt = (xcd & 1) * 4 + (slot & 3), stl = (xcd >> 1) * 8 + (slot >> 2);
  WSrc s2;
  s2.rs = g < 3 ? srd(w2, 1376 * 4096) : srd(wo, 512 * 1024); s2.stride = g < 3 ? 4096 : 1024; s2.lds = s1.lds;
  s2.voff = (lane >> 3) * s2.stride + wch * 16;
  s2.sbase = g < 3 ? (g * 512 + hf * 32) * 4096 + stl * 128 : hf * 32 * 1024 + dt * 128;

  if (isG1) {
    u32x4 wr[16][2];
    w_step<0, 9>(s1, a0, wr); w_step<1, 9>(s1, a0, wr); w_step<2, 8>(s1, a0, wr); w_step<3, 8>(s1, a0, wr);
    bar(); w_issue(s1, 7);
    const int fb = w >> 2, dh = (w >> 1) & 1, hs = w & 1;
    u32x2 bb0, bb1;
    {
      const u32x4 rb = srd(b1, 2752); const u32 o = ft * 128 + fb * 64 + hs * 32;
      asm volatile("buffer_load_dwordx2 %0, %2, %3, %4 offen\n\tbuffer_load_dwordx2 %1, %2, %3, %4 offen offset:16" : "=&v"(bb0), "=&v"(bb1) : "v"(hh * 8), "s"(rb), "s"(o) : "memory");
    }
    asm volatile("s_waitcnt vmcnt(0) lgkmcnt(0)\n\ts_barrier" : "+v"(bb0), "+v"(bb1)::"memory");
    TS(3);
    poll(WS_HF + (g * 64 + lane) * 4, rws, tag);
    TS(4);
    f32x16 c0, c1;
    wr_gemm<true>(wr, a0, lane * 16, rws, WS_H + (dt1 * 2 + hf) * 131072 + g * 32768, c0, c1);
    TS(5);
    bar();
    if (threadIdx.x == 0 && blockIdx.x == 0) st32c(tag, 0, rws, WS_E);
    const u32 R = L + 163840 - 64 * RSL;
    red_write(R, w, lane, c0, c1);
    w_issue(s2, 0); w_issue(s2, 1);
    bar();
    f32x4 lo = {blo(bb0[0]), bhi(bb0[0]), blo(bb0[1]), bhi(bb0[1])}, hi = {blo(bb1[0]), bhi(bb1[0]), blo(bb1[1]), bhi(bb1[1])};
#pragma unroll
    for (int k = 0; k < 4; ++k) {
      const u32 a = R + ((dh * 4 + k) * 8 + fb * 4 + hs * 2) * RSL + lane * 16;
      lo += ldsf4(a); hi += ldsf4(a + RSL);
    }
    const u32x4 uv = {pk(gelu(lo[0]), gelu(lo[1])), pk(gelu(lo[2]), gelu(lo[3])), pk(gelu(hi[0]), gelu(hi[1])), pk(gelu(hi[2]), gelu(hi[3]))};
    st128c(uv, lane * 16, rws, WS_U + (dt1 * 2 + dh) * UDB + (ft * 4 + fb * 2 + hs) * 1024);
    vmbar<0>();
    if (threadIdx.x == 0) st32c(tag, 0, rws, WS_UF + dt1 * 128 + ft * 4);
    TS(6);
    w_issue(s2, 2); w_issue(s2, 3);
  } else {
    const int mi = w >> 1, ni = w & 1;
    f32x16 c[5];
#pragma unroll
    for (int j = 0; j < 5; ++j)
#pragma unroll
      for (int i = 0; i < 16; ++i) c[j][i] = 0.f;
    const u32 xa0 = L + (mi * 64 + ni * 32 + col) * 128, xa1 = L + (mi * 64 + (ni ^ 1) * 32 + col) * 128;
    const u32 xs = (col >> 1) & 7;
    const u32 wa = L + 32768 + (8 * hh + q4) * 128 + 64 * ((q4 >> 1) ^ ni) + 32 * ((lane >> 4) & 1) + 8 * (lane & 3);
    const u32 qa = L + 49152 + (8 * hh + q4) * 64 + 32 * ((lane >> 4) & 1) + 8 * (lane & 3);
    u32 xo[4];
#pragma unroll
    for (int k = 0; k < 4; ++k) xo[k] = ((2 * k + hh) ^ xs) * 16;
    if (isKV) {
      u32 so = 0;
#pragma unroll 1
      for (int cc = 0; cc < 8; ++cc, so = so == 2 * AST ? 0 : so + AST) {
        if (cc < 7) vmbar<6>(); else vmbar<0>();
        if (cc < 6) a_issue<true>(sa, L + (so == 0 ? 2 * AST : so - AST), w, cc + 2);
#pragma unroll
        for (int k = 0; k < 4; ++k) {
          const u32x4 x0 = lds128(xa0 + xo[k] + so), x1 = lds128(xa1 + xo[k] + so);
          const u32x4 wk = trd2(wa + so + k * 2048, wa + so + k * 2048 + 512), wv = trd2(wa + so + 8192 + k * 2048, wa + so + 8192 + k * 2048 + 512);
          const u32x4 wq = trd2(qa + so + k * 1024, qa + so + k * 1024 + 256);
          c[0] = mfma(x0, wk, c[0]); c[1] = mfma(x1, wk, c[1]); c[2] = mfma(x0, wv, c[2]); c[3] = mfma(x1, wv, c[3]); c[4] = mfma(wq, x0, c[4]);
        }
      }
    } else {
      u32 so = 0;
#pragma unroll 1
      for (int cc = 0; cc < 8; ++cc, so = so == 2 * AST ? 0 : so + AST) {
        if (cc < 7) vmbar<6>(); else vmbar<0>();
        if (cc < 6) a_issue<false>(sa, L + (so == 0 ? 2 * AST : so - AST), w, cc + 2);
#pragma unroll
        for (int k = 0; k < 4; ++k) {
          const u32x4 x0 = lds128(xa0 + xo[k] + so), x1 = lds128(xa1 + xo[k] + so);
          const u32x4 wk = trd2(wa + so + k * 2048, wa + so + k * 2048 + 512), wv = trd2(wa + so + 8192 + k * 2048, wa + so + 8192 + k * 2048 + 512);
          c[0] = mfma(wk, x0, c[0]); c[1] = mfma(wk, x1, c[1]); c[2] = mfma(wv, x0, c[2]); c[3] = mfma(wv, x1, c[3]);
        }
      }
    }
    TS(3);
    bar();
    w_issue(s2, 0); w_issue(s2, 1);
    const u32 E = L + 131072;
    const u32 KT = E + 20480, ZT = E + 20480 + 4096;
    if (isKV) {
      f32x16 d1, d2;
#pragma unroll
      for (int i = 0; i < 16; ++i) { d1[i] = 0.f; d2[i] = 0.f; }
      const u32x4 ones = {0x3f803f80u, 0x3f803f80u, 0x3f803f80u, 0x3f803f80u};
#pragma unroll
      for (int b = 0; b < 2; ++b) {
#pragma unroll
        for (int i = 0; i < 16; ++i) c[b][i] = phi(c[b][i]);
#pragma unroll
        for (int s = 0; s < 2; ++s) { const u32x4 pa = pack8(c[b], 8 * s); d1 = mfma(pa, pack8(c[2 + b], 8 * s), d1); d2 = mfma(pa, ones, d2); }
      }
      const bool lo = col < 16;
      const u32 ka = E + (ni * 4 + mi) * 2048 + lane * 16;
      *(LAS f32x4*)ka = f32x4{lo ? d1[0] : d1[8], lo ? d1[1] : d1[9], lo ? d1[2] : d1[10], lo ? d1[3] : d1[11]};
      *(LAS f32x4*)(ka + 1024) = f32x4{lo ? d1[4] : d1[12], lo ? d1[5] : d1[13], lo ? d1[6] : d1[14], lo ? d1[7] : d1[15]};
      if (col == 0) {
#pragma unroll
        for (int q = 0; q < 4; ++q) *(LAS f32x4*)(E + 16384 + (ni * 4 + mi) * 128 + q * 32 + hh * 16) = f32x4{d2[4 * q], d2[4 * q + 1], d2[4 * q + 2], d2[4 * q + 3]};
      }
      bar();
      if (w < 4) {
        const u32 a = E + (w >> 1) * 8192 + (w & 1) * 1024 + lane * 16;
        const f32x4 t = (ldsf4(a) + ldsf4(a + 2048)) + (ldsf4(a + 4096) + ldsf4(a + 6144));
        st128c(__builtin_bit_cast(u32x4, t), lane * 16, rws, WS_KV + (rc * 16 + hg * 2) * 2048 + w * 1024);
      } else if (w == 4) {
        const u32 a = E + 16384 + hh * 512 + col * 4;
        const float t = (*(LAS float*)a + *(LAS float*)(a + 128)) + (*(LAS float*)(a + 256) + *(LAS float*)(a + 384));
        st32c(__float_as_uint(t), lane * 4, rws, WS_Z + rc * 2048 + hg * 256);
      }
      vmbar<0>();
      if (threadIdx.x == 0) st32c(tag, 0, rws, WS_KF + hg * 128 + rc * 4);
#pragma unroll
      for (int i = 0; i < 16; ++i) c[0][i] = c[4][i];
    }
    TS(4);
    u32x4 pq[4][2];
#pragma unroll
    for (int j = 0; j < 4; ++j) {
      if (j == 0 || !isKV) {
#pragma unroll
        for (int i = 0; i < 16; ++i) c[j][i] = phi(c[j][i]);
      }
      pq[j][0] = pack8(c[j], 0); pq[j][1] = pack8(c[j], 8);
    }
    const int pair0 = isKV ? hg : 8 + 4 * qj;
    if (w < 2 || !isKV) {
      const int pr = pair0 + (w >> 1);
      poll(WS_KF + (pr >> 1) * 128 + (lane & 7) * 4, rws, tag);
      TS(5);
      u32x4 t[8]; u32 z[8];
#pragma unroll
      for (int r = 0; r < 8; ++r) { ld128c<0>(t[r], lane * 16, rws, WS_KV + (r * 16 + pr) * 2048 + (w & 1) * 1024); ld32c(z[r], (lane & 15) * 4, rws, WS_Z + r * 2048 + pr * 128 + (w & 1) * 64); }
      asm volatile("s_waitcnt vmcnt(0)" : "+v"(t[0]), "+v"(t[1]), "+v"(t[2]), "+v"(t[3]), "+v"(t[4]), "+v"(t[5]), "+v"(t[6]), "+v"(t[7]),
                   "+v"(z[0]), "+v"(z[1]), "+v"(z[2]), "+v"(z[3]), "+v"(z[4]), "+v"(z[5]), "+v"(z[6]), "+v"(z[7])::"memory");
      f32x4 a = __builtin_bit_cast(f32x4, t[0]); float zs = __uint_as_float(z[0]);
#pragma unroll
      for (int r = 1; r < 8; ++r) { a += __builtin_bit_cast(f32x4, t[r]); zs += __uint_as_float(z[r]); }
      *(LAS u32x2*)(KT + (w >> 1) * 1024 + lane * 16 + (w & 1) * 8) = u32x2{pk(a[0], a[1]), pk(a[2], a[3])};
      if (lane < 16) *(LAS unsigned short*)(ZT + w * 32 + ((lane >> 2) & 1) * 16 + ((lane & 3) + 4 * (lane >> 3)) * 2) = (unsigned short)pk(zs, zs);
    }
    bar();
    f32x16 zero;
#pragma unroll
    for (int i = 0; i < 16; ++i) zero[i] = 0.f;
#pragma unroll
    for (int j = 0; j < 4; ++j) {
      if (j == 0 || !isKV) {
        const int pl = isKV ? 0 : (j >> 1) * 2 + ni;
        const int sb = rc * 8 + mi * 2 + (ni ^ (j & 1));
#pragma unroll
        for (int h = 0; h < 2; ++h) {
          const u32x4 af = lds128(((col < 16) == (h == 0)) ? KT + pl * 1024 + lane * 16 : ZT + pl * 64 + h * 32 + hh * 16);
          const f32x16 dd = mfma(af, pq[j][h], zero);
          const float rd = __builtin_amdgcn_rcpf(dd[8 - 8 * h] + 1e-6f);
          const int o = 8 * h;
          const u32x4 ov = {pk(dd[o] * rd, dd[o + 1] * rd), pk(dd[o + 2] * rd, dd[o + 3] * rd), pk(dd[o + 4] * rd, dd[o + 5] * rd), pk(dd[o + 6] * rd, dd[o + 7] * rd)};
          st128c(ov, lane * 16, rws, WS_O + sb * 32768 + ((pair0 + pl) * 2 + h) * 1024);
        }
      }
    }
    vmbar<0>();
    if (threadIdx.x == 0) st32c(tag, 0, rws, WS_OF + rc * 128 + (isKV ? hg : 8 + qj) * 4);
    TS(6);
    w_issue(s2, 2); w_issue(s2, 3);
  }

  {
    const int sr = w * 8 + (lane >> 3), cq = lane & 7;
    u32 b2v;
    asm volatile("buffer_load_ushort %0, %1, %2, %3 offen" : "=v"(b2v) : "v"(sr * 2), "s"(srd(b2, 4096)), "s"(stl * 128) : "memory");
    const u32 GT = L + 131072;
    if (g < 3) {
      u32x4 wr[16][2]; f32x16 c0, c1;
      w_step<0, 9>(s2, a0, wr); w_step<1, 9>(s2, a0, wr); w_step<2, 9>(s2, a0, wr); w_step<3, 8>(s2, a0, wr);
      bar(); w_issue(s2, 7); vmbar<0>();
      TS(7);
      const int nf = g < 2 ? 8 : 6;
      poll(WS_UF + dt * 128 + (g * 8 + (lane < nf ? lane : 0)) * 4, rws, tag);
      TS(8);
      u32 gv;
      ld32c(gv, lane * 4, rws, WS_G + stl * 256);
      const u32 ub = WS_U + (dt * 2 + hf) * UDB;
      wr_gemm<false>(wr, a0, lane * 16, srd(ws, ub + 86 * 1024), ub + g * 32768, c0, c1);
      asm volatile("" : "+v"(gv));
      *(LAS u32*)(GT + lane * 4) = gv;
      TS(9);
      bar();
      red_write(L, w, lane, c0, c1);
    } else {
      u32x4 wr[16][2]; f32x16 c0, c1;
      w_step<0, 9>(s2, a0, wr); w_step<1, 9>(s2, a0, wr); w_step<2, 9>(s2, a0, wr); w_step<3, 8>(s2, a0, wr);
      bar(); w_issue(s2, 7); vmbar<0>();
      TS(7);
      poll(WS_OF + (stl >> 2) * 128 + (lane < 10 ? lane : 0) * 4, rws, tag);
      TS(8);
      wr_gemm<true>(wr, a0, lane * 16, rws, WS_O + (stl * 2 + hf) * 32768, c0, c1);
      TS(9);
      bar();
      red_write(L, w, lane, c0, c1);
    }
    asm volatile("s_waitcnt lgkmcnt(0)\n\ts_barrier" : "+v"(b2v)::"memory");
    const int db = cq >> 2, sb = w >> 2;
    const u32 lo = (cq & 3) * RSL + (sr & 31) * 16;
    f32x4 y0 = {0.f, 0.f, 0.f, 0.f}, y1 = y0;
#pragma unroll
    for (int k = 0; k < 3; ++k) { const u32 a = L + ((db * 4 + k) * 8 + sb * 4) * RSL + lo; y0 += ldsf4(a); y1 += ldsf4(a + 512); }
    const u32 aa = L + ((sb * 4 + 3) * 8 + db * 4) * RSL + lo;
    const f32x4 a0v = ldsf4(aa), a1v = ldsf4(aa + 512);
    const float gt = *(LAS float*)(GT + sr * 4), bb = blo(b2v), og = 1.f - gt;
    u32x4 ov;
    ov[0] = pk(gt * a0v[0] + og * (y0[0] + bb), gt * a0v[1] + og * (y0[1] + bb));
    ov[1] = pk(gt * a0v[2] + og * (y0[2] + bb), gt * a0v[3] + og * (y0[3] + bb));
    ov[2] = pk(gt * a1v[0] + og * (y1[0] + bb), gt * a1v[1] + og * (y1[1] + bb));
    ov[3] = pk(gt * a1v[2] + og * (y1[2] + bb), gt * a1v[3] + og * (y1[3] + bb));
    *(u32x4*)(out + (size_t)(stl * 64 + sr) * 1024 + dt * 128 + cq * 16) = ov;
    TS(10);
  }
}

extern "C" void kernel_launch(void* const* d_in, const int* in_sizes, int n_in, void* d_out, int out_size, void* d_ws, size_t ws_size, hipStream_t stream) {
  if (XP == 'C' || XP == 'H' || XP == 'I') tiny_first<<<1, 64, 0, stream>>>((char*)d_ws);
  fused_kernel<<<256, 512, 0, stream>>>((const char*)d_in[0], (const char*)d_in[1], (const char*)d_in[2], (const char*)d_in[4], (const char*)d_in[5],
                                        (const char*)d_in[6], (const char*)d_in[7], (const char*)d_in[8], (char*)d_out, (char*)d_ws);
  if (XP == 'G' || XP == 'H') tiny_last<<<1, 64, 0, stream>>>((char*)d_ws);
}
```

```cpp
#include <hip/hip_runtime.h>
#include <hip/hip_bf16.h>

#pragma clang diagnostic ignored "-Wint-to-pointer-cast"
#define DI __device__ __forceinline__
#define LAS __attribute__((address_space(3)))
#define SB() __builtin_amdgcn_sched_barrier(0)
typedef unsigned u32;
typedef u32 u32x4 __attribute__((ext_vector_type(4)));
typedef u32 u32x2 __attribute__((ext_vector_type(2)));
typedef float f32x2 __attribute__((ext_vector_type(2)));
typedef float f32x4 __attribute__((ext_vector_type(4)));
typedef float f32x16 __attribute__((ext_vector_type(16)));
typedef __bf16 bf16x8 __attribute__((ext_vector_type(8)));
typedef __bf16 bf16x2 __attribute__((ext_vector_type(2)));
typedef short s16x4 __attribute__((ext_vector_type(4)));

constexpr u32 WS_E = 0, WS_RF = 4096, WS_UF = 8192, WS_OF = 16384, WS_G = 32768;
constexpr u32 WS_KV = 65536, WS_Z = WS_KV + 262144, WS_H = 1u << 20, WS_U = 3u << 20, WS_O = 5u << 20, WS_END = 7u << 20;
constexpr u32 UDB = 88 * 1024;
constexpr int SPIN_MAX = 20000;
#define DSC "sc1"
#define R1 3
#define R2 3
#define RA 7
#define WIN 10

#define TS(i)
#define TSP
#define TSA
#define TSK
#define TSBEG
#define TSEND

DI u32x4 srd(const void* p, u32 bytes) { const unsigned long long a = (unsigned long long)p; return u32x4{(u32)a, (u32)(a >> 32), bytes, 0x00020000u}; }
DI f32x16 mfma(u32x4 a, u32x4 b, f32x16 c) { return __builtin_amdgcn_mfma_f32_32x32x16_bf16(__builtin_bit_cast(bf16x8, a), __builtin_bit_cast(bf16x8, b), c, 0, 0, 0); }
DI u32x2 trd(u32 a) { return __builtin_bit_cast(u32x2, __builtin_amdgcn_ds_read_tr16_b64_v4i16((LAS s16x4*)a)); }
DI u32x4 trd2(u32 a, u32 b) { const u32x2 x = trd(a), y = trd(b); return u32x4{x[0], x[1], y[0], y[1]}; }
DI u32x4 lds128(u32 a) { return *(LAS u32x4*)a; }
DI f32x4 ldsf4(u32 a) { return *(LAS f32x4*)a; }
DI float ldsf(u32 a) { return *(LAS float*)a; }
DI u32 pk(float a, float b) { return __builtin_bit_cast(u32, __builtin_convertvector(f32x2{a, b}, bf16x2)); }
DI float blo(u32 v) { return __uint_as_float(v << 16); }
DI float bhi(u32 v) { return __uint_as_float(v & 0xffff0000u); }
DI u32x4 pack8(const f32x16& x, int o) { return u32x4{pk(x[o], x[o + 1]), pk(x[o + 2], x[o + 3]), pk(x[o + 4], x[o + 5]), pk(x[o + 6], x[o + 7])}; }
DI f32x16 splat(float v) { return f32x16{v, v, v, v, v, v, v, v, v, v, v, v, v, v, v, v}; }
DI u32 umin(u32 a, u32 b) { return a < b ? a : b; }
DI u32 umax(u32 a, u32 b) { return a > b ? a : b; }

DI void glds(u32 lds, u32 voff, u32x4 rs, u32 soff) {
  asm volatile("s_mov_b32 m0, %0\n\ts_nop 0\n\tbuffer_load_dwordx4 %1, %2, %3 offen lds" ::"s"(lds), "v"(voff), "s"(rs), "s"(soff) : "memory");
}
template <int OFF> DI void ld128c(u32x4& d, u32 voff, u32x4 rs, u32 soff) {
  asm volatile("buffer_load_dwordx4 %0, %1, %2, %3 offen offset:%4 " DSC : "=v"(d) : "v"(voff), "s"(rs), "s"(soff), "i"(OFF) : "memory");
}
DI void ld128(u32x4& d, u32 voff, u32x4 rs, u32 soff) { asm volatile("buffer_load_dwordx4 %0, %1, %2, %3 offen" : "=v"(d) : "v"(voff), "s"(rs), "s"(soff) : "memory"); }
template <int OFF = 0> DI void ld32c(u32& d, u32 voff, u32x4 rs, u32 soff) { asm volatile("buffer_load_dword %0, %1, %2, %3 offen offset:%4 sc1" : "=v"(d) : "v"(voff), "s"(rs), "s"(soff), "i"(OFF) : "memory"); }
DI void st128c(u32x4 v, u32 voff, u32x4 rs, u32 soff) { asm volatile("buffer_store_dwordx4 %0, %1, %2, %3 offen sc1\n\ts_nop 1" ::"v"(v), "v"(voff), "s"(rs), "s"(soff) : "memory"); }
DI void st32c(u32 v, u32 voff, u32x4 rs, u32 soff) { asm volatile("buffer_store_dword %0, %1, %2, %3 offen sc1" ::"v"(v), "v"(voff), "s"(rs), "s"(soff) : "memory"); }
template <int N> DI void vmwait() { asm volatile("s_waitcnt vmcnt(%0)" ::"i"(N) : "memory"); }
template <int N> DI void vmwait(u32x4& a) { asm volatile("s_waitcnt vmcnt(%1)" : "+v"(a) : "i"(N) : "memory"); }
DI void bar() { asm volatile("s_waitcnt lgkmcnt(0)\n\ts_barrier" ::: "memory"); }
template <int N> DI void vmbar() { asm volatile("s_waitcnt vmcnt(%0) lgkmcnt(0)\n\ts_barrier" ::"i"(N) : "memory"); }

DI void poll(u32 voff, u32x4 ws, u32 tag) {
  for (int it = 0; it < SPIN_MAX; ++it) {
    u32 f;
    asm volatile("buffer_load_dword %0, %1, %2, 0 offen sc1\n\ts_waitcnt vmcnt(0)" : "=&v"(f) : "v"(voff), "s"(ws) : "memory");
    if (__builtin_amdgcn_ballot_w64(f != tag) == 0) break;
    __builtin_amdgcn_s_sleep(2);
  }
}
DI void poll_bar(u32 voff, u32x4 ws, u32 tag) {
  u32 f;
  asm volatile("buffer_load_dword %0, %1, %2, 0 offen sc1\n\ts_waitcnt vmcnt(0)\n\ts_barrier" : "=&v"(f) : "v"(voff), "s"(ws) : "memory");
  if (__builtin_amdgcn_ballot_w64(f != tag) != 0) poll(voff, ws, tag);
}
template <int C> DI float dppadd(float v) { return v + __int_as_float(__builtin_amdgcn_update_dpp(0, __float_as_int(v), C, 0xf, 0xf, true)); }
DI float wsum(float v) {
  v = dppadd<0xB1>(v); v = dppadd<0x4E>(v); v = dppadd<0x141>(v); v = dppadd<0x140>(v);
  const int i = __float_as_int(v);
  return (__int_as_float(__builtin_amdgcn_readlane(i, 0)) + __int_as_float(__builtin_amdgcn_readlane(i, 16))) +
         (__int_as_float(__builtin_amdgcn_readlane(i, 32)) + __int_as_float(__builtin_amdgcn_readlane(i, 48)));
}
DI float phi(float t) {
  const float e = __builtin_amdgcn_fmed3f(__builtin_amdgcn_exp2f(__builtin_fmaf(t, 1.4426950408889634f, -1.4426950408889634e-6f)), 0.f, 1.f);
  return __builtin_amdgcn_fmed3f(t, 1e-6f, __builtin_inff()) + e;
}
DI float gelu(float v) {
  const float t = v * (-2.3022081981f - 0.1029432397f * v * v);
  return v * __builtin_amdgcn_rcpf(1.f + __builtin_amdgcn_exp2f(t));
}

struct WSrc { u32x4 rs; u32 voff, sbase, stride, lds, rot; };
DI void w_one(const WSrc& s, int c, int i) { glds(s.lds + (c & 3) * 32768 + i * 1024, s.voff, s.rs, s.sbase + (64 * ((c + s.rot) & 7) + 8 * i) * s.stride); }
DI void w_issue(const WSrc& s, int c) {
#pragma unroll
  for (int i = 0; i < 4; ++i) w_one(s, c, i);
}
struct WA4 { u32 a[2][2]; };
DI WA4 w_addr(u32 a0) { WA4 r; r.a[0][0] = a0; r.a[0][1] = a0 ^ 64; r.a[1][0] = a0 + 65536; r.a[1][1] = (a0 ^ 64) + 65536;
  asm volatile("" : "+v"(r.a[0][0]), "+v"(r.a[0][1]), "+v"(r.a[1][0]), "+v"(r.a[1][1])); return r; }
DI u32x4 w_frag(const WA4& r, int b, int slot, int ks) { const u32 a = r.a[slot >> 1][b] + (slot & 1) * 32768 + ks * 2048; return trd2(a, a + 1024); }
template <int C> DI void w_stage(const WA4& a0, u32x4 (&wr)[16][2]) {
#pragma unroll
  for (int k = 0; k < 8; ++k) { wr[4 * C + k][0] = w_frag(a0, 0, C + (k >> 2), k & 3); wr[4 * C + k][1] = w_frag(a0, 1, C + (k >> 2), k & 3); }
}
template <int B> DI void w_fill(const WSrc& s, const WA4& a0, u32x4 (&wr)[16][2], int w, u32 tag, u32x4 rws, u32 flag TSP) {
  w_stage<0>(a0, wr);
  TS(B);
  vmbar<8>();
  TS(B + 1);
  if (threadIdx.x == 0) st32c(tag, 0, rws, flag);
  w_issue(s, 4); w_issue(s, 5);
  TS(B + 2);
  if (w == 0) vmwait<9>(); else vmwait<8>();
  bar();
  w_stage<2>(a0, wr);
  bar();
  TS(B + 3);
  w_issue(s, 6); w_issue(s, 7);
}
struct DSrc { u32x4 rs; u32 voff, sb, rot; };
template <int I> DI void d_issue(u32x4 (&d)[32], const DSrc& s) { ld128c<(I & 3) * 1024>(d[I], s.voff, s.rs, s.sb + (((I >> 2) + s.rot) & 7) * 4096); }
template <bool PF, int K> DI void wr_steps(u32x4 (&d)[32], u32x4 (&wr)[16][2], const WA4& a0, const DSrc& s, const WSrc& nx, u32 sig, f32x16& c0, f32x16& c1) {
  if constexpr (K < 32) {
    constexpr int KB = 32 - WIN, issued = WIN + K < 32 ? WIN + K : 32, pf = !PF || K < KB ? 0 : (K - KB < 8 ? K - KB : 8);
    if constexpr (K == KB) {
      if constexpr (PF) bar();
      else { asm volatile("s_waitcnt lgkmcnt(0)" ::: "memory"); *(volatile LAS u32*)sig = 1; }
    }
    vmwait<issued - 1 - K + pf>(d[K]);
    c0 = mfma(wr[K & 15][0], d[K], c0); c1 = mfma(wr[K & 15][1], d[K], c1);
    asm volatile("" : "+v"(c0), "+v"(c1));
    if constexpr (K + WIN < 32) d_issue<K + WIN>(d, s);
    if constexpr (PF && K >= KB && K < KB + 8) w_one(nx, (K - KB) >> 2, (K - KB) & 3);
    if constexpr (K >= 1 && K <= 16) { constexpr int J = K - 1; wr[J][0] = w_frag(a0, 0, J >> 2, J & 3); wr[J][1] = w_frag(a0, 1, J >> 2, J & 3); }
    wr_steps<PF, K + 1>(d, wr, a0, s, nx, sig, c0, c1);
  }
}
template <int I> DI void d_first(u32x4 (&d)[32], const DSrc& s) { if constexpr (I < WIN) { d_issue<I>(d, s); d_first<I + 1>(d, s); } }
template <bool PF> DI void wr_gemm(u32x4 (&wr)[16][2], const WA4& a0, const DSrc& s, const WSrc& nx, u32 sig, f32x16& c0, f32x16& c1) {
  u32x4 d[32];
  d_first<0>(d, s);
  c0 = splat(0.f); c1 = splat(0.f);
  wr_steps<PF, 0>(d, wr, a0, s, nx, sig, c0, c1);
}
constexpr u32 RSL = 1088;
DI void red_write(u32 a, u32 bs, const f32x16& c0, const f32x16& c1) {
#pragma unroll
  for (int q = 0; q < 4; ++q) {
    *(LAS f32x4*)(a + q * RSL) = f32x4{c0[4 * q], c0[4 * q + 1], c0[4 * q + 2], c0[4 * q + 3]};
    *(LAS f32x4*)(a + bs + q * RSL) = f32x4{c1[4 * q], c1[4 * q + 1], c1[4 * q + 2], c1[4 * q + 3]};
  }
}

constexpr u32 AST = 49152;
struct ASrc { u32x4 rx, rw; u32 vx0, vx1, vw, vq, sx, s0, s1, sq, rot, L; int w; };
DI void a_one(const ASrc& s, int c, int j) {
  const u32 st = s.L + (c % 3) * AST, cc = (c + s.rot) & 7;
  if (j < 4) glds(st + s.w * 4096 + j * 1024, (j & 1) ? s.vx1 : s.vx0, s.rx, s.sx + j * 8192 + cc * 128);
  else glds(st + 32768 + (j - 4) * 8192 + s.w * 1024, s.vw, s.rw, (j == 4 ? s.s0 : s.s1) + cc * (64 * 3072));
}
template <bool KV, int C, int J> DI void a_pref(const ASrc& s, const WSrc& nx) {
  if constexpr (C < 8) a_one(s, C, J);
  else if constexpr (J < 4) {
    if constexpr (C == 8) { if constexpr (KV) glds(s.L + 98304 + s.w * 4096 + J * 1024, s.vq, s.rw, s.sq + J * (16 * 3072)); }
    else w_one(nx, C - 9, J);
  }
}
struct AAd { u32 x0[4], x1[4], wa; };
template <bool KV, int T> DI void a_steps(const ASrc& s, const WSrc& nx, AAd& ad, u32x4 (&x0)[33], u32x4 (&f)[2][3], f32x16 (&c)[4], u32& cf, u32 cfoff, u32x4 rws) {
  if constexpr (T < 32) {
    constexpr int C = T >> 2, K = T & 3, P = T & 1, N = (T + 1) & 3;
    if constexpr (K == 3) {
      if constexpr (C < 6) vmbar<6>(); else if constexpr (C == 6) vmbar<KV ? 4 : 0>(); else bar();
      if constexpr (C < 7) {
        constexpr u32 dl = (C + 1) % 3 ? AST : 0u - 2 * AST;
#pragma unroll
        for (int k = 0; k < 4; ++k) { ad.x0[k] += dl; ad.x1[k] += dl; }
        ad.wa += dl;
        asm volatile("" : "+v"(ad.x0[0]), "+v"(ad.x0[1]), "+v"(ad.x0[2]), "+v"(ad.x0[3]), "+v"(ad.x1[0]), "+v"(ad.x1[1]), "+v"(ad.x1[2]), "+v"(ad.x1[3]), "+v"(ad.wa));
      }
      if constexpr (C == 3) ld32c(cf, cfoff, rws, 0);
    }
    constexpr int PC = K == 3 ? C + 3 : C + 2, PJ = K == 3 ? 0 : C == 0 ? 2 * K : 2 + 2 * K;
    constexpr bool pf = K != 2 || C == 0;
    if constexpr (T < 31) { x0[T + 1] = lds128(ad.x0[N]); f[P ^ 1][1] = trd2(ad.wa + N * 2048, ad.wa + N * 2048 + 512); }
    SB();
    if constexpr (KV) c[0] = mfma(x0[T], f[P][1], c[0]); else c[0] = mfma(f[P][1], x0[T], c[0]);
    SB();
    if constexpr (T < 31) { f[P ^ 1][0] = lds128(ad.x1[N]); f[P ^ 1][2] = trd2(ad.wa + 8192 + N * 2048, ad.wa + 8192 + N * 2048 + 512); }
    SB();
    if constexpr (KV) c[1] = mfma(f[P][0], f[P][1], c[1]); else c[1] = mfma(f[P][1], f[P][0], c[1]);
    SB();
    if constexpr (pf) a_pref<KV, PC, PJ>(s, nx);
    SB();
    if constexpr (KV) c[2] = mfma(x0[T], f[P][2], c[2]); else c[2] = mfma(f[P][2], x0[T], c[2]);
    SB();
    if constexpr (pf) a_pref<KV, PC, PJ + 1>(s, nx);
    SB();
    if constexpr (KV) c[3] = mfma(f[P][0], f[P][2], c[3]); else c[3] = mfma(f[P][2], f[P][0], c[3]);
    SB();
    a_steps<KV, T + 1>(s, nx, ad, x0, f, c, cf, cfoff, rws);
  }
}

template <bool KV> DI u32 attn(const char* x, const char* wqkv, u32x4 rws, const WSrc& s2, u32 L, int lane, int w, int xcd, int slot, u32& flag TSP) {
  const int hh = lane >> 5, col = lane & 31, q4 = (lane & 15) >> 2, mi = w >> 1, ni = w & 1;
  const int ai = slot - 22, rc = (xcd & 6) + (KV ? ai >> 2 : slot - 30), hg = (xcd & 1) * 4 + (ai & 3), qj = xcd & 1;
  u32 ev;
  ld32c(ev, 0, rws, WS_E);
  if (KV && w < 2) {
    const u32 o = (hg * 2 + w) * 8 + rc; const u32x4 bad = {~0u, ~0u, ~0u, ~0u};
    st128c(bad, lane * 16, rws, WS_KV + o * 2048); st128c(bad, lane * 16, rws, WS_KV + o * 2048 + 1024); st32c(~0u, col * 4, rws, WS_Z + o * 128);
  }
  ASrc sa;
  sa.rx = srd(x, 2048 * 1024); sa.rw = srd(wqkv, 512 * 3072); sa.L = L; sa.w = w;
  sa.rot = ((rc & 1) * 4 + (rc >> 1) + (KV ? 2 * (ai & 3) : 1)) & RA;
  sa.vx0 = (lane >> 3) * 1024 + ((lane & 7) ^ (lane >> 4)) * 16; sa.vx1 = sa.vx0 ^ 64;
  sa.vw = (lane >> 3) * 3072 + ((lane & 7) ^ ((lane >> 2) & 4)) * 16;
  sa.vq = (lane >> 2) * 3072 + (lane & 3) * 16;
  sa.sx = (rc * 256 + w * 32) * 1024;
  sa.s0 = w * 8 * 3072 + (KV ? 1024 + hg * 128 : 512 + qj * 256);
  sa.s1 = w * 8 * 3072 + (KV ? 2048 + hg * 128 : 640 + qj * 256);
  sa.sq = ((w + sa.rot) & 7) * (64 * 3072) + hg * 64;
#pragma unroll
  for (int j = 0; j < 6; ++j) a_one(sa, 0, j);
  asm volatile("s_barrier" ::: "memory");
#pragma unroll
  for (int j = 0; j < 6; ++j) a_one(sa, 1, j);
  AAd ad;
#pragma unroll
  for (int k = 0; k < 4; ++k) { ad.x0[k] = L + (mi * 64 + ni * 32 + col) * 128 + ((2 * k + hh) ^ ((col >> 1) & 7)) * 16; ad.x1[k] = ad.x0[k] ^ 4096; }
  ad.wa = L + 32768 + (8 * hh + q4) * 128 + 64 * ((q4 >> 1) ^ ni) + 32 * ((lane >> 4) & 1) + 8 * (lane & 3);
  const int pair0 = KV ? hg : 8 + 4 * qj, pr = KV ? pair0 : pair0 + mi;
  const u32 cfoff = WS_RF + (128 + 6 + ((lane & 6) + (pr >> 3)) * 16 + (lane & 1) * 4 + ((pr >> 1) & 3)) * 4;
  asm volatile("s_waitcnt vmcnt(6)\n\ts_barrier" : "+v"(ev), "+v"(ad.x0[0]), "+v"(ad.x0[1]), "+v"(ad.x0[2]), "+v"(ad.x0[3]), "+v"(ad.x1[0]), "+v"(ad.x1[1]), "+v"(ad.x1[2]), "+v"(ad.x1[3]), "+v"(ad.wa)::"memory");
  TS(1);
  const u32 tag = __builtin_amdgcn_readfirstlane(ev) * 1664525u + 1013904223u;
  if (threadIdx.x == 0) st32c(tag, 0, rws, WS_RF + (128 + xcd * 16 + slot - 16) * 4);
  u32x4 x0[33], f[2][3]; f32x16 c[4]; u32 cf;
  c[0] = c[1] = splat(1e-6f); c[2] = c[3] = splat(KV ? 0.f : 1e-6f);
  x0[0] = lds128(ad.x0[0]); f[0][0] = lds128(ad.x1[0]); f[0][1] = trd2(ad.wa, ad.wa + 512); f[0][2] = trd2(ad.wa + 8192, ad.wa + 8192 + 512);
  a_steps<KV, 0>(sa, s2, ad, x0, f, c, cf, cfoff, rws);
  TS(2);
  w_one(s2, 1, 2); w_one(s2, 1, 3);
  asm volatile("" : "+v"(cf), "+v"(c[0]), "+v"(c[1]), "+v"(c[2]), "+v"(c[3]));
  TS(3);
  const u32 S = L + 147456, KT = KV ? S + 14336 : S, ZT = KT + (KV ? 1024 : 4096);
  u32x4 pq[KV ? 1 : 4][2];
  if constexpr (KV) {
    f32x16 d1 = splat(0.f); float zs = 0.f;
#pragma unroll
    for (int b = 0; b < 2; ++b) {
#pragma unroll
      for (int i = 0; i < 16; ++i) { c[b][i] = phi(c[b][i]); zs += c[b][i]; }
#pragma unroll
      for (int s = 0; s < 2; ++s) d1 = mfma(pack8(c[b], 8 * s), pack8(c[2 + b], 8 * s), d1);
    }
    const bool lo = col < 16;
    f32x4 k0 = {lo ? d1[0] : d1[8], lo ? d1[1] : d1[9], lo ? d1[2] : d1[10], lo ? d1[3] : d1[11]};
    f32x4 k1 = {lo ? d1[4] : d1[12], lo ? d1[5] : d1[13], lo ? d1[6] : d1[14], lo ? d1[7] : d1[15]};
    *(LAS float*)(S + 12288 + threadIdx.x * 4) = zs;
    if (w >= 2) { *(LAS f32x4*)(S + (w - 2) * 2048 + lane * 16) = k0; *(LAS f32x4*)(S + (w - 2) * 2048 + 1024 + lane * 16) = k1; }
    TS(13);
    vmbar<8>();
    TS(14);
    if (w < 2) {
      const u32 a = S + w * 2048 + lane * 16, z = S + 12288 + w * 256 + col * 4;
      k0 += (ldsf4(a) + ldsf4(a + 4096)) + ldsf4(a + 8192); k1 += (ldsf4(a + 1024) + ldsf4(a + 5120)) + ldsf4(a + 9216);
      const float t = ((ldsf(z) + ldsf(z + 128)) + (ldsf(z + 512) + ldsf(z + 640))) + ((ldsf(z + 1024) + ldsf(z + 1152)) + (ldsf(z + 1536) + ldsf(z + 1664)));
      u32x4 p0 = __builtin_bit_cast(u32x4, k0), p1 = __builtin_bit_cast(u32x4, k1);
#pragma unroll
      for (int i = 0; i < 4; ++i) { p0[i] = umin(p0[i], ~1u); p1[i] = umin(p1[i], ~1u); }
      const u32 o = (hg * 2 + w) * 8 + rc;
      st128c(p0, lane * 16, rws, WS_KV + o * 2048); st128c(p1, lane * 16, rws, WS_KV + o * 2048 + 1024); st32c(umin(__float_as_uint(t), ~1u), col * 4, rws, WS_Z + o * 128);
    }
    TS(4);
    u32 qa = L + 98304 + (8 * hh + q4) * 64 + 32 * ((lane >> 4) & 1) + 8 * (lane & 3);
    asm volatile("" : "+v"(qa));
    f32x16 cq = splat(1e-6f);
#pragma unroll
    for (int t = 0; t < 32; ++t) cq = mfma(trd2(qa + t * 1024, qa + t * 1024 + 256), x0[t], cq);
    asm volatile("" : "+v"(cq)); TS(15);
#pragma unroll
    for (int i = 0; i < 16; ++i) cq[i] = phi(cq[i]);
    pq[0][0] = pack8(cq, 0); pq[0][1] = pack8(cq, 8);
  } else {
#pragma unroll
    for (int j = 0; j < 4; ++j) {
#pragma unroll
      for (int i = 0; i < 16; ++i) c[j][i] = phi(c[j][i]);
      pq[j][0] = pack8(c[j], 0); pq[j][1] = pack8(c[j], 8);
    }
    if (blockIdx.x == 255 && w == 0) {
#pragma unroll 1
      for (int i = 0; i < 4; ++i) poll(WS_RF + (i * 64 + lane) * 4, rws, tag);
      if (lane == 0) st32c(tag, 0, rws, WS_E);
    }
  }
  asm volatile("" : "+v"(pq[0][0]), "+v"(pq[0][1]), "+v"(pq[KV ? 0 : 1][0]), "+v"(pq[KV ? 0 : 1][1]));
  asm volatile("" : "+v"(pq[KV ? 0 : 2][0]), "+v"(pq[KV ? 0 : 2][1]));
  asm volatile("" : "+v"(pq[KV ? 0 : 3][0]), "+v"(pq[KV ? 0 : 3][1]));
  TS(5);
  if (!KV || mi == 1) {
    if (__builtin_amdgcn_ballot_w64(cf != tag) != 0) poll(cfoff, rws, tag);
    f32x4 a; float zs;
    for (int it = 0; it < SPIN_MAX; ++it) {
      u32x4 t[8]; u32 z[8];
#pragma unroll
      for (int r = 0; r < 8; ++r) ld128c<0>(t[r], lane * 16, rws, WS_KV + (pr * 8 + r) * 2048 + ni * 1024);
      const u32 zo = WS_Z + pr * 1024 + ni * 64, zv = (lane & 15) * 4;
      ld32c<0>(z[0], zv, rws, zo); ld32c<128>(z[1], zv, rws, zo); ld32c<256>(z[2], zv, rws, zo); ld32c<384>(z[3], zv, rws, zo);
      ld32c<512>(z[4], zv, rws, zo); ld32c<640>(z[5], zv, rws, zo); ld32c<768>(z[6], zv, rws, zo); ld32c<896>(z[7], zv, rws, zo);
      asm volatile("s_waitcnt vmcnt(0)" : "+v"(t[0]), "+v"(t[1]), "+v"(t[2]), "+v"(t[3]), "+v"(t[4]), "+v"(t[5]), "+v"(t[6]), "+v"(t[7]),
                   "+v"(z[0]), "+v"(z[1]), "+v"(z[2]), "+v"(z[3]), "+v"(z[4]), "+v"(z[5]), "+v"(z[6]), "+v"(z[7])::"memory");
      u32 m = 0;
#pragma unroll
      for (int r = 0; r < 8; ++r) m = umax(umax(umax(m, t[r][0]), umax(t[r][1], t[r][2])), umax(t[r][3], z[r]));
      a = __builtin_bit_cast(f32x4, t[0]); zs = __uint_as_float(z[0]);
#pragma unroll
      for (int r = 1; r < 8; ++r) { a += __builtin_bit_cast(f32x4, t[r]); zs += __uint_as_float(z[r]); }
      if (__builtin_amdgcn_ballot_w64(m == ~0u) == 0) break;
    }
    TS(16);
    const int pl = KV ? 0 : mi;
    *(LAS u32x2*)(KT + pl * 1024 + lane * 16 + ni * 8) = u32x2{pk(a[0], a[1]), pk(a[2], a[3])};
    if (lane < 16) *(LAS unsigned short*)(ZT + pl * 64 + ni * 32 + ((lane >> 2) & 1) * 16 + ((lane & 3) + 4 * (lane >> 3)) * 2) = (unsigned short)pk(zs, zs);
  }
  bar();
  TS(6);
  constexpr int NJ = KV ? 1 : 4;
  u32x4 af[KV ? 1 : 2][2]; f32x16 dd[NJ][2];
#pragma unroll
  for (int j = 0; j < (KV ? 1 : 2); ++j)
#pragma unroll
    for (int h = 0; h < 2; ++h) { const int pl = KV ? 0 : j * 2 + ni; af[j][h] = lds128(((col < 16) == (h == 0)) ? KT + pl * 1024 + lane * 16 : ZT + pl * 64 + h * 32 + hh * 16); }
#pragma unroll
  for (int j = 0; j < NJ; ++j)
#pragma unroll
    for (int h = 0; h < 2; ++h) dd[j][h] = mfma(af[j >> 1][h], pq[j][h], splat(0.f));
  SB();
#pragma unroll
  for (int j = 0; j < NJ; ++j)
#pragma unroll
    for (int h = 0; h < 2; ++h) {
      const int pl = KV ? 0 : (j >> 1) * 2 + ni, sb = rc * 8 + mi * 2 + (ni ^ (j & 1)), o = 8 * h;
      const f32x16& e = dd[j][h];
      const float rd = __builtin_amdgcn_rcpf(e[8 - o] + 1e-6f);
      const u32x4 ov = {pk(e[o] * rd, e[o + 1] * rd), pk(e[o + 2] * rd, e[o + 3] * rd), pk(e[o + 4] * rd, e[o + 5] * rd), pk(e[o + 6] * rd, e[o + 7] * rd)};
      st128c(ov, lane * 16, rws, WS_O + sb * 32768 + ((pair0 + pl) * 2 + h) * 1024);
    }
  TS(17);
  vmbar<2 * NJ>();
  flag = WS_OF + rc * 128 + (KV ? hg : 8 + qj) * 4;
  return tag;
}

__global__ __launch_bounds__(512, 2) void fused_kernel(const char* __restrict__ x, const char* __restrict__ wqkv, const char* __restrict__ wo,
                                                        const char* __restrict__ w1, const char* __restrict__ b1, const char* __restrict__ w2,
                                                        const char* __restrict__ b2, const char* __restrict__ gw, char* __restrict__ out, char* ws TSK) {
  __shared__ __attribute__((aligned(1024))) char sm[163840];
  const u32 L = (u32)(size_t)(LAS char*)sm;
  const int lane = threadIdx.x & 63, w = __builtin_amdgcn_readfirstlane(threadIdx.x >> 6);
  const int xcd = blockIdx.x & 7, slot = blockIdx.x >> 3;
  const int hh = lane >> 5;
  const u32x4 rws = srd(ws, WS_END);
  TSBEG; TS(0);
  const int g = w >> 1, hf = w & 1, q4 = (lane & 15) >> 2;
  const u32 wch = (lane & 7) ^ ((lane >> 2) & 4);
  const u32 wa0 = L + g * 8192 + (4 * hh + q4) * 128 + 64 * (q4 >> 1) + 32 * ((lane >> 4) & 1) + 8 * (lane & 3);

  const int dt = (xcd & 1) * 4 + (slot & 3), stl = (xcd >> 1) * 8 + (slot >> 2);
  WSrc s2;
  s2.rs = g < 3 ? srd(w2, 1376 * 4096) : srd(wo, 512 * 1024); s2.stride = g < 3 ? 4096 : 1024; s2.lds = L + g * 8192 + hf * 4096;
  s2.voff = (lane >> 3) * s2.stride + wch * 16;
  s2.sbase = g < 3 ? (g * 512 + hf * 32) * 4096 + stl * 128 : hf * 32 * 1024 + dt * 128;
  s2.rot = 2 * ((slot + (slot >> 2)) & R2);
  u32 tag, flag;
  asm volatile("s_nop 4" ::: "memory");

  if (slot < 22) {
    const int gi = (xcd >> 1) * 22 + slot, ft = gi >> 2, dt1 = (xcd & 1) * 4 + (gi & 3);
    WSrc s1;
    s1.rs = srd(w1, 2048 * 2752); s1.stride = 2752; s1.lds = s2.lds;
    s1.voff = (lane >> 3) * 2752 + (ft == 21 ? (wch & 3) : wch) * 16;
    s1.sbase = (g * 512 + hf * 32) * 2752 + ft * 128;
    s1.rot = 2 * ((gi + ft) & R1);
    u32 ev;
    ld32c(ev, 0, rws, WS_E);
    if (slot < 16) {
      const int sb = xcd * 16 + slot;
      u32x4 xr[2], gr;
      const u32x4 rx = srd(x, 2048 * 1024);
      ld128(xr[0], lane * 16, rx, (sb * 16 + w) * 1024); ld128(xr[1], lane * 16, rx, (sb * 16 + w + 8) * 1024);
      ld128(gr, lane * 16, srd(gw, 1024), 0);
      asm volatile("s_barrier" ::: "memory");
      w_issue(s1, 0);
      asm volatile("s_waitcnt vmcnt(4)" : "+v"(xr[0]), "+v"(xr[1]), "+v"(gr)::"memory");
      TS(1);
      const u32 st = L + 131072;
      float gd[2];
#pragma unroll
      for (int j = 0; j < 2; ++j) {
        float v[8], s = 0.f, q = 0.f; gd[j] = 0.f;
#pragma unroll
        for (int i = 0; i < 4; ++i) {
          v[2 * i] = blo(xr[j][i]); v[2 * i + 1] = bhi(xr[j][i]);
          s += v[2 * i] + v[2 * i + 1]; gd[j] += v[2 * i] * blo(gr[i]) + v[2 * i + 1] * bhi(gr[i]);
        }
        const float m = wsum(s) * (1.f / 512.f);
#pragma unroll
        for (int i = 0; i < 8; ++i) { v[i] -= m; q += v[i] * v[i]; }
        const float r = __builtin_amdgcn_rsqf(wsum(q) * (1.f / 512.f) + 1e-5f);
        *(LAS u32x4*)(st + (w + 8 * j) * 1088 + lane * 16) = u32x4{pk(v[0] * r, v[1] * r), pk(v[2] * r, v[3] * r), pk(v[4] * r, v[5] * r), pk(v[6] * r, v[7] * r)};
      }
      TS(13);
      bar();
      const u32 ta = st + (4 * hh + q4) * 1088 + w * 128 + 32 * ((lane >> 4) & 1) + 8 * (lane & 3);
      const u32x4 h0 = trd2(ta, ta + 8 * 1088), h1 = trd2(ta + 64, ta + 64 + 8 * 1088);
      st128c(h0, lane * 16, rws, WS_H + w * 262144 + sb * 1024);
      st128c(h1, lane * 16, rws, WS_H + w * 262144 + 131072 + sb * 1024);
      const float g0 = wsum(gd[0]), g1 = wsum(gd[1]);
      const float gt = __builtin_amdgcn_rcpf(1.f + __builtin_amdgcn_exp2f((lane ? g1 : g0) * -1.4426950408889634f));
      if (lane < 2) st32c(__float_as_uint(gt), lane * 32, rws, WS_G + (sb * 16 + w) * 4);
      TS(14);
      w_issue(s1, 1); w_issue(s1, 2); w_issue(s1, 3);
      TS(15);
      vmbar<8>();
    } else {
      w_issue(s1, 0); w_issue(s1, 1); w_issue(s1, 2); w_issue(s1, 3);
      vmbar<8>();
    }
    asm volatile("" : "+v"(ev));
    tag = __builtin_amdgcn_readfirstlane(ev) * 1664525u + 1013904223u;
    TS(2);
    const WA4 a0 = w_addr(wa0);
    u32x4 wr[16][2];
    w_fill<18>(s1, a0, wr, w, tag, rws, WS_RF + (xcd * 16 + slot + (slot < 16 ? 0 : 112)) * 4 TSA);
    const int fb = w >> 2, dh = (w >> 1) & 1, hs = w & 1;
    u32x2 bb0, bb1;
    {
      const u32x4 rb = srd(b1, 2752); const u32 o = ft * 128 + fb * 64 + hs * 32;
      asm volatile("buffer_load_dwordx2 %0, %2, %3, %4 offen\n\tbuffer_load_dwordx2 %1, %2, %3, %4 offen offset:16" : "=&v"(bb0), "=&v"(bb1) : "v"(hh * 8), "s"(rb), "s"(o) : "memory");
    }
    TS(3);
    poll_bar(WS_RF + (g * 32 + (lane & 31)) * 4, rws, tag);
    asm volatile("" : "+v"(bb0), "+v"(bb1));
    TS(4);
    f32x16 c0, c1;
    wr_gemm<true>(wr, a0, DSrc{rws, (u32)lane * 16, WS_H + (dt1 * 2 + hf) * 131072 + g * 32768, s1.rot}, s2, 0, c0, c1);
    TS(5);
    const u32 R = L + 163840 - 64 * RSL;
    red_write(R + w * 8 * RSL + lane * 16, 4 * RSL, c0, c1);
    TS(16);
    bar();
    TS(17);
    f32x4 lo = {blo(bb0[0]), bhi(bb0[0]), blo(bb0[1]), bhi(bb0[1])}, hi = {blo(bb1[0]), bhi(bb1[0]), blo(bb1[1]), bhi(bb1[1])};
#pragma unroll
    for (int k = 0; k < 4; ++k) {
      const u32 a = R + ((2 * k + dh) * 8 + fb * 4 + hs * 2) * RSL + lane * 16;
      lo += ldsf4(a); hi += ldsf4(a + RSL);
    }
    const u32x4 uv = {pk(gelu(lo[0]), gelu(lo[1])), pk(gelu(lo[2]), gelu(lo[3])), pk(gelu(hi[0]), gelu(hi[1])), pk(gelu(hi[2]), gelu(hi[3]))};
    st128c(uv, lane * 16, rws, WS_U + (dt1 * 2 + dh) * UDB + (ft * 4 + fb * 2 + hs) * 1024);
    TS(6);
    vmbar<1>();
    flag = WS_UF + dt1 * 128 + ft * 4;
  } else if (slot < 30) tag = attn<true>(x, wqkv, rws, s2, L, lane, w, xcd, slot, flag TSA);
  else tag = attn<false>(x, wqkv, rws, s2, L, lane, w, xcd, slot, flag TSA);

  {
    TS(7);
    const int sr = w * 8 + (lane >> 3), cq = lane & 7;
    u32 b2v;
    asm volatile("buffer_load_ushort %0, %1, %2, %3 offen" : "=v"(b2v) : "v"(sr * 2), "s"(srd(b2, 4096)), "s"(stl * 128) : "memory");
    w_issue(s2, 2); w_issue(s2, 3);
    const u32 GT = L + 131072, SG = GT + 256;
    *(volatile LAS u32*)(SG + w * 4) = 0;
    const WA4 a0 = w_addr(wa0);
    u32x4 wr[16][2]; f32x16 c0, c1;
    w_fill<22>(s2, a0, wr, w, tag, rws, flag TSA);
    TS(8);
    const int nf = g < 2 ? 8 : g == 2 ? 6 : 10;
    poll_bar((g < 3 ? WS_UF + dt * 128 + g * 32 : WS_OF + (stl >> 2) * 128) + (lane < nf ? lane : 0) * 4, rws, tag);
    asm volatile("" : "+v"(b2v));
    TS(9);
    u32 gv;
    ld32c(gv, lane * 4, rws, g < 3 ? WS_G + stl * 256 : WS_OF);
    const u32 ub = WS_U + (dt * 2 + hf) * UDB;
    wr_gemm<false>(wr, a0, DSrc{srd(ws, g < 3 ? ub + 86 * 1024 : WS_END), (u32)lane * 16, g < 3 ? ub + g * 32768 : WS_O + (stl * 2 + hf) * 32768, s2.rot}, s2, SG + w * 4, c0, c1);
    asm volatile("" : "+v"(gv));
    TS(10);
    if (w == 0) *(LAS u32*)(GT + lane * 4) = gv;
    for (int it = 0; it < SPIN_MAX * 16 && *(volatile LAS u32*)(SG + (w ^ 1) * 4) == 0; ++it) {}
    const u32 pb = L + g * 8192 + hf * 65536;
    if (g < 3) {
      const u32 a = pb + hf * 128 + lane * 4;
#pragma unroll
      for (int i = 0; i < 16; ++i) { *(LAS float*)(a + (i >> 1) * 768 + (i & 1) * 272) = c0[i]; *(LAS float*)(a + 32768 + (i >> 1) * 768 + (i & 1) * 272) = c1[i]; }
    } else red_write(pb + lane * 16, 32768, c0, c1);
    bar();
    TS(11);
    const int sl = lane >> 3, i = (sl & 3) + 4 * (w & 3);
    const u32 ya = L + (cq >> 2) * (65536 + 128) + (w >> 2) * 32768 + (i >> 1) * 768 + (i & 1) * 272 + (sl >> 2) * 128 + (cq & 3) * 32;
    const f32x4 y0 = (ldsf4(ya) + ldsf4(ya + 8192)) + ldsf4(ya + 16384), y1 = (ldsf4(ya + 16) + ldsf4(ya + 8208)) + ldsf4(ya + 16400);
    const u32 aa = L + 24576 + (w >> 2) * 65536 + (cq >> 2) * 32768 + (cq & 3) * RSL + (sr & 31) * 16;
    const f32x4 a0v = ldsf4(aa), a1v = ldsf4(aa + 512);
    const float gt = ldsf(GT + sr * 4), bb = blo(b2v), og = 1.f - gt;
    u32x4 ov;
    ov[0] = pk(gt * a0v[0] + og * (y0[0] + bb), gt * a0v[1] + og * (y0[1] + bb));
    ov[1] = pk(gt * a0v[2] + og * (y0[2] + bb), gt * a0v[3] + og * (y0[3] + bb));
    ov[2] = pk(gt * a1v[0] + og * (y1[0] + bb), gt * a1v[1] + og * (y1[1] + bb));
    ov[3] = pk(gt * a1v[2] + og * (y1[2] + bb), gt * a1v[3] + og * (y1[3] + bb));
    *(u32x4*)(out + (size_t)(stl * 64 + sr) * 1024 + dt * 128 + cq * 16) = ov;
    TS(12); TS(26); TSEND;
  }
}

extern "C" void kernel_launch(void* const* d_in, const int* in_sizes, int n_in, void* d_out, int out_size, void* d_ws, size_t ws_size, hipStream_t stream) {
  fused_kernel<<<256, 512, 0, stream>>>((const char*)d_in[0], (const char*)d_in[1], (const char*)d_in[2], (const char*)d_in[4], (const char*)d_in[5],
                                        (const char*)d_in[6], (const char*)d_in[7], (const char*)d_in[8], (char*)d_out, (char*)d_ws);
}
```

```cpp
#include <hip/hip_runtime.h>
#include <hip/hip_bf16.h>

#pragma clang diagnostic ignored "-Wint-to-pointer-cast"
#define DI __device__ __forceinline__
#define LAS __attribute__((address_space(3)))
#define SB() __builtin_amdgcn_sched_barrier(0)
typedef unsigned u32;
typedef u32 u32x4 __attribute__((ext_vector_type(4)));
typedef u32 u32x2 __attribute__((ext_vector_type(2)));
typedef float f32x2 __attribute__((ext_vector_type(2)));
typedef float f32x4 __attribute__((ext_vector_type(4)));
typedef float f32x16 __attribute__((ext_vector_type(16)));
typedef __bf16 bf16x8 __attribute__((ext_vector_type(8)));
typedef __bf16 bf16x2 __attribute__((ext_vector_type(2)));
typedef short s16x4 __attribute__((ext_vector_type(4)));
typedef short s16x2 __attribute__((ext_vector_type(2)));

constexpr u32 WS_E = 0, WS_G = 32768;
constexpr u32 WS_F = 7u << 20, FRS = 641 * 128;
constexpr u32 WS_KV = 65536, WS_Z = WS_KV + 262144, WS_H = 1u << 20, WS_U = 3u << 20, WS_O = 5u << 20, WS_P = 8u << 20, WS_END = 9u << 20, OOB = 1u << 30;
constexpr u32 UDB = 88 * 1024;
constexpr int SPIN_MAX = 20000;
#define DSC "sc1"
#define R1 7
#define R2 3
#define RA 6
#define NF 1
#define NE 2
#define QP 16
#define GI 0
#define NP1 8
#define NP2 8
#define SLP 4
#define MAP 0
#define AGAP {1, 3, 6, 9, 11, 14}
#define PRIO 3
#define WIN 10

#define TS(i)
#define TSP
#define TSA
#define TSK
#define TSBEG
#define TSEND

DI u32x4 srd(const void* p, u32 bytes) { const unsigned long long a = (unsigned long long)p; return u32x4{(u32)a, (u32)(a >> 32), bytes, 0x00020000u}; }
DI f32x16 mfma(u32x4 a, u32x4 b, f32x16 c) { return __builtin_amdgcn_mfma_f32_32x32x16_bf16(__builtin_bit_cast(bf16x8, a), __builtin_bit_cast(bf16x8, b), c, 0, 0, 0); }
DI u32x2 trd(u32 a) { return __builtin_bit_cast(u32x2, __builtin_amdgcn_ds_read_tr16_b64_v4i16((LAS s16x4*)a)); }
DI u32x4 trd2(u32 a, u32 b) { const u32x2 x = trd(a), y = trd(b); return u32x4{x[0], x[1], y[0], y[1]}; }
DI u32x4 lds128(u32 a) { return *(LAS u32x4*)a; }
DI f32x4 ldsf4(u32 a) { return *(LAS f32x4*)a; }
DI float ldsf(u32 a) { return *(LAS float*)a; }
DI u32 pk(float a, float b) { return __builtin_bit_cast(u32, __builtin_convertvector(f32x2{a, b}, bf16x2)); }
DI float blo(u32 v) { return __uint_as_float(v << 16); }
DI float bhi(u32 v) { return __uint_as_float(v & 0xffff0000u); }
DI u32x4 pack8(const f32x16& x, int o) { return u32x4{pk(x[o], x[o + 1]), pk(x[o + 2], x[o + 3]), pk(x[o + 4], x[o + 5]), pk(x[o + 6], x[o + 7])}; }
DI f32x16 splat(float v) { return f32x16{v, v, v, v, v, v, v, v, v, v, v, v, v, v, v, v}; }
DI u32 umin(u32 a, u32 b) { return a < b ? a : b; }
DI u32 umax(u32 a, u32 b) { return a > b ? a : b; }

DI void glds(u32 lds, u32 voff, u32x4 rs, u32 soff) {
  asm volatile("s_mov_b32 m0, %0\n\ts_nop 0\n\tbuffer_load_dwordx4 %1, %2, %3 offen lds" ::"s"(lds), "v"(voff), "s"(rs), "s"(soff) : "memory");
}
template <int OFF> DI void ld128c(u32x4& d, u32 voff, u32x4 rs, u32 soff) {
  asm volatile("buffer_load_dwordx4 %0, %1, %2, %3 offen offset:%4 " DSC : "=v"(d) : "v"(voff), "s"(rs), "s"(soff), "i"(OFF) : "memory");
}
DI void ld128(u32x4& d, u32 voff, u32x4 rs, u32 soff) { asm volatile("buffer_load_dwordx4 %0, %1, %2, %3 offen" : "=v"(d) : "v"(voff), "s"(rs), "s"(soff) : "memory"); }
template <int OFF = 0> DI void ld32c(u32& d, u32 voff, u32x4 rs, u32 soff) { asm volatile("buffer_load_dword %0, %1, %2, %3 offen offset:%4 sc1" : "=v"(d) : "v"(voff), "s"(rs), "s"(soff), "i"(OFF) : "memory"); }
DI void st128c(u32x4 v, u32 voff, u32x4 rs, u32 soff) { asm volatile("buffer_store_dwordx4 %0, %1, %2, %3 offen sc1\n\ts_nop 1" ::"v"(v), "v"(voff), "s"(rs), "s"(soff) : "memory"); }
DI void st32c(u32 v, u32 voff, u32x4 rs, u32 soff) { asm volatile("buffer_store_dword %0, %1, %2, %3 offen sc1" ::"v"(v), "v"(voff), "s"(rs), "s"(soff) : "memory"); }
template <int N> DI void vmwait() { asm volatile("s_waitcnt vmcnt(%0)" ::"i"(N) : "memory"); }
template <int N> DI void vmwait(u32x4& a) { asm volatile("s_waitcnt vmcnt(%1)" : "+v"(a) : "i"(N) : "memory"); }
DI void bar() { asm volatile("s_waitcnt lgkmcnt(0)\n\ts_barrier" ::: "memory"); }
template <int N> DI void vmbar() { asm volatile("s_waitcnt vmcnt(%0) lgkmcnt(0)\n\ts_barrier" ::"i"(N) : "memory"); }

DI void ev_load(u32& e, u32x4 ws) { asm volatile("buffer_load_dword %0, off, %1, 0" : "=v"(e) : "s"(ws) : "memory"); }
template <int N> DI u32 tag_wait(u32 e) {
  asm volatile("s_waitcnt vmcnt(%1)" : "+v"(e) : "i"(N) : "memory");
  return __builtin_amdgcn_readfirstlane(e) * 1664525u + 1013904223u;
}
DI void poll(u32 voff, u32x4 ws, u32 tag) {
  for (int it = 0; it < SPIN_MAX; ++it) {
    u32 f;
    asm volatile("buffer_load_dword %0, %1, %2, 0 offen sc1\n\ts_waitcnt vmcnt(0)" : "=&v"(f) : "v"(voff), "s"(ws) : "memory");
    if (__builtin_amdgcn_ballot_w64(f != tag) == 0) break;
    __builtin_amdgcn_s_sleep(SLP);
  }
}
DI void poll_bar(u32 voff, u32x4 ws, u32 tag) {
  u32 f;
  asm volatile("buffer_load_dword %0, %1, %2, 0 offen sc1\n\ts_waitcnt vmcnt(0) lgkmcnt(0)\n\ts_barrier" : "=&v"(f) : "v"(voff), "s"(ws) : "memory");
  if (__builtin_amdgcn_ballot_w64(f != tag) != 0) poll(voff, ws, tag);
}
template <int C> DI float dppadd(float v) { return v + __int_as_float(__builtin_amdgcn_update_dpp(0, __float_as_int(v), C, 0xf, 0xf, true)); }
DI float rowsum(float v) { v = dppadd<0xB1>(v); v = dppadd<0x4E>(v); v = dppadd<0x141>(v); return dppadd<0x140>(v); }
DI float rdl(float v, int l) { return __int_as_float(__builtin_amdgcn_readlane(__float_as_int(v), l)); }
DI float fold32(float a, float b) { const auto r = __builtin_amdgcn_permlane32_swap(__float_as_uint(a), __float_as_uint(b), false, false); return __uint_as_float(r[0]) + __uint_as_float(r[1]); }
DI float fold16(float a, float b) { const auto r = __builtin_amdgcn_permlane16_swap(__float_as_uint(a), __float_as_uint(b), false, false); return __uint_as_float(r[0]) + __uint_as_float(r[1]); }
constexpr float PH0 = 1.000001f;
DI u32 phi2(float a, float b) {
  f32x2 e = __builtin_elementwise_fma(f32x2{a, b}, f32x2{1.4426950408889634f, 1.4426950408889634f}, f32x2{-1.4426950408889634f * PH0, -1.4426950408889634f * PH0});
  e = f32x2{__builtin_amdgcn_fmed3f(__builtin_amdgcn_exp2f(e[0]), 0.f, 1.f), __builtin_amdgcn_fmed3f(__builtin_amdgcn_exp2f(e[1]), 0.f, 1.f)} + 1e-6f;
  return __builtin_bit_cast(u32, __builtin_elementwise_max(__builtin_bit_cast(s16x2, pk(a, b)), __builtin_bit_cast(s16x2, pk(e[0], e[1]))));
}
DI u32x4 phi8(const f32x16& x, int o) { return u32x4{phi2(x[o], x[o + 1]), phi2(x[o + 2], x[o + 3]), phi2(x[o + 4], x[o + 5]), phi2(x[o + 6], x[o + 7])}; }
DI float gelu(float v) {
  const float t = v * (-2.3022081981f - 0.1029432397f * v * v);
  return v * __builtin_amdgcn_rcpf(1.f + __builtin_amdgcn_exp2f(t));
}

constexpr int wslot(int P, int c) { return (c + (P && c < 3 ? 4 : 1)) % 5; }
struct WSrc { u32x4 rs; u32 voff, sbase, stride, lds, rot; };
template <int P> DI void w_one(const WSrc& s, int c, int i) { glds(s.lds + wslot(P, c) * 32768 + i * 1024, s.voff, s.rs, s.sbase + (64 * ((c + s.rot) & 7) + 8 * i) * s.stride); }
template <int P> DI void w_issue(const WSrc& s, int c) {
#pragma unroll
  for (int i = 0; i < 4; ++i) w_one<P>(s, c, i);
}
struct WA { u32 a[3][2]; };
DI WA w_addr(u32 a0) { WA r;
#pragma unroll
  for (int i = 0; i < 3; ++i) { r.a[i][0] = a0 + i * 65536; r.a[i][1] = (a0 ^ 64) + i * 65536; asm volatile("" : "+v"(r.a[i][0]), "+v"(r.a[i][1])); }
  return r; }
template <int P> DI void w_frag(const WA& r, u32x4 (&f)[2], int c, int ks) {
#pragma unroll
  for (int b = 0; b < 2; ++b) { const u32 a = r.a[wslot(P, c) >> 1][b] + (wslot(P, c) & 1) * 32768 + ks * 2048; f[b] = trd2(a, a + 1024); }
}
template <int P, int C, int F> DI void w_round(const WSrc& s, const WA& a0, u32x4 (&wr)[16][2]) {
#pragma unroll
  for (int k = 0; k < 4; ++k) { w_frag<P>(a0, wr[4 * C + k], C, k); SB(); if constexpr (F < 8) w_one<P>(s, F, k); SB(); }
}
DI void w_stage(const WA& a0, u32x4 (&wr)[16][2], int k0, int k1) {
#pragma unroll
  for (int k = k0; k < k1; ++k) w_frag<1>(a0, wr[k], k >> 2, k & 3);
}
template <int C0> DI void w_flag(const WSrc& s, int w, u32 tag, u32 fv, u32x4 rws, u32 flag) {
  if constexpr (C0 < 6) w_issue<1>(s, C0);
  vmbar<(C0 < 6 ? 4 : 0)>();
  if (w == 0) st32c(tag, fv, rws, flag);
#pragma unroll
  for (int c = C0 + 1; c < 6; ++c) w_issue<1>(s, c);
}
template <int N> DI void vmbar0(int w) { if (w == 0) vmwait<N + 1>(); else vmwait<N>(); bar(); }
struct DSrc { u32x4 rs; u32 voff, sb, rot; };
template <int I> DI void d_issue(u32x4 (&d)[32], const DSrc& s) { ld128c<(I & 3) * 1024>(d[I], s.voff, s.rs, s.sb + (((I >> 2) + s.rot) & 7) * 4096); }
constexpr int KB = 32 - WIN;
template <int P, int K> DI void wr_steps(u32x4 (&d)[32], u32x4 (&wr)[16][2], const WA& a0, const DSrc& s, const WSrc& nx, u32 sig, f32x16& c0, f32x16& c1) {
  if constexpr (K < 32) {
    if constexpr (K == KB) {
      if constexpr (P == 0) bar();
      else { asm volatile("s_waitcnt lgkmcnt(0)" ::: "memory"); *(volatile LAS u32*)sig = 1; }
    }
    if constexpr (K % 8 == 0) __builtin_amdgcn_s_setprio(3 - K / 8);
    vmwait<(P == 0 || K < KB ? WIN - 1 : 31 - K)>(d[K]);
    c0 = mfma(wr[K & 15][0], d[K], c0); c1 = mfma(wr[K & 15][1], d[K], c1);
    asm volatile("" : "+v"(c0), "+v"(c1));
    if constexpr (K + WIN < 32) d_issue<K + WIN>(d, s);
    if constexpr (P == 0 && K == 1) asm volatile("s_barrier" ::: "memory");
    if constexpr (P == 0 && K >= KB) w_one<1>(nx, (K - KB) >> 2, (K - KB) & 3);
    if constexpr (K >= 1 && K <= 16) w_frag<P>(a0, wr[K - 1], 4 + ((K - 1) >> 2), (K - 1) & 3);
    wr_steps<P, K + 1>(d, wr, a0, s, nx, sig, c0, c1);
  }
}
template <int I> DI void d_first(u32x4 (&d)[32], const DSrc& s) { if constexpr (I < WIN) { d_issue<I>(d, s); d_first<I + 1>(d, s); } }
template <int P> DI void wr_gemm(u32x4 (&wr)[16][2], const WA& a0, const DSrc& s, const WSrc& nx, u32 sig, f32x16& c0, f32x16& c1) {
  u32x4 d[32];
  d_first<0>(d, s);
  c0 = splat(0.f); c1 = splat(0.f);
  wr_steps<P, 0>(d, wr, a0, s, nx, sig, c0, c1);
  if constexpr (P == 0) {
#pragma unroll
    for (int i = WIN; i < 12; ++i) w_one<1>(nx, i >> 2, i & 3);
  }
}
constexpr u32 RSL = 1088;
template <u32 RS> DI void red_write(u32 a, u32 bs, const f32x16& c0, const f32x16& c1) {
#pragma unroll
  for (int q = 0; q < 4; ++q) {
    *(LAS f32x4*)(a + q * RS) = f32x4{c0[4 * q], c0[4 * q + 1], c0[4 * q + 2], c0[4 * q + 3]};
    *(LAS f32x4*)(a + bs + q * RS) = f32x4{c1[4 * q], c1[4 * q + 1], c1[4 * q + 2], c1[4 * q + 3]};
  }
}

template <int O, int I> DI void put16(const f32x16& c) {
  if constexpr (I < 16) { asm volatile("ds_write_addtid_b32 %0 offset:%1" ::"v"(c[I]), "i"(O + (I >> 1) * 768 + (I & 1) * 272) : "memory"); put16<O, I + 1>(c); }
}

constexpr u32 AST = 49152;
struct ASrc { u32x4 rx, rw; u32 vx0, vx1, vw, vq, sx, s0, s1, sq, rot, L; int w; };
DI void a_one(const ASrc& s, int c, int j) {
  const u32 st = s.L + (c % 3) * AST, cc = (c + s.rot) & 7;
  if (j < 4) glds(st + s.w * 4096 + j * 1024, (j & 1) ? s.vx1 : s.vx0, s.rx, s.sx + j * 8192 + cc * 128);
  else glds(st + 32768 + (j - 4) * 8192 + s.w * 1024, s.vw, s.rw, (j == 4 ? s.s0 : s.s1) + cc * (64 * 3072));
}
template <bool KV, int C, int J> DI void a_pref(const ASrc& s, const WSrc& nx) {
  if constexpr (J < 0) {}
  else if constexpr (C < 8) a_one(s, C, J);
  else if constexpr (C == 8) { if constexpr (KV && J < 4) glds(s.L + 98304 + s.w * 4096 + J * 1024, s.vq, s.rw, s.sq + J * (16 * 3072)); }
  else w_one<1>(nx, (6 * (C - 9) + J) >> 2, (6 * (C - 9) + J) & 3);
}
constexpr int a_gap(int C, int g) {
  const int t0[6] = {5, 6, 9, 10, 13, 14}, t1[6] = AGAP, t2[6] = {1, 2, -1, -1, -1, -1};
  for (int j = 0; j < 6; ++j) if ((C == 2 ? t0[j] : C == 10 ? t2[j] : t1[j]) == g) return j;
  return -1;
}
struct AMisc { u32x4 rws; u32 cfoff, tag, rdy, clr; int lane; };
struct AAd { u32 x0[4], x1[4], wa; };
template <bool KV, int T> DI void a_steps(const ASrc& s, const WSrc& nx, AAd& ad, u32x4 (&x0)[33], u32x4 (&f)[2][3], f32x16 (&c)[4], u32& cf, const AMisc& ms TSP) {
  if constexpr (T < 32) {
    constexpr int C = T >> 2, K = T & 3, P = T & 1, N = (T + 1) & 3;
    if constexpr (K == 3) {
      if constexpr (C < 6) vmbar<6 + ((KV && C == 1) || C == 3)>(); else if constexpr (C == 6) vmbar<KV ? 5 : 0>(); else vmbar<6>();
      if constexpr (C < 7) {
        constexpr u32 dl = (C + 1) % 3 ? AST : 0u - 2 * AST;
#pragma unroll
        for (int k = 0; k < 4; ++k) { ad.x0[k] += dl; ad.x1[k] += dl; }
        ad.wa += dl;
        asm volatile("" : "+v"(ad.x0[0]), "+v"(ad.x0[1]), "+v"(ad.x0[2]), "+v"(ad.x0[3]), "+v"(ad.x1[0]), "+v"(ad.x1[1]), "+v"(ad.x1[2]), "+v"(ad.x1[3]), "+v"(ad.wa));
      }
      if constexpr (C == 0 && KV) { if (s.w < 4) st128c(u32x4{~0u, ~0u, ~0u, ~0u}, ms.lane * 16, ms.rws, ms.clr); else st32c(~0u, (ms.lane & 31) * 4, ms.rws, ms.clr); }
      if constexpr (C == 2) { st32c(ms.tag, ms.rdy, ms.rws, 0); }
      if constexpr (C == 5 && KV) ld32c(cf, ms.cfoff, ms.rws, 0);
    }
    constexpr int PC = K == 3 ? C + 3 : C + 2, G0 = 4 * N;
    if (PRIO & 1) __builtin_amdgcn_s_setprio(3 - N);
    if constexpr (T < 31) { x0[T + 1] = lds128(ad.x0[N]); f[P ^ 1][1] = trd2(ad.wa + N * 2048, ad.wa + N * 2048 + 512); }
    SB();
    if constexpr (KV) c[0] = mfma(x0[T], f[P][1], c[0]); else c[0] = mfma(f[P][1], x0[T], c[0]);
    SB();
    if constexpr (T < 31) { f[P ^ 1][0] = lds128(ad.x1[N]); f[P ^ 1][2] = trd2(ad.wa + 8192 + N * 2048, ad.wa + 8192 + N * 2048 + 512); }
    a_pref<KV, PC, a_gap(PC, G0)>(s, nx);
    SB();
    if constexpr (KV) c[1] = mfma(f[P][0], f[P][1], c[1]); else c[1] = mfma(f[P][1], f[P][0], c[1]);
    SB();
    a_pref<KV, PC, a_gap(PC, G0 + 1)>(s, nx);
    SB();
    if constexpr (KV) c[2] = mfma(x0[T], f[P][2], c[2]); else c[2] = mfma(f[P][2], x0[T], c[2]);
    SB();
    a_pref<KV, PC, a_gap(PC, G0 + 2)>(s, nx);
    SB();
    if constexpr (KV) c[3] = mfma(f[P][0], f[P][2], c[3]); else c[3] = mfma(f[P][2], f[P][0], c[3]);
    SB();
    a_pref<KV, PC, a_gap(PC, G0 + 3)>(s, nx);
    SB();
    a_steps<KV, T + 1>(s, nx, ad, x0, f, c, cf, ms TSA);
  }
}

template <bool KV> DI u32 attn(const char* x, const char* wqkv, u32x4 rws, const WSrc& s2, u32 L, int lane, int w, int xcd, int slot, u32 wa0, u32x4 (&wr)[16][2] TSP) {
  u32 ev; ev_load(ev, rws);
  const int hh = lane >> 5, col = lane & 31, q4 = (lane & 15) >> 2, mi = w >> 1, ni = w & 1;
  const int ai = slot - 22, rc = (xcd & 6) + (KV ? ai >> 2 : slot - 30), hg = (xcd & 1) * 4 + (ai & 3), qj = xcd & 1;
  ASrc sa;
  sa.rx = srd(x, 2048 * 1024); sa.rw = srd(wqkv, 512 * 3072); sa.L = L; sa.w = w;
  sa.rot = RA == 7 ? ((rc & 1) * 4 + (rc >> 1) + (KV ? 2 * (ai & 3) : 1)) & 7 : xcd & RA;
  sa.vx0 = (lane >> 3) * 1024 + ((lane & 7) ^ (lane >> 4)) * 16; sa.vx1 = sa.vx0 ^ 64;
  sa.vw = (lane >> 3) * 3072 + ((lane & 7) ^ ((lane >> 2) & 4)) * 16;
  sa.vq = (lane >> 2) * 3072 + (lane & 3) * 16;
  sa.sx = (rc * 256 + w * 32) * 1024;
  sa.s0 = w * 8 * 3072 + (KV ? 1024 + hg * 128 : 512 + qj * 256);
  sa.s1 = w * 8 * 3072 + (KV ? 2048 + hg * 128 : 640 + qj * 256);
  sa.sq = ((w + sa.rot) & 7) * (64 * 3072) + hg * 64;
#pragma unroll
  for (int j = 0; j < 6; ++j) a_one(sa, 0, j);
  asm volatile("s_barrier" ::: "memory");
#pragma unroll
  for (int j = 0; j < 6; ++j) a_one(sa, 1, j);
  AAd ad;
#pragma unroll
  for (int k = 0; k < 4; ++k) { ad.x0[k] = L + (mi * 64 + ni * 32 + col) * 128 + ((2 * k + hh) ^ ((col >> 1) & 7)) * 16; ad.x1[k] = ad.x0[k] ^ 4096; }
  ad.wa = L + 32768 + (8 * hh + q4) * 128 + 64 * ((q4 >> 1) ^ ni) + 32 * ((lane >> 4) & 1) + 8 * (lane & 3);
  const int pr = hg + (mi & 2) * 4;
  const bool ga = mi & 1;
  const u32 cfoff = ga ? WS_F + xcd * FRS + (128 + (pr >> 1) * 8 + (lane & 7)) * 128 : OOB;
  asm volatile("" : "+v"(ad.x0[0]), "+v"(ad.x0[1]), "+v"(ad.x0[2]), "+v"(ad.x0[3]), "+v"(ad.x1[0]), "+v"(ad.x1[1]), "+v"(ad.x1[2]), "+v"(ad.x1[3]), "+v"(ad.wa)::"memory");
  const u32 tag = tag_wait<6>(ev);
  bar();
  TS(1);
  const AMisc ms = {rws, cfoff, tag, lane == 0 ? WS_F + w * FRS + (KV ? 128 + hg * 8 + rc : 192 + qj * 8 + rc) * 128 : OOB, w < 4 ? WS_KV + ((hg * 2 + (w >> 1)) * 8 + rc) * 2048 + (w & 1) * 1024 : WS_Z + ((hg * 2 + (w & 1)) * 8 + rc) * 128, lane};
  u32x4 x0[33], f[2][3]; f32x16 c[4]; u32 cf;
  c[0] = c[1] = splat(PH0); c[2] = c[3] = splat(KV ? 0.f : PH0);
  x0[0] = lds128(ad.x0[0]); f[0][0] = lds128(ad.x1[0]); f[0][1] = trd2(ad.wa, ad.wa + 512); f[0][2] = trd2(ad.wa + 8192, ad.wa + 8192 + 512);
  a_steps<KV, 0>(sa, s2, ad, x0, f, c, cf, ms TSA);
  __builtin_amdgcn_s_setprio(0);
  TS(2);
  asm volatile("" : "+v"(c[0]), "+v"(c[1]), "+v"(c[2]), "+v"(c[3]));
  TS(3);
  const u32 pfl = WS_F + xcd * FRS + (512 + rc * 16 + 8 + qj) * 128;
  if constexpr (!KV) {
#pragma unroll
    for (int j = 0; j < 4; ++j)
#pragma unroll
      for (int h = 0; h < 2; ++h) { st128c(phi8(c[j], 8 * h), lane * 16 + qj * 8192 + ni * 2048, rws, WS_P + (rc * 8 + (w ^ (j & 1))) * 16384 + ((j & 2) * 2 + h) * 1024); SB(); }
    TS(5);
    asm volatile("s_barrier" ::: "memory");
    w_issue<1>(s2, 2);
    vmbar<4>();
    if (w == 0) st32c(tag, lane == 0 ? 0 : OOB, rws, pfl);
    TS(4);
    if (blockIdx.x == 255 && w == 0) {
#pragma unroll 1
      for (int i = 0; i < 4; ++i) poll(WS_F + 7 * FRS + (i * 64 + lane) * 128, rws, tag);
      st32c(tag, lane == 0 ? 0 : OOB, rws, WS_E);
    }
    vmbar<0>();
    w_stage(w_addr(wa0), wr, 0, 12);
    bar();
    w_flag<3>(s2, w, tag, OOB, rws, 0);
    return tag;
  }
  const u32 S = L + 65536, KT = S + 14336, ZT = KT + 2048;
  {
    f32x16 d1 = splat(0.f), d2 = splat(0.f);
#pragma unroll
    for (int b = 0; b < 2; ++b)
#pragma unroll
      for (int s = 0; s < 2; ++s) {
        const u32x4 p = phi8(c[b], 8 * s);
        d1 = mfma(p, pack8(c[2 + b], 8 * s), d1);
        d2 = mfma(u32x4{0x3f803f80u, 0x3f803f80u, 0x3f803f80u, 0x3f803f80u}, p, d2);
        SB(); w_one<1>(s2, 2, 2 * b + s); SB();
      }
    const bool lo = col < 16;
    f32x4 k0 = {lo ? d1[0] : d1[8], lo ? d1[1] : d1[9], lo ? d1[2] : d1[10], lo ? d1[3] : d1[11]};
    f32x4 k1 = {lo ? d1[4] : d1[12], lo ? d1[5] : d1[13], lo ? d1[6] : d1[14], lo ? d1[7] : d1[15]};
    if (w >= 2) { *(LAS f32x4*)(S + (w - 2) * 2048 + lane * 16) = k0; *(LAS f32x4*)(S + (w - 2) * 2048 + 1024 + lane * 16) = k1; *(LAS float*)(S + 12288 + (w - 2) * 256 + lane * 4) = d2[0]; }
    TS(13);
    vmbar<4>();
    TS(14);
    if (w < 2) {
      const u32 a = S + w * 2048 + lane * 16, z = S + 12288 + w * 256 + lane * 4;
      k0 += (ldsf4(a) + ldsf4(a + 4096)) + ldsf4(a + 8192); k1 += (ldsf4(a + 1024) + ldsf4(a + 5120)) + ldsf4(a + 9216);
      const float t = (d2[0] + ldsf(z)) + (ldsf(z + 512) + ldsf(z + 1024));
      u32x4 p0 = __builtin_bit_cast(u32x4, k0), p1 = __builtin_bit_cast(u32x4, k1);
#pragma unroll
      for (int i = 0; i < 4; ++i) { p0[i] = umin(p0[i], ~1u); p1[i] = umin(p1[i], ~1u); }
      const u32 o = (hg * 2 + w) * 8 + rc;
      st128c(p0, lane * 16, rws, WS_KV + o * 2048); st128c(p1, lane * 16, rws, WS_KV + o * 2048 + 1024); st32c(umin(__float_as_uint(t), ~1u), col * 4, rws, WS_Z + o * 128);
    }
  }
  TS(4);
  u32x4 pq[2][2], t[8]; u32 pf, z[8];
  u32 qa = L + 98304 + (8 * hh + q4) * 64 + 32 * ((lane >> 4) & 1) + 8 * (lane & 3);
  asm volatile("" : "+v"(qa));
  f32x16 cq = splat(PH0);
#pragma unroll
  for (int i = 0; i < 32; ++i) {
    cq = mfma(trd2(qa + i * 1024, qa + i * 1024 + 256), x0[i], cq);
    if (i == QP) { asm volatile("" : "+v"(cq)); ld32c(pf, pfl, rws, 0); }
  }
  asm volatile("s_waitcnt vmcnt(0)" : "+v"(pf), "+v"(cf), "+v"(cq)::"memory");
  TS(18);
  if (__builtin_amdgcn_ballot_w64(pf != tag) != 0) poll(pfl, rws, tag);
  if (ga && __builtin_amdgcn_ballot_w64(cf != tag) != 0) poll(cfoff, rws, tag);
  auto sweep = [&](u32 m) {
#pragma unroll
    for (int r = 0; r < 8; ++r) ld128c<0>(t[r], lane * 16 | m, rws, WS_KV + (pr * 8 + r) * 2048 + ni * 1024);
    const u32 zo = WS_Z + pr * 1024 + ni * 64, zv = (lane & 15) * 4 | m;
    ld32c<0>(z[0], zv, rws, zo); ld32c<128>(z[1], zv, rws, zo); ld32c<256>(z[2], zv, rws, zo); ld32c<384>(z[3], zv, rws, zo);
    ld32c<512>(z[4], zv, rws, zo); ld32c<640>(z[5], zv, rws, zo); ld32c<768>(z[6], zv, rws, zo); ld32c<896>(z[7], zv, rws, zo);
  };
  sweep(ga ? 0 : OOB);
  ld128c<0>(pq[1][0], lane * 16, rws, WS_P + (rc * 8 + w) * 16384 + hg * 2048); ld128c<1024>(pq[1][1], lane * 16, rws, WS_P + (rc * 8 + w) * 16384 + hg * 2048);
  const WA a0 = w_addr(wa0);
#pragma unroll
  for (int i = 0; i < 4; ++i) {
    w_stage(a0, wr, 2 * i, 2 * i + 2); SB();
    pq[0][i >> 1][2 * (i & 1)] = phi2(cq[4 * i], cq[4 * i + 1]); pq[0][i >> 1][2 * (i & 1) + 1] = phi2(cq[4 * i + 2], cq[4 * i + 3]); SB();
  }
  TS(5);
  bar();
  w_stage(a0, wr, 8, 12); SB();
#pragma unroll
  for (int c = 3; c < 3 + NE; ++c) w_issue<1>(s2, c);
  TS(15);
#define GW(N) asm volatile("s_waitcnt vmcnt(%16)" : "+v"(t[0]), "+v"(t[1]), "+v"(t[2]), "+v"(t[3]), "+v"(t[4]), "+v"(t[5]), "+v"(t[6]), "+v"(t[7]), \
             "+v"(z[0]), "+v"(z[1]), "+v"(z[2]), "+v"(z[3]), "+v"(z[4]), "+v"(z[5]), "+v"(z[6]), "+v"(z[7]) : "i"(N) : "memory")
  GW(4 * NE);
  asm volatile("" : "+v"(pq[1][0]), "+v"(pq[1][1]));
  if (ga) {
    for (int it = 0; it < SPIN_MAX; ++it) {
      u32 m = 0;
#pragma unroll
      for (int r = 0; r < 8; ++r) m = umax(umax(umax(m, t[r][0]), umax(t[r][1], t[r][2])), umax(t[r][3], z[r]));
      if (__builtin_amdgcn_ballot_w64(m == ~0u) == 0) break;
      sweep(0);
      GW(0);
    }
    f32x4 a = __builtin_bit_cast(f32x4, t[0]); float zs = __uint_as_float(z[0]);
#pragma unroll
    for (int r = 1; r < 8; ++r) { a += __builtin_bit_cast(f32x4, t[r]); zs += __uint_as_float(z[r]); }
    TS(16);
    const int pl = mi >> 1;
    *(LAS u32x2*)(KT + pl * 1024 + lane * 16 + ni * 8) = u32x2{pk(a[0], a[1]), pk(a[2], a[3])};
    if (lane < 16) *(LAS unsigned short*)(ZT + pl * 64 + ni * 32 + ((lane >> 2) & 1) * 16 + ((lane & 3) + 4 * (lane >> 3)) * 2) = (unsigned short)pk(zs, zs);
  }
  bar();
  TS(6);
  f32x16 dd[2][2];
#pragma unroll
  for (int j = 0; j < 2; ++j)
#pragma unroll
    for (int h = 0; h < 2; ++h) dd[j][h] = mfma(lds128(((col < 16) == (h == 0)) ? KT + j * 1024 + lane * 16 : ZT + j * 64 + h * 32 + hh * 16), pq[j][h], splat(0.f));
  SB();
#pragma unroll
  for (int j = 0; j < 2; ++j)
#pragma unroll
    for (int h = 0; h < 2; ++h) {
      const int o = 8 * h;
      const f32x16& e = dd[j][h];
      const float rd = __builtin_amdgcn_rcpf(e[8 - o] + 1e-6f);
      const u32x4 ov = {pk(e[o] * rd, e[o + 1] * rd), pk(e[o + 2] * rd, e[o + 3] * rd), pk(e[o + 4] * rd, e[o + 5] * rd), pk(e[o + 6] * rd, e[o + 7] * rd)};
      st128c(ov, lane * 16, rws, WS_O + (rc * 8 + w) * 32768 + ((hg + 8 * j) * 2 + h) * 1024);
    }
  TS(17);
  bar();
  w_flag<3 + NE>(s2, w, tag, lane < 2 ? ((rc & 6) + lane) * FRS : OOB, rws, WS_F + (512 + rc * 16 + hg) * 128);
  return tag;
}

__global__ __launch_bounds__(512, 2) void fused_kernel(const char* __restrict__ x, const char* __restrict__ wqkv, const char* __restrict__ wo,
                                                        const char* __restrict__ w1, const char* __restrict__ b1, const char* __restrict__ w2,
                                                        const char* __restrict__ b2, const char* __restrict__ gw, char* __restrict__ out, char* ws TSK) {
  __shared__ __attribute__((aligned(1024))) char sm[163840];
  const u32 L = (u32)(size_t)(LAS char*)sm;
  const int lane = threadIdx.x & 63, w = __builtin_amdgcn_readfirstlane(threadIdx.x >> 6);
  const int xcd = blockIdx.x & 7, slot = blockIdx.x >> 3;
  const int hh = lane >> 5;
  const u32x4 rws = srd(ws, WS_END);
  TSBEG; TS(0);
  const int g = w >> 1, hf = w & 1, q4 = (lane & 15) >> 2;
  const u32 wch = (lane & 7) ^ ((lane >> 2) & 4);
  const u32 wa0 = L + g * 8192 + (4 * hh + q4) * 128 + 64 * (q4 >> 1) + 32 * ((lane >> 4) & 1) + 8 * (lane & 3);

  const int dt = (xcd & 1) * 4 + (slot & 3), stl = (xcd >> 1) * 8 + (slot >> 2);
  WSrc s2;
  s2.rs = g < 3 ? srd(w2, 1376 * 4096) : srd(wo, 512 * 1024); s2.stride = g < 3 ? 4096 : 1024; s2.lds = L + g * 8192 + hf * 4096;
  s2.voff = (lane >> 3) * s2.stride + wch * 16;
  s2.sbase = g < 3 ? (g * 512 + hf * 32) * 4096 + stl * 128 : hf * 32 * 1024 + dt * 128;
  s2.rot = 2 * ((slot + (slot >> 2)) & R2);
  u32 tag;
  u32x4 wr[16][2];
  asm volatile("s_nop 4" ::: "memory");

  if (slot < 22) {
    const int gi = slot < 16 ? xcd * 16 + slot : 112 + xcd * 6 + slot;
    const int gj = slot < 16 ? (xcd >> 1) * 16 + slot : 48 + (xcd >> 1) * 6 + slot, ft = gj >> 2, dt1 = (xcd & 1) * 4 + (gj & 3);
    WSrc s1;
    s1.rs = srd(w1, 2048 * 2752); s1.stride = 2752; s1.lds = s2.lds;
    s1.voff = (lane >> 3) * 2752 + (ft == 21 ? (wch & 3) : wch) * 16;
    s1.sbase = (g * 512 + hf * 32) * 2752 + ft * 128;
    s1.rot = ((MAP ? 3 : 2) * (dt1 + ft)) & R1;
    if (slot < 16) {
      const int sb = xcd * 16 + slot;
      u32x4 xr[2], gr; u32 ev;
      ev_load(ev, rws);
      const u32x4 rx = srd(x, 2048 * 1024);
      ld128(xr[0], lane * 16, rx, (sb * 16 + w) * 1024); ld128(xr[1], lane * 16, rx, (sb * 16 + w + 8) * 1024);
      ld128(gr, lane * 16, srd(gw, 1024), 0);
      asm volatile("s_barrier" ::: "memory");
      w_issue<0>(s1, 0);
      tag = tag_wait<4>(ev);
      asm volatile("" : "+v"(xr[0]), "+v"(xr[1]), "+v"(gr)::"memory");
      TS(1);
      f32x2 v[2][4], t; float s[2], gd[2], q[2];
#pragma unroll
      for (int j = 0; j < 2; ++j) {
#pragma unroll
        for (int i = 0; i < 4; ++i) v[j][i] = f32x2{blo(xr[j][i]), bhi(xr[j][i])};
        t = (v[j][0] + v[j][1]) + (v[j][2] + v[j][3]); s[j] = t[0] + t[1];
      }
      const float t0 = fold32(s[0], s[1]), t1 = rowsum(fold16(t0, t0)) * (1.f / 512.f);
#pragma unroll
      for (int j = 0; j < 2; ++j) {
        t = f32x2{0.f, 0.f};
#pragma unroll
        for (int i = 0; i < 4; ++i) t = __builtin_elementwise_fma(v[j][i], f32x2{blo(gr[i]), bhi(gr[i])}, t);
        gd[j] = t[0] + t[1];
        const float m = rdl(t1, 32 * j);
        t = f32x2{0.f, 0.f};
#pragma unroll
        for (int i = 0; i < 4; ++i) { v[j][i] -= m; t = __builtin_elementwise_fma(v[j][i], v[j][i], t); }
        q[j] = t[0] + t[1];
      }
      const float t2 = rowsum(fold16(fold32(q[0], q[1]), fold32(gd[0], gd[1])));
      const float rs = __builtin_amdgcn_rsqf(t2 * (1.f / 512.f) + 1e-5f);
#pragma unroll
      for (int j = 0; j < 2; ++j) {
        const float r = rdl(rs, 32 * j);
#pragma unroll
        for (int i = 0; i < 4; ++i) v[j][i] *= r;
        *(LAS u32x4*)(L + (w + 8 * j) * 1088 + lane * 16) = u32x4{pk(v[j][0][0], v[j][0][1]), pk(v[j][1][0], v[j][1][1]), pk(v[j][2][0], v[j][2][1]), pk(v[j][3][0], v[j][3][1])};
      }
      const float gt = __builtin_amdgcn_rcpf(1.f + __builtin_amdgcn_exp2f(t2 * -1.4426950408889634f));
      TS(13);
      bar();
      const u32 ta = L + (4 * hh + q4) * 1088 + w * 128 + 32 * ((lane >> 4) & 1) + 8 * (lane & 3);
      const u32x4 h0 = trd2(ta, ta + 8 * 1088), h1 = trd2(ta + 64, ta + 64 + 8 * 1088);
      st32c(__float_as_uint(gt), (lane & 31) == 16 ? lane & 32 : OOB, rws, WS_G + (sb * 16 + w) * 4);
      st128c(h0, lane * 16, rws, WS_H + w * 262144 + sb * 1024);
      st128c(h1, lane * 16, rws, WS_H + w * 262144 + 131072 + sb * 1024);
      asm volatile("s_barrier" ::: "memory");
      TS(14);
      w_issue<0>(s1, 1);
      TS(15);
      vmbar<4>();
    } else {
      u32 ev; ev_load(ev, rws);
      w_issue<0>(s1, 0); w_issue<0>(s1, 1);
      tag = tag_wait<4>(ev);
      bar();
    }
    TS(2);
    const WA a0 = w_addr(wa0);
    if (w == 0) st32c(tag, lane < 8 ? lane * FRS : OOB, rws, WS_F + (slot < 16 ? gi : gi + 80) * 128);
    w_round<0, 0, 2>(s1, a0, wr); w_issue<0>(s1, 3);
    TS(18);
    vmbar0<8>(w);
    TS(19);
    w_round<0, 1, 4>(s1, a0, wr);
    vmbar<8>();
    TS(20);
    w_round<0, 2, 5>(s1, a0, wr);
    vmbar<8>();
    TS(21);
    const u32 hfl = hf ? WS_F + xcd * FRS + (g * 32 + (lane & 31)) * 128 : OOB;
    u32 pf; u32x2 bb0, bb1;
    ld32c(pf, hfl, rws, 0);
    asm volatile("buffer_load_dwordx2 %0, %2, %3, %4 offen\n\tbuffer_load_dwordx2 %1, %2, %3, %4 offen offset:16" : "=&v"(bb0), "=&v"(bb1) : "v"(hh * 8), "s"(srd(b1, 2752)), "s"(ft * 128 + g * 32) : "memory");
    w_round<0, 3, 6>(s1, a0, wr); w_issue<0>(s1, 7);
    TS(3);
    asm volatile("s_waitcnt vmcnt(8)" : "+v"(pf), "+v"(bb0), "+v"(bb1)::"memory");
    if (hf && __builtin_amdgcn_ballot_w64(pf != tag) != 0) poll(hfl, rws, tag);
    bar();
    TS(4);
    f32x16 c0, c1;
    wr_gemm<0>(wr, a0, DSrc{rws, (u32)lane * 16, WS_H + (dt1 * 2 + hf) * 131072 + g * 32768, s1.rot}, s2, 0, c0, c1);
    TS(5);
    const u32 R = L + 65536 + hf * 32768 + lane * 16;
    f32x4 lo = {blo(bb0[0]), bhi(bb0[0]), blo(bb0[1]), bhi(bb0[1])}, hi = {blo(bb1[0]), bhi(bb1[0]), blo(bb1[1]), bhi(bb1[1])};
#pragma unroll
    for (int q = 0; q < 4; ++q) {
      const f32x16& c = q < 2 ? c0 : c1; const int o = (q & 1) * 8;
      const f32x4 a = {c[o], c[o + 1], c[o + 2], c[o + 3]}, b = {c[o + 4], c[o + 5], c[o + 6], c[o + 7]};
      if (q == g) { lo += a; hi += b; }
      else { const u32 t = R + q * 8192 + ((g - q) & 3) * 2048; *(LAS f32x4*)t = a; *(LAS f32x4*)(t + 1024) = b; }
    }
    TS(16);
    vmbar<0>();
    TS(17);
#pragma unroll
    for (int k = 1; k < 4; ++k) { lo += ldsf4(R + g * 8192 + k * 2048); hi += ldsf4(R + g * 8192 + k * 2048 + 1024); }
    u32x4 uv;
    asm volatile("" : "+v"(lo), "+v"(hi));
#pragma unroll
    for (int i = 0; i < 4; ++i) { w_stage(a0, wr, GI * i, GI * i + GI); SB(); const f32x4& v = i < 2 ? lo : hi; uv[i] = pk(gelu(v[2 * (i & 1)]), gelu(v[2 * (i & 1) + 1])); SB(); }
    st128c(uv, lane * 16, rws, WS_U + (dt1 * 2 + hf) * UDB + (ft * 4 + g) * 1024);
    TS(6);
    w_stage(a0, wr, 4 * GI, 12);
    bar();
    TS(22);
    w_flag<3>(s2, w, tag, lane < 4 ? (lane * 2 + (dt1 >> 2)) * FRS : OOB, rws, WS_F + (256 + dt1 * 32 + ft) * 128);
  } else if (slot < 30) tag = attn<true>(x, wqkv, rws, s2, L, lane, w, xcd, slot, wa0, wr TSA);
  else tag = attn<false>(x, wqkv, rws, s2, L, lane, w, xcd, slot, wa0, wr TSA);

  {
    TS(7);
    asm volatile("" : "+s"(s2.lds), "+s"(s2.rot), "+s"(s2.sbase));
    const int sr = w * 8 + (lane >> 3), cq = lane & 7;
    u32 b2v;
    asm volatile("buffer_load_ushort %0, %1, %2, %3 offen" : "=v"(b2v) : "v"(sr * 2), "s"(srd(b2, 4096)), "s"(stl * 128) : "memory");
    const u32 GT = L + 131072, SG = GT + 256;
    const WA a0 = w_addr(wa0);
    f32x16 c0, c1;
    w_issue<1>(s2, 6); w_issue<1>(s2, 7);
    TS(24);
    vmbar0<17>(w);
    w_round<1, 3, 8>(s2, a0, wr);
    bar();
    TS(25);
    *(volatile LAS u32*)(SG + w * 4) = 0;
    TS(8);
    const int nf = g == 2 ? 6 : 8;
    poll_bar(WS_F + xcd * FRS + ((g < 3 ? 256 + dt * 32 + g * 8 : 512 + (stl >> 2) * 16) + (lane < nf ? lane : 0)) * 128, rws, tag);
    asm volatile("" : "+v"(b2v));
    TS(9);
    u32 gv;
    ld32c(gv, w == 0 ? lane * 4 : OOB, rws, WS_G + stl * 256);
    const u32 ub = WS_U + (dt * 2 + hf) * UDB;
    wr_gemm<1>(wr, a0, DSrc{srd(ws, g < 3 ? ub + 86 * 1024 : WS_END), (u32)lane * 16, g < 3 ? ub + g * 32768 : WS_O + (stl * 2 + hf) * 32768, s2.rot}, s2, SG + w * 4, c0, c1);
    asm volatile("" : "+v"(gv));
    TS(10);
    if (w == 0) *(LAS u32*)(GT + lane * 4) = gv;
    for (int it = 0; it < SPIN_MAX * 16 && *(volatile LAS u32*)(SG + (w ^ 1) * 4) == 0; ++it) {}
    const u32 pb = L + g * 8192 + hf * 32768;
    if (g < 3) {
      asm volatile("s_mov_b32 m0, %0\n\ts_nop 15\n\ts_nop 7" ::"s"(pb + hf * 128) : "memory");
      put16<0, 0>(c0);
      asm volatile("s_mov_b32 m0, %0\n\ts_nop 0" ::"s"(pb + hf * 128 + 6144) : "memory");
      put16<65536 - 6144, 0>(c1);
    } else red_write<RSL>(pb + lane * 16, 65536, c0, c1);
    bar();
    TS(11);
    const int sl = lane >> 3, i = (sl & 3) + 4 * (w & 3);
    const u32 ya = L + (cq >> 2) * (32768 + 128) + (w >> 2) * 65536 + (i >> 1) * 768 + (i & 1) * 272 + (sl >> 2) * 128 + (cq & 3) * 32;
    const f32x4 y0 = (ldsf4(ya) + ldsf4(ya + 8192)) + ldsf4(ya + 16384), y1 = (ldsf4(ya + 16) + ldsf4(ya + 8208)) + ldsf4(ya + 16400);
    const u32 aa = L + 24576 + (w >> 2) * 32768 + (cq >> 2) * 65536 + (cq & 3) * RSL + (sr & 31) * 16;
    const f32x4 a0v = ldsf4(aa), a1v = ldsf4(aa + 512);
    const float gt = ldsf(GT + sr * 4), bb = blo(b2v), og = 1.f - gt;
    u32x4 ov;
    ov[0] = pk(gt * a0v[0] + og * (y0[0] + bb), gt * a0v[1] + og * (y0[1] + bb));
    ov[1] = pk(gt * a0v[2] + og * (y0[2] + bb), gt * a0v[3] + og * (y0[3] + bb));
    ov[2] = pk(gt * a1v[0] + og * (y1[0] + bb), gt * a1v[1] + og * (y1[1] + bb));
    ov[3] = pk(gt * a1v[2] + og * (y1[2] + bb), gt * a1v[3] + og * (y1[3] + bb));
    *(u32x4*)(out + (size_t)(stl * 64 + sr) * 1024 + dt * 128 + cq * 16) = ov;
    TS(12); TS(26); TSEND;
  }
}

extern "C" void kernel_launch(void* const* d_in, const int* in_sizes, int n_in, void* d_out, int out_size, void* d_ws, size_t ws_size, hipStream_t stream) {
  fused_kernel<<<256, 512, 0, stream>>>((const char*)d_in[0], (const char*)d_in[1], (const char*)d_in[2], (const char*)d_in[4], (const char*)d_in[5],
                                        (const char*)d_in[6], (const char*)d_in[7], (const char*)d_in[8], (char*)d_out, (char*)d_ws);
}
```

```cpp
#include <hip/hip_runtime.h>
#include <hip/hip_bf16.h>

#pragma clang diagnostic ignored "-Wint-to-pointer-cast"
#define DI __device__ __forceinline__
#define LAS __attribute__((address_space(3)))
#define SB() __builtin_amdgcn_sched_barrier(0)
typedef unsigned u32;
typedef u32 u32x4 __attribute__((ext_vector_type(4)));
typedef u32 u32x2 __attribute__((ext_vector_type(2)));
typedef float f32x2 __attribute__((ext_vector_type(2)));
typedef float f32x4 __attribute__((ext_vector_type(4)));
typedef float f32x16 __attribute__((ext_vector_type(16)));
typedef __bf16 bf16x8 __attribute__((ext_vector_type(8)));
typedef __bf16 bf16x2 __attribute__((ext_vector_type(2)));
typedef short s16x4 __attribute__((ext_vector_type(4)));
typedef short s16x2 __attribute__((ext_vector_type(2)));

constexpr u32 WS_E = 0, WS_G = 32768;
constexpr u32 WS_F = 7u << 20, FRS = 641 * 128;
constexpr u32 WS_KV = 65536, WS_Z = WS_KV + 262144, WS_H = 1u << 20, WS_U = 3u << 20, WS_O = 5u << 20, WS_P = 8u << 20, WS_END = 9u << 20, OOB = 1u << 30;
constexpr u32 UDB = 88 * 1024;
constexpr int SPIN_MAX = 20000;
#define DSC "sc1"
#define R1 7
#define R2 3
#define RA 6
#define NF 1
#define NE 1
#define QP 16
#define GI 0
#define QPR 3
#define QN -1
#define PM 0
#define SPR 1
#define PP 2
#define SO 0
#define EP 0
#define KA 1
#define CW 1
#define NP1 8
#define NP2 8
#define SLP 4
#define MAP 0
#define AGAP {1, 3, 6, 9, 11, 14}
#define PRIO 3
#define WIN 10

#define TS(i)
#define TSP
#define TSA
#define TSK
#define TSBEG
#define TSEND

DI u32x4 srd(const void* p, u32 bytes) { const unsigned long long a = (unsigned long long)p; return u32x4{(u32)a, (u32)(a >> 32), bytes, 0x00020000u}; }
DI f32x16 mfma(u32x4 a, u32x4 b, f32x16 c) { return __builtin_amdgcn_mfma_f32_32x32x16_bf16(__builtin_bit_cast(bf16x8, a), __builtin_bit_cast(bf16x8, b), c, 0, 0, 0); }
DI f32x4 mfma16(u32x4 a, u32x4 b, f32x4 c) { return __builtin_amdgcn_mfma_f32_16x16x32_bf16(__builtin_bit_cast(bf16x8, a), __builtin_bit_cast(bf16x8, b), c, 0, 0, 0); }
DI u32x2 trd(u32 a) { return __builtin_bit_cast(u32x2, __builtin_amdgcn_ds_read_tr16_b64_v4i16((LAS s16x4*)a)); }
DI u32x4 trd2(u32 a, u32 b) { const u32x2 x = trd(a), y = trd(b); return u32x4{x[0], x[1], y[0], y[1]}; }
DI u32x4 lds128(u32 a) { return *(LAS u32x4*)a; }
DI f32x4 ldsf4(u32 a) { return *(LAS f32x4*)a; }
DI float ldsf(u32 a) { return *(LAS float*)a; }
DI u32 pk(float a, float b) { return __builtin_bit_cast(u32, __builtin_convertvector(f32x2{a, b}, bf16x2)); }
DI float blo(u32 v) { return __uint_as_float(v << 16); }
DI float bhi(u32 v) { return __uint_as_float(v & 0xffff0000u); }
DI u32x4 pack8(const f32x16& x, int o) { return u32x4{pk(x[o], x[o + 1]), pk(x[o + 2], x[o + 3]), pk(x[o + 4], x[o + 5]), pk(x[o + 6], x[o + 7])}; }
DI f32x16 splat(float v) { return f32x16{v, v, v, v, v, v, v, v, v, v, v, v, v, v, v, v}; }
DI float add1(float a, float b) { float r; asm("v_add_f32 %0, %1, %2" : "=v"(r) : "v"(a), "v"(b)); return r; }
DI f32x4 add4(f32x4 a, f32x4 b) { return f32x4{add1(a[0], b[0]), add1(a[1], b[1]), add1(a[2], b[2]), add1(a[3], b[3])}; }
DI u32 umin(u32 a, u32 b) { return a < b ? a : b; }
DI u32 umax(u32 a, u32 b) { return a > b ? a : b; }

DI void glds(u32 lds, u32 voff, u32x4 rs, u32 soff) {
  asm volatile("s_mov_b32 m0, %0\n\ts_nop 0\n\tbuffer_load_dwordx4 %1, %2, %3 offen lds" ::"s"(lds), "v"(voff), "s"(rs), "s"(soff) : "memory");
}
template <int OFF> DI void ld128c(u32x4& d, u32 voff, u32x4 rs, u32 soff) {
  asm volatile("buffer_load_dwordx4 %0, %1, %2, %3 offen offset:%4 " DSC : "=v"(d) : "v"(voff), "s"(rs), "s"(soff), "i"(OFF) : "memory");
}
DI void ld128(u32x4& d, u32 voff, u32x4 rs, u32 soff) { asm volatile("buffer_load_dwordx4 %0, %1, %2, %3 offen" : "=v"(d) : "v"(voff), "s"(rs), "s"(soff) : "memory"); }
template <int OFF = 0> DI void ld32c(u32& d, u32 voff, u32x4 rs, u32 soff) { asm volatile("buffer_load_dword %0, %1, %2, %3 offen offset:%4 sc1" : "=v"(d) : "v"(voff), "s"(rs), "s"(soff), "i"(OFF) : "memory"); }
DI void st128c(u32x4 v, u32 voff, u32x4 rs, u32 soff) { asm volatile("buffer_store_dwordx4 %0, %1, %2, %3 offen sc1\n\ts_nop 1" ::"v"(v), "v"(voff), "s"(rs), "s"(soff) : "memory"); }
DI void st32c(u32 v, u32 voff, u32x4 rs, u32 soff) { asm volatile("buffer_store_dword %0, %1, %2, %3 offen sc1" ::"v"(v), "v"(voff), "s"(rs), "s"(soff) : "memory"); }
template <int N> DI void vmwait() { asm volatile("s_waitcnt vmcnt(%0)" ::"i"(N) : "memory"); }
template <int N> DI void vmwait(u32x4& a) { asm volatile("s_waitcnt vmcnt(%1)" : "+v"(a) : "i"(N) : "memory"); }
DI void bar() { asm volatile("s_waitcnt lgkmcnt(0)\n\ts_barrier" ::: "memory"); }
template <int N> DI void vmbar() { asm volatile("s_waitcnt vmcnt(%0) lgkmcnt(0)\n\ts_barrier" ::"i"(N) : "memory"); }

DI void ev_load(u32& e, u32x4 ws) { asm volatile("buffer_load_dword %0, off, %1, 0" : "=v"(e) : "s"(ws) : "memory"); }
template <int N> DI u32 tag_wait(u32 e) {
  u32 t;
  asm volatile("s_waitcnt vmcnt(%2)\n\tv_readfirstlane_b32 %0, %1\n\ts_nop 1" : "=s"(t) : "v"(e), "i"(N) : "memory");
  return t * 1664525u + 1013904223u;
}
DI void poll(u32 voff, u32x4 ws, u32 tag) {
  for (int it = 0; it < SPIN_MAX; ++it) {
    u32 f;
    asm volatile("buffer_load_dword %0, %1, %2, 0 offen sc1\n\ts_waitcnt vmcnt(0)" : "=&v"(f) : "v"(voff), "s"(ws) : "memory");
    if (__builtin_amdgcn_ballot_w64(f != tag) == 0) break;
    __builtin_amdgcn_s_sleep(SLP);
  }
}
DI void poll_bar(u32 voff, u32x4 ws, u32 tag) {
  u32 f;
  asm volatile("buffer_load_dword %0, %1, %2, 0 offen sc1\n\ts_waitcnt vmcnt(0) lgkmcnt(0)\n\ts_barrier" : "=&v"(f) : "v"(voff), "s"(ws) : "memory");
  if (__builtin_amdgcn_ballot_w64(f != tag) != 0) poll(voff, ws, tag);
}
template <int C> DI float dppadd(float v) { return v + __int_as_float(__builtin_amdgcn_update_dpp(0, __float_as_int(v), C, 0xf, 0xf, true)); }
DI float rowsum(float v) { v = dppadd<0xB1>(v); v = dppadd<0x4E>(v); v = dppadd<0x141>(v); return dppadd<0x140>(v); }
DI float rdl(float v, int l) { return __int_as_float(__builtin_amdgcn_readlane(__float_as_int(v), l)); }
DI float fold32(float a, float b) { const auto r = __builtin_amdgcn_permlane32_swap(__float_as_uint(a), __float_as_uint(b), false, false); return __uint_as_float(r[0]) + __uint_as_float(r[1]); }
DI float fold16(float a, float b) { const auto r = __builtin_amdgcn_permlane16_swap(__float_as_uint(a), __float_as_uint(b), false, false); return __uint_as_float(r[0]) + __uint_as_float(r[1]); }
template <bool E> DI u32 phi2(float a, float b) {
  constexpr float l2e = 1.4426950408889634f, c = -l2e * (E ? 1.000001f : 1.f);
  float e0 = __builtin_amdgcn_fmed3f(__builtin_amdgcn_exp2f(__builtin_fmaf(a, l2e, c)), 0.f, 1.f), e1 = __builtin_amdgcn_fmed3f(__builtin_amdgcn_exp2f(__builtin_fmaf(b, l2e, c)), 0.f, 1.f);
  if (E) { e0 += 1e-6f; asm("" : "+v"(e0)); e1 += 1e-6f; }
  return pk(__builtin_amdgcn_fmed3f(a, e0, 3.4028234664e38f), __builtin_amdgcn_fmed3f(b, e1, 3.4028234664e38f));
}
template <bool E> DI u32x4 phi8(const f32x16& x, int o) { return u32x4{phi2<E>(x[o], x[o + 1]), phi2<E>(x[o + 2], x[o + 3]), phi2<E>(x[o + 4], x[o + 5]), phi2<E>(x[o + 6], x[o + 7])}; }
DI float gelu(float v) {
  const float t = v * (-2.3022081981f - 0.1029432397f * v * v);
  return v * __builtin_amdgcn_rcpf(1.f + __builtin_amdgcn_exp2f(t));
}

constexpr int wslot(int P, int c) { return (c + (P && c < 3 ? 4 : 1)) % 5; }
struct WSrc { u32x4 rs; u32 voff, sbase, stride, lds, rot; };
template <int P> DI void w_one(const WSrc& s, int c, int i) { glds(s.lds + wslot(P, c) * 32768 + i * 1024, s.voff, s.rs, s.sbase + (64 * ((c + s.rot) & 7) + 8 * i) * s.stride); }
template <int P> DI void w_issue(const WSrc& s, int c) {
#pragma unroll
  for (int i = 0; i < 4; ++i) w_one<P>(s, c, i);
}
struct WA { u32 a[3][2]; };
DI WA w_addr(u32 a0) { WA r;
#pragma unroll
  for (int i = 0; i < 3; ++i) { r.a[i][0] = a0 + i * 65536; r.a[i][1] = (a0 ^ 64) + i * 65536; asm volatile("" : "+v"(r.a[i][0]), "+v"(r.a[i][1])); }
  return r; }
template <int P> DI void w_frag(const WA& r, u32x4 (&f)[2], int c, int ks) {
#pragma unroll
  for (int b = 0; b < 2; ++b) { const u32 a = r.a[wslot(P, c) >> 1][b] + (wslot(P, c) & 1) * 32768 + ks * 2048; f[b] = trd2(a, a + 1024); }
}
template <int P, int C, int F> DI void w_round(const WSrc& s, const WA& a0, u32x4 (&wr)[16][2]) {
#pragma unroll
  for (int k = 0; k < 4; ++k) { w_frag<P>(a0, wr[4 * C + k], C, k); SB(); if constexpr (F < 8) w_one<P>(s, F, k); SB(); }
}
DI void w_stage(const WA& a0, u32x4 (&wr)[16][2], int k0, int k1) {
#pragma unroll
  for (int k = k0; k < k1; ++k) w_frag<1>(a0, wr[k], k >> 2, k & 3);
}
template <int C0> DI void w_flag(const WSrc& s, int w, u32 tag, u32 fv, u32x4 rws, u32 flag) {
  if constexpr (C0 < 6) w_issue<1>(s, C0);
  vmbar<(C0 < 6 ? 4 : 0)>();
  if (w == 0) st32c(tag, fv, rws, flag);
#pragma unroll
  for (int c = C0 + 1; c < 6; ++c) w_issue<1>(s, c);
}
template <int N> DI void vmbar0(int w) { if (w == 0) vmwait<N + 1>(); else vmwait<N>(); bar(); }
struct DSrc { u32x4 rs; u32 voff, sb, rot; };
template <int I> DI void d_issue(u32x4 (&d)[32], const DSrc& s) { ld128c<(I & 3) * 1024>(d[I], s.voff, s.rs, s.sb + (((I >> 2) + s.rot) & 7) * 4096); }
constexpr int KB = 32 - WIN;
template <int P, int K> DI void wr_steps(u32x4 (&d)[32], u32x4 (&wr)[16][2], const WA& a0, const DSrc& s, const WSrc& nx, u32 sig, bool late, f32x16& c0, f32x16& c1) {
  if constexpr (K < 32) {
    if constexpr (K == KB) {
      if constexpr (P == 0) bar();
      else { asm volatile("s_waitcnt lgkmcnt(0)" ::: "memory"); *(volatile LAS u32*)sig = 1; }
    }
    if constexpr (K % 8 == 0) __builtin_amdgcn_s_setprio(3 - K / 8);
    vmwait<(P == 0 || K < KB ? WIN - 1 : 31 - K)>(d[K]);
    c0 = mfma(wr[K & 15][0], d[K], c0); c1 = mfma(wr[K & 15][1], d[K], c1);
    asm volatile("" : "+v"(c0), "+v"(c1));
    if constexpr (K + WIN < 32) d_issue<K + WIN>(d, s);
    if constexpr (K == 1) { if (late) bar(); }
    if constexpr (P == 0 && K >= KB) w_one<1>(nx, (K - KB) >> 2, (K - KB) & 3);
    if constexpr (K >= 1 && K <= 16) w_frag<P>(a0, wr[K - 1], 4 + ((K - 1) >> 2), (K - 1) & 3);
    wr_steps<P, K + 1>(d, wr, a0, s, nx, sig, late, c0, c1);
  }
}
template <int I> DI void d_first(u32x4 (&d)[32], const DSrc& s) { if constexpr (I < WIN) { d_issue<I>(d, s); d_first<I + 1>(d, s); } }
template <int P> DI void wr_gemm(u32x4 (&wr)[16][2], const WA& a0, const DSrc& s, const WSrc& nx, u32 sig, bool late, f32x16& c0, f32x16& c1) {
  u32x4 d[32];
  d_first<0>(d, s);
  c0 = splat(0.f); c1 = splat(0.f);
  wr_steps<P, 0>(d, wr, a0, s, nx, sig, late, c0, c1);
  if constexpr (P == 0) {
#pragma unroll
    for (int i = WIN; i < 12; ++i) w_one<1>(nx, i >> 2, i & 3);
  }
}
constexpr u32 RSL = 1088;
template <u32 RS> DI void red_write(u32 a, u32 bs, const f32x16& c0, const f32x16& c1) {
#pragma unroll
  for (int q = 0; q < 4; ++q) {
    *(LAS f32x4*)(a + q * RS) = f32x4{c0[4 * q], c0[4 * q + 1], c0[4 * q + 2], c0[4 * q + 3]};
    *(LAS f32x4*)(a + bs + q * RS) = f32x4{c1[4 * q], c1[4 * q + 1], c1[4 * q + 2], c1[4 * q + 3]};
  }
}

template <int O, int I> DI void put16(const f32x16& c) {
  if constexpr (I < 16) { asm volatile("ds_write_addtid_b32 %0 offset:%1" ::"v"(c[I]), "i"(O + (I >> 1) * 768 + (I & 1) * 272) : "memory"); put16<O, I + 1>(c); }
}

constexpr u32 AST = 49152;
struct ASrc { u32x4 rx, rw; u32 vx0, vx1, vw, vq, sx, s0, s1, sq, rot, L; int w; };
DI void a_one(const ASrc& s, int c, int j) {
  const u32 st = s.L + (c % 3) * AST, cc = (c + s.rot) & 7;
  if (j < 4) glds(st + s.w * 4096 + j * 1024, (j & 1) ? s.vx1 : s.vx0, s.rx, s.sx + j * 8192 + cc * 128);
  else glds(st + 32768 + (j - 4) * 8192 + s.w * 1024, s.vw, s.rw, (j == 4 ? s.s0 : s.s1) + cc * (64 * 3072));
}
template <bool KV, int C, int J> DI void a_pref(const ASrc& s, const WSrc& nx) {
  if constexpr (J < 0) {}
  else if constexpr (C < 8) a_one(s, C, J);
  else if constexpr (C == 8) { if constexpr (KV && J < 4) glds(s.L + 98304 + s.w * 4096 + J * 1024, s.vq, s.rw, s.sq + J * (16 * 3072)); }
  else w_one<1>(nx, (6 * (C - 9) + J) >> 2, (6 * (C - 9) + J) & 3);
}
constexpr int a_gap(int C, int g) {
  const int t0[6] = {5, 6, 9, 10, 13, 14}, t1[6] = AGAP, t2[6] = {1, 2, -1, -1, -1, -1};
  for (int j = 0; j < 6; ++j) if ((C == 2 ? t0[j] : C == 10 ? t2[j] : t1[j]) == g) return j;
  return -1;
}
struct AMisc { u32x4 rws; u32 cfoff, tag, rdy, clr; int lane; };
struct AAd { u32 x0[4], x1[4], wa; };
template <bool KV, int T> DI void a_steps(const ASrc& s, const WSrc& nx, AAd& ad, u32x4 (&x0)[33], u32x4 (&f)[2][3], f32x16 (&c)[4], u32& cf, const AMisc& ms TSP) {
  if constexpr (T < 32) {
    constexpr int C = T >> 2, K = T & 3, P = T & 1, N = (T + 1) & 3;
    if constexpr (K == 3) {
      if constexpr (C < 6) vmbar<6 + ((KV && C == 1) || C == 3)>(); else if constexpr (C == 6) vmbar<KV ? 5 : 0>(); else vmbar<6>();
      if constexpr (C < 7) {
        constexpr u32 dl = (C + 1) % 3 ? AST : 0u - 2 * AST;
#pragma unroll
        for (int k = 0; k < 4; ++k) { ad.x0[k] += dl; ad.x1[k] += dl; }
        ad.wa += dl;
        asm volatile("" : "+v"(ad.x0[0]), "+v"(ad.x0[1]), "+v"(ad.x0[2]), "+v"(ad.x0[3]), "+v"(ad.x1[0]), "+v"(ad.x1[1]), "+v"(ad.x1[2]), "+v"(ad.x1[3]), "+v"(ad.wa));
      }
      if constexpr (C == 0 && KV) { if (s.w < 4) st128c(u32x4{~0u, ~0u, ~0u, ~0u}, ms.lane * 16, ms.rws, ms.clr); else st32c(~0u, (ms.lane & 31) * 4, ms.rws, ms.clr); }
      if constexpr (C == 2) { st32c(ms.tag, ms.rdy, ms.rws, 0); }
      if constexpr (C == 5 && KV) ld32c(cf, ms.cfoff, ms.rws, 0);
    }
    constexpr int PC = K == 3 ? C + 3 : C + 2, G0 = 4 * N;
    if (PRIO & 1) __builtin_amdgcn_s_setprio(3 - N);
    if constexpr (T < 31) { x0[T + 1] = lds128(ad.x0[N]); f[P ^ 1][1] = trd2(ad.wa + N * 2048, ad.wa + N * 2048 + 512); }
    SB();
    if constexpr (KV) c[0] = mfma(x0[T], f[P][1], c[0]); else c[0] = mfma(f[P][1], x0[T], c[0]);
    SB();
    if constexpr (T < 31) { f[P ^ 1][0] = lds128(ad.x1[N]); f[P ^ 1][2] = trd2(ad.wa + 8192 + N * 2048, ad.wa + 8192 + N * 2048 + 512); }
    a_pref<KV, PC, a_gap(PC, G0)>(s, nx);
    SB();
    if constexpr (KV) c[1] = mfma(f[P][0], f[P][1], c[1]); else c[1] = mfma(f[P][1], f[P][0], c[1]);
    SB();
    a_pref<KV, PC, a_gap(PC, G0 + 1)>(s, nx);
    SB();
    if constexpr (KV) c[2] = mfma(x0[T], f[P][2], c[2]); else c[2] = mfma(f[P][2], x0[T], c[2]);
    SB();
    a_pref<KV, PC, a_gap(PC, G0 + 2)>(s, nx);
    SB();
    if constexpr (KV) c[3] = mfma(f[P][0], f[P][2], c[3]); else c[3] = mfma(f[P][2], f[P][0], c[3]);
    SB();
    a_pref<KV, PC, a_gap(PC, G0 + 3)>(s, nx);
    SB();
    a_steps<KV, T + 1>(s, nx, ad, x0, f, c, cf, ms TSA);
  }
}

DI ASrc a_src(const char* x, const char* wqkv, u32 L, int lane, int w, int xcd, int slot) {
  const bool kv = slot < 30;
  const int ai = slot - 22, rc = (xcd & 6) + (kv ? ai >> 2 : slot - 30), hg = (xcd & 1) * 4 + (ai & 3), qj = xcd & 1;
  ASrc sa;
  sa.rx = srd(x, 2048 * 1024); sa.rw = srd(wqkv, 512 * 3072); sa.L = L; sa.w = w;
  sa.rot = xcd & RA;
  sa.vx0 = (lane >> 3) * 1024 + ((lane & 7) ^ (lane >> 4)) * 16; sa.vx1 = sa.vx0 ^ 64;
  sa.vw = (lane >> 3) * 3072 + ((lane & 7) ^ ((lane >> 2) & 4)) * 16;
  sa.vq = (lane >> 2) * 3072 + (lane & 3) * 16;
  sa.sx = (rc * 256 + w * 32) * 1024;
  sa.s0 = w * 8 * 3072 + (kv ? 1024 + hg * 128 : 512 + qj * 256);
  sa.s1 = sa.s0 + (kv ? 1024 : 128);
  sa.sq = ((w + sa.rot) & 7) * (64 * 3072) + hg * 64;
  return sa;
}
DI WSrc w1_src(const char* w1, u32 lds, int lane, int w, int xcd, int slot, int& ft, int& dt1) {
  const int gj = slot < 16 ? (xcd >> 1) * 16 + slot : 48 + (xcd >> 1) * 6 + slot;
  const u32 wch = (lane & 7) ^ ((lane >> 2) & 4);
  ft = gj >> 2; dt1 = (xcd & 1) * 4 + (gj & 3);
  WSrc s1;
  s1.rs = srd(w1, 2048 * 2752); s1.stride = 2752; s1.lds = lds;
  s1.voff = (lane >> 3) * 2752 + (ft == 21 ? (wch & 3) : wch) * 16;
  s1.sbase = ((w >> 1) * 512 + (w & 1) * 32) * 2752 + ft * 128;
  s1.rot = (2 * (dt1 + ft)) & R1;
  return s1;
}
template <bool KV> DI u32 attn(u32 ev, const char* x, const char* wqkv, u32x4 rws, const WSrc& s2, u32 L, int lane, int w, int xcd, int slot, u32 wa0, u32x4 (&wr)[16][2] TSP) {
  const int hh = lane >> 5, col = lane & 31, q4 = (lane & 15) >> 2, mi = w >> 1, ni = w & 1;
  const int ai = slot - 22, rc = (xcd & 6) + (KV ? ai >> 2 : slot - 30), hg = (xcd & 1) * 4 + (ai & 3), qj = xcd & 1;
  const ASrc sa = a_src(x, wqkv, L, lane, w, xcd, slot);
  AAd ad;
#pragma unroll
  for (int k = 0; k < 4; ++k) { ad.x0[k] = L + (mi * 64 + ni * 32 + col) * 128 + ((2 * k + hh) ^ ((col >> 1) & 7)) * 16; ad.x1[k] = ad.x0[k] ^ 4096; }
  ad.wa = L + 32768 + (8 * hh + q4) * 128 + 64 * ((q4 >> 1) ^ ni) + 32 * ((lane >> 4) & 1) + 8 * (lane & 3);
  const int pr = hg + (mi & 1) * 8;
  const bool ga = w < 4;
  const u32 cfoff = ga ? WS_F + xcd * FRS + (128 + (pr >> 1) * 8 + (lane & 7)) * 128 : OOB;
  asm volatile("" : "+v"(ad.x0[0]), "+v"(ad.x0[1]), "+v"(ad.x0[2]), "+v"(ad.x0[3]), "+v"(ad.x1[0]), "+v"(ad.x1[1]), "+v"(ad.x1[2]), "+v"(ad.x1[3]), "+v"(ad.wa)::"memory");
  const u32 tag = tag_wait<6>(ev);
  bar();
  TS(1);
  const AMisc ms = {rws, cfoff, tag, lane == 0 ? WS_F + w * FRS + (KV ? 128 + hg * 8 + rc : 192 + qj * 8 + rc) * 128 : OOB, w < 4 ? WS_KV + ((hg * 2 + (w >> 1)) * 8 + rc) * 2048 + (w & 1) * 1024 : WS_Z + ((hg * 2 + (w & 1)) * 8 + rc) * 128, lane};
  u32x4 x0[33], f[2][3]; f32x16 c[4]; u32 cf;
  c[0] = c[1] = splat(KV ? 1.f : 1.000001f); c[2] = c[3] = splat(KV ? 0.f : 1.000001f);
  x0[0] = lds128(ad.x0[0]); f[0][0] = lds128(ad.x1[0]); f[0][1] = trd2(ad.wa, ad.wa + 512); f[0][2] = trd2(ad.wa + 8192, ad.wa + 8192 + 512);
  a_steps<KV, 0>(sa, s2, ad, x0, f, c, cf, ms TSA);
  __builtin_amdgcn_s_setprio(0);
  TS(2);
  asm volatile("" : "+v"(c[0]), "+v"(c[1]), "+v"(c[2]), "+v"(c[3]));
  TS(3);
  const u32 pfl = WS_F + xcd * FRS + (512 + rc * 16 + 8 + qj) * 128;
  if constexpr (!KV) {
#pragma unroll
    for (int j = 0; j < 4; ++j)
#pragma unroll
      for (int h = 0; h < 2; ++h) { st128c(phi8<true>(c[j], 8 * h), lane * 16 + qj * 8192 + ni * 2048, rws, WS_P + (rc * 8 + (w ^ (j & 1))) * 16384 + ((j & 2) * 2 + h) * 1024); SB(); }
    TS(5);
    asm volatile("s_barrier" ::: "memory");
    w_issue<1>(s2, 2);
    vmbar<4>();
    if (w == 0) st32c(tag, lane == 0 ? 0 : OOB, rws, pfl);
    TS(4);
    if (blockIdx.x == 255 && w == 0) {
#pragma unroll 1
      for (int i = 0; i < 4; ++i) poll(WS_F + 7 * FRS + (i * 64 + lane) * 128, rws, tag);
      st32c(tag, lane == 0 ? 0 : OOB, rws, WS_E);
    }
    vmbar<0>();
    w_stage(w_addr(wa0), wr, 0, 12);
    bar();
    w_flag<3>(s2, w, tag, OOB, rws, 0);
    return tag;
  }
  const u32 S = L + 65536, KT = S + 14336, ZT = KT + 2048;
  if (w >= 4) w_issue<1>(s2, 2);
  const u32 on = ((lane >> 4) ^ (lane >> 2)) & 1 ? 0 : 0x3f803f80u;
  const u32x4 cs = {on, on, on, on};
  f32x16 d1 = splat(0.f); f32x4 d2 = {64e-6f, 64e-6f, 64e-6f, 64e-6f}, d3 = {0.f, 0.f, 0.f, 0.f};
  u32x4 p[4];
#pragma unroll
  for (int i = 0; i < 4; ++i) {
    const f32x16& k = c[i >> 1]; const int o = 8 * (i & 1);
    p[i][0] = phi2<false>(k[o], k[o + 1]); SB();
    if (i) d2 = mfma16(cs, p[i - 1], d2);
    const u32x4 v = pack8(c[2 + (i >> 1)], o);
    p[i][1] = phi2<false>(k[o + 2], k[o + 3]); SB();
    d3 = mfma16(cs, v, d3);
    p[i][2] = phi2<false>(k[o + 4], k[o + 5]); p[i][3] = phi2<false>(k[o + 6], k[o + 7]); SB();
    d1 = mfma(p[i], v, d1);
    SB(); if (w < 4) w_one<1>(s2, 2, i); SB();
  }
  d2 = mfma16(cs, p[3], d2);
  const bool lo = col < 16;
  f32x4 k0 = {lo ? d1[0] : d1[8], lo ? d1[1] : d1[9], lo ? d1[2] : d1[10], lo ? d1[3] : d1[11]};
  f32x4 k1 = {lo ? d1[4] : d1[12], lo ? d1[5] : d1[13], lo ? d1[6] : d1[14], lo ? d1[7] : d1[15]};
  const float ev6 = d3[0] * 1e-6f;
  k0 = add4(k0, f32x4{ev6, ev6, ev6, ev6}); k1 = add4(k1, f32x4{ev6, ev6, ev6, ev6});
  const float zp = d2[0];
  if (w >= 2) { *(LAS f32x4*)(S + (w - 2) * 2048 + lane * 16) = k0; *(LAS f32x4*)(S + (w - 2) * 2048 + 1024 + lane * 16) = k1; *(LAS float*)(S + 12288 + (w - 2) * 256 + lane * 4) = zp; }
  TS(13);
  vmbar<0>();
  TS(14);
  if (w < 2) {
    const u32 a = S + w * 2048 + lane * 16, z = S + 12288 + w * 256 + lane * 4;
    k0 = add4(add4(k0, ldsf4(a)), add4(ldsf4(a + 4096), ldsf4(a + 8192))); k1 = add4(add4(k1, ldsf4(a + 1024)), add4(ldsf4(a + 5120), ldsf4(a + 9216)));
    const float t = add1(add1(zp, ldsf(z)), add1(ldsf(z + 512), ldsf(z + 1024)));
    u32x4 p0 = __builtin_bit_cast(u32x4, k0), p1 = __builtin_bit_cast(u32x4, k1);
#pragma unroll
    for (int i = 0; i < 4; ++i) { p0[i] = umin(p0[i], ~1u); p1[i] = umin(p1[i], ~1u); }
    const u32 o = (hg * 2 + w) * 8 + rc;
    st128c(p0, lane * 16, rws, WS_KV + o * 2048); st128c(p1, lane * 16, rws, WS_KV + o * 2048 + 1024); st32c(umin(__float_as_uint(t), ~1u), col * 4, rws, WS_Z + o * 128);
  }
  TS(4);
  u32x4 pq[2][2], t[8]; u32x2 z; u32 pf;
  u32 qa = L + 98304 + (8 * hh + q4) * 64 + 32 * ((lane >> 4) & 1) + 8 * (lane & 3);
  asm volatile("" : "+v"(qa), "+v"(cf));
  u32x4 qf[4];
#pragma unroll
  for (int i = 0; i < 3; ++i) qf[i] = trd2(qa + i * 1024, qa + i * 1024 + 256);
  f32x16 cq = splat(1.000001f);
#pragma unroll
  for (int i = 0; i < 32; ++i) {
    SB();
    if (i < 29) qf[(i + 3) & 3] = trd2(qa + (i + 3) * 1024, qa + (i + 3) * 1024 + 256);
    if (QN >= 0) asm volatile("s_nop %0" ::"i"(QN < 0 ? 0 : QN));
    SB();
    cq = mfma(qf[i & 3], x0[i], cq);
  }
  SB();
  TS(18);
  auto sweep = [&]() {
#pragma unroll
    for (int r = 0; r < 8; ++r) ld128c<0>(t[r], lane * 16, rws, WS_KV + (pr * 8 + r) * 2048 + ni * 1024);
    asm volatile("buffer_load_dwordx2 %0, %1, %2, %3 offen sc1" : "=v"(z) : "v"((lane >> 3) * 128 + (lane & 7) * 8), "s"(rws), "s"(WS_Z + pr * 1024 + ni * 64) : "memory");
  };
  asm volatile("" : "=v"(t[0]), "=v"(t[1]), "=v"(t[2]), "=v"(t[3]), "=v"(t[4]), "=v"(t[5]), "=v"(t[6]), "=v"(t[7]), "=v"(z));
  if (ga && __builtin_amdgcn_ballot_w64(cf != tag) != 0) poll(cfoff, rws, tag);
  ld32c(pf, pfl, rws, 0);
  if (mi == 0) sweep();
  pq[0][0] = phi8<true>(cq, 0); pq[0][1] = phi8<true>(cq, 8);
  asm volatile("" : "+v"(pq[0][0]), "+v"(pq[0][1]));
  TS(5);
  w_stage(w_addr(wa0), wr, 0, 12);
  SB();
  if (mi == 1) sweep();
  if (!ga) vmwait<0>();
  asm volatile("s_waitcnt vmcnt(9)" : "+v"(pf)::"memory");
  if (__builtin_amdgcn_ballot_w64(pf != tag) != 0) poll(pfl, rws, tag);
  ld128c<0>(pq[1][0], lane * 16, rws, WS_P + (rc * 8 + w) * 16384 + hg * 2048); ld128c<1024>(pq[1][1], lane * 16, rws, WS_P + (rc * 8 + w) * 16384 + hg * 2048);
  TS(15);
#define GW(N) asm volatile("s_waitcnt vmcnt(%9)" : "+v"(t[0]), "+v"(t[1]), "+v"(t[2]), "+v"(t[3]), "+v"(t[4]), "+v"(t[5]), "+v"(t[6]), "+v"(t[7]), "+v"(z) : "i"(N) : "memory")
  GW(2);
  if (ga) {
    for (int it = 0; it < SPIN_MAX; ++it) {
      u32 m = umax(z[0], z[1]);
#pragma unroll
      for (int r = 0; r < 8; ++r) m = umax(umax(umax(m, t[r][0]), t[r][1]), umax(t[r][2], t[r][3]));
      if (__builtin_amdgcn_ballot_w64(m == ~0u) == 0) break;
      sweep();
      GW(0);
    }
    TS(16);
    f32x4 a = __builtin_bit_cast(f32x4, t[0]);
#pragma unroll
    for (int r = 1; r < 8; ++r) a += __builtin_bit_cast(f32x4, t[r]);
    float zs = fold32(__uint_as_float(z[0]), __uint_as_float(z[1]));
    zs = dppadd<0x128>(fold16(zs, zs));
    const int pl = mi, zf = 2 * (lane & 7) + hh;
    *(LAS u32x2*)(KT + pl * 1024 + lane * 16 + ni * 8) = u32x2{pk(a[0], a[1]), pk(a[2], a[3])};
    *(LAS unsigned short*)(ZT + pl * 64 + ni * 32 + ((zf >> 2) & 1) * 16 + ((zf & 3) + 4 * (zf >> 3)) * 2) = (unsigned short)pk(zs, zs);
  }
  bar();
  TS(6);
  f32x16 dd[2][2];
#pragma unroll
  for (int j = 0; j < 2; ++j) {
    if (j) asm volatile("s_waitcnt vmcnt(0)" : "+v"(pq[1][0]), "+v"(pq[1][1])::"memory");
#pragma unroll
    for (int h = 0; h < 2; ++h) dd[j][h] = mfma(lds128(((col < 16) == (h == 0)) ? KT + j * 1024 + lane * 16 : ZT + j * 64 + h * 32 + hh * 16), pq[j][h], splat(0.f));
  }
  SB();
#pragma unroll
  for (int c = 3; c < 3 + NE; ++c) w_issue<1>(s2, c);
  SB();
#pragma unroll
  for (int j = 0; j < 2; ++j)
#pragma unroll
    for (int h = 0; h < 2; ++h) {
      const int o = 8 * h;
      const f32x16& e = dd[j][h];
      const float rd = __builtin_amdgcn_rcpf(e[8 - o] + 1e-6f);
      const u32x4 ov = {pk(e[o] * rd, e[o + 1] * rd), pk(e[o + 2] * rd, e[o + 3] * rd), pk(e[o + 4] * rd, e[o + 5] * rd), pk(e[o + 6] * rd, e[o + 7] * rd)};
      st128c(ov, lane * 16, rws, WS_O + (rc * 8 + w) * 32768 + ((hg + 8 * j) * 2 + h) * 1024);
    }
  TS(17);
  bar();
  w_flag<3 + NE>(s2, w, tag, lane < 2 ? ((rc & 6) + lane) * FRS : OOB, rws, WS_F + (512 + rc * 16 + hg) * 128);
  return tag;
}

__global__ __launch_bounds__(512, 2) void fused_kernel(const char* __restrict__ x, const char* __restrict__ wqkv, const char* __restrict__ wo,
                                                        const char* __restrict__ w1, const char* __restrict__ b1, const char* __restrict__ w2,
                                                        const char* __restrict__ b2, const char* __restrict__ gw, char* __restrict__ out, char* ws TSK) {
  __shared__ __attribute__((aligned(1024))) char sm[163840];
  TSBEG; TS(0);
  asm volatile("" : "+s"(x), "+s"(wqkv), "+s"(wo), "+s"(w1), "+s"(b1), "+s"(w2), "+s"(b2), "+s"(gw), "+s"(out), "+s"(ws));
  const u32 L = (u32)(size_t)(LAS char*)sm;
  const int lane = threadIdx.x & 63, w = __builtin_amdgcn_readfirstlane(threadIdx.x >> 6);
  int xcd = blockIdx.x & 7, slot = blockIdx.x >> 3;
  const int hh = lane >> 5;
  const u32x4 rws = srd(ws, WS_END);
  u32 ev; u32x4 xr[2], gr;
  asm volatile("s_nop 4" : "=v"(xr[0]), "=v"(xr[1]), "=v"(gr)::"memory");
  ev_load(ev, rws);
  if (slot < 22) {
    int ft, dt1;
    const WSrc s1 = w1_src(w1, L + w * 4096, lane, w, xcd, slot, ft, dt1);
    if (slot < 16) {
      const int r = (xcd * 16 + slot) * 16 + w;
      const u32x4 rx = srd(x, 2048 * 1024);
      ld128(xr[0], lane * 16, rx, r * 1024); ld128(xr[1], lane * 16, rx, (r + 8) * 1024);
      ld128(gr, lane * 16, srd(gw, 1024), 0);
      asm volatile("s_barrier" ::: "memory");
    }
    w_issue<0>(s1, 0);
  } else {
    const ASrc sa = a_src(x, wqkv, L, lane, w, xcd, slot);
#pragma unroll
    for (int j = 0; j < 6; ++j) a_one(sa, 0, j);
    asm volatile("s_barrier" ::: "memory");
#pragma unroll
    for (int j = 0; j < 6; ++j) a_one(sa, 1, j);
  }
  asm volatile("" : "+s"(slot), "+s"(xcd));
  const int g = w >> 1, hf = w & 1, q4 = (lane & 15) >> 2;
  const u32 wch = (lane & 7) ^ ((lane >> 2) & 4);
  const u32 wa0 = L + g * 8192 + (4 * hh + q4) * 128 + 64 * (q4 >> 1) + 32 * ((lane >> 4) & 1) + 8 * (lane & 3);

  const int dt = (xcd & 1) * 4 + (slot & 3), stl = (xcd >> 1) * 8 + (slot >> 2);
  WSrc s2;
  s2.rs = g < 3 ? srd(w2, 1376 * 4096) : srd(wo, 512 * 1024); s2.stride = g < 3 ? 4096 : 1024; s2.lds = L + g * 8192 + hf * 4096;
  s2.voff = (lane >> 3) * s2.stride + wch * 16;
  s2.sbase = g < 3 ? (g * 512 + hf * 32) * 4096 + stl * 128 : hf * 32 * 1024 + dt * 128;
  s2.rot = 2 * ((slot + (slot >> 2)) & R2);
  u32 tag;
  u32x4 wr[16][2];

  if (slot >= 30) tag = attn<false>(ev, x, wqkv, rws, s2, L, lane, w, xcd, slot, wa0, wr TSA);
  else if (slot >= 22) tag = attn<true>(ev, x, wqkv, rws, s2, L, lane, w, xcd, slot, wa0, wr TSA);
  else {
    const int gi = slot < 16 ? xcd * 16 + slot : 112 + xcd * 6 + slot;
    int ft, dt1;
    const WSrc s1 = w1_src(w1, s2.lds, lane, w, xcd, slot, ft, dt1);
    if (slot < 16) {
      const int sb = xcd * 16 + slot;
      tag = tag_wait<4>(ev);
      asm volatile("" : "+v"(xr[0]), "+v"(xr[1]), "+v"(gr)::"memory");
      TS(1);
      f32x2 v[2][4], t; float s[2], gd[2], q[2];
#pragma unroll
      for (int j = 0; j < 2; ++j) {
#pragma unroll
        for (int i = 0; i < 4; ++i) v[j][i] = f32x2{blo(xr[j][i]), bhi(xr[j][i])};
        t = (v[j][0] + v[j][1]) + (v[j][2] + v[j][3]); s[j] = t[0] + t[1];
      }
      const float t0 = fold32(s[0], s[1]), t1 = rowsum(fold16(t0, t0)) * (1.f / 512.f);
#pragma unroll
      for (int j = 0; j < 2; ++j) {
        t = f32x2{0.f, 0.f};
#pragma unroll
        for (int i = 0; i < 4; ++i) t = __builtin_elementwise_fma(v[j][i], f32x2{blo(gr[i]), bhi(gr[i])}, t);
        gd[j] = t[0] + t[1];
        const float m = rdl(t1, 32 * j);
        t = f32x2{0.f, 0.f};
#pragma unroll
        for (int i = 0; i < 4; ++i) { v[j][i] -= m; t = __builtin_elementwise_fma(v[j][i], v[j][i], t); }
        q[j] = t[0] + t[1];
      }
      const float t2 = rowsum(fold16(fold32(q[0], q[1]), fold32(gd[0], gd[1])));
      const float rs = __builtin_amdgcn_rsqf(t2 * (1.f / 512.f) + 1e-5f);
#pragma unroll
      for (int j = 0; j < 2; ++j) {
        const float r = rdl(rs, 32 * j);
#pragma unroll
        for (int i = 0; i < 4; ++i) v[j][i] *= r;
        *(LAS u32x4*)(L + (w + 8 * j) * 1088 + lane * 16) = u32x4{pk(v[j][0][0], v[j][0][1]), pk(v[j][1][0], v[j][1][1]), pk(v[j][2][0], v[j][2][1]), pk(v[j][3][0], v[j][3][1])};
      }
      const float gt = __builtin_amdgcn_rcpf(1.f + __builtin_amdgcn_exp2f(t2 * -1.4426950408889634f));
      TS(13);
      bar();
      const u32 ta = L + (4 * hh + q4) * 1088 + w * 128 + 32 * ((lane >> 4) & 1) + 8 * (lane & 3);
      const u32x4 h0 = trd2(ta, ta + 8 * 1088), h1 = trd2(ta + 64, ta + 64 + 8 * 1088);
      st32c(__float_as_uint(gt), (lane & 31) == 16 ? lane & 32 : OOB, rws, WS_G + (sb * 16 + w) * 4);
      st128c(h0, lane * 16, rws, WS_H + w * 262144 + sb * 1024);
      st128c(h1, lane * 16, rws, WS_H + w * 262144 + 131072 + sb * 1024);
      asm volatile("s_barrier" ::: "memory");
      TS(14);
      w_issue<0>(s1, 1);
      TS(15);
      vmbar<4>();
    } else {
      u32 cw; unsigned long long pc; u32 cn;
      asm volatile("s_getpc_b64 %0\n.Lfk_pc:\n\ts_mov_b32 %1, .Lfk_end-.Lfk_pc" : "=s"(pc), "=s"(cn));
      asm volatile("buffer_load_dword %0, %1, %2, 0 offen" : "=v"(cw) : "v"(w == 0 ? ((slot - 16) * 64 + lane) * 128 : OOB), "s"(srd((const void*)pc, cn)) : "memory");
      w_issue<0>(s1, 1);
      tag = tag_wait<4>(ev);
      asm volatile("" ::"v"(cw));
      bar();
    }
    TS(2);
    const WA a0 = w_addr(wa0);
    if (w == 0) st32c(tag, lane < 8 ? lane * FRS : OOB, rws, WS_F + (slot < 16 ? gi : gi + 80) * 128);
    w_round<0, 0, 2>(s1, a0, wr); w_issue<0>(s1, 3);
    TS(18);
    vmbar0<8>(w);
    TS(19);
    w_round<0, 1, 4>(s1, a0, wr);
    vmbar<8>();
    TS(20);
    w_round<0, 2, 5>(s1, a0, wr);
    vmbar<8>();
    TS(21);
    const u32 hfl = hf ? WS_F + xcd * FRS + (g * 32 + (lane & 31)) * 128 : OOB;
    u32 pf; u32x2 bb0, bb1;
    ld32c(pf, hfl, rws, 0);
    asm volatile("buffer_load_dwordx2 %0, %2, %3, %4 offen\n\tbuffer_load_dwordx2 %1, %2, %3, %4 offen offset:16" : "=&v"(bb0), "=&v"(bb1) : "v"(hh * 8), "s"(srd(b1, 2752)), "s"(ft * 128 + g * 32) : "memory");
    w_round<0, 3, 6>(s1, a0, wr); w_issue<0>(s1, 7);
    TS(3);
    asm volatile("s_waitcnt vmcnt(8)" : "+v"(pf), "+v"(bb0), "+v"(bb1)::"memory");
    if (hf && __builtin_amdgcn_ballot_w64(pf != tag) != 0) poll(hfl, rws, tag);
    bar();
    TS(4);
    f32x16 c0, c1;
    wr_gemm<0>(wr, a0, DSrc{rws, (u32)lane * 16, WS_H + (dt1 * 2 + hf) * 131072 + g * 32768, s1.rot}, s2, 0, true, c0, c1);
    TS(5);
    const u32 R = L + 65536 + hf * 32768 + lane * 16;
    f32x4 lo = {blo(bb0[0]), bhi(bb0[0]), blo(bb0[1]), bhi(bb0[1])}, hi = {blo(bb1[0]), bhi(bb1[0]), blo(bb1[1]), bhi(bb1[1])};
#pragma unroll
    for (int q = 0; q < 4; ++q) {
      const f32x16& c = q < 2 ? c0 : c1; const int o = (q & 1) * 8;
      const f32x4 a = {c[o], c[o + 1], c[o + 2], c[o + 3]}, b = {c[o + 4], c[o + 5], c[o + 6], c[o + 7]};
      if (q == g) { lo += a; hi += b; }
      else { const u32 t = R + q * 8192 + ((g - q) & 3) * 2048; *(LAS f32x4*)t = a; *(LAS f32x4*)(t + 1024) = b; }
    }
    TS(16);
    vmbar<0>();
    TS(17);
    if (SO && w >= 4) { w_stage(a0, wr, 0, 12); SB(); }
#pragma unroll
    for (int k = 1; k < 4; ++k) { lo += ldsf4(R + g * 8192 + k * 2048); hi += ldsf4(R + g * 8192 + k * 2048 + 1024); }
    const u32x4 uv = {pk(gelu(lo[0]), gelu(lo[1])), pk(gelu(lo[2]), gelu(lo[3])), pk(gelu(hi[0]), gelu(hi[1])), pk(gelu(hi[2]), gelu(hi[3]))};
    st128c(uv, lane * 16, rws, WS_U + (dt1 * 2 + hf) * UDB + (ft * 4 + g) * 1024);
    TS(6);
    if (!SO || w < 4) w_stage(a0, wr, 0, 12);
    bar();
    TS(22);
    w_flag<3>(s2, w, tag, lane < 4 ? (lane * 2 + (dt1 >> 2)) * FRS : OOB, rws, WS_F + (256 + dt1 * 32 + ft) * 128);
  }

  {
    TS(7);
    asm volatile("" : "+s"(s2.lds), "+s"(s2.rot), "+s"(s2.sbase));
    const int sr = w * 8 + (lane >> 3), cq = lane & 7;
    u32 b2v;
    asm volatile("buffer_load_ushort %0, %1, %2, %3 offen" : "=v"(b2v) : "v"(sr * 2), "s"(srd(b2, 4096)), "s"(stl * 128) : "memory");
    const u32 GT = L + 131072, SG = GT + 256;
    const WA a0 = w_addr(wa0);
    f32x16 c0, c1;
    const int nf = g == 2 ? 6 : 8;
    const u32 fo = WS_F + xcd * FRS + ((g < 3 ? 256 + dt * 32 + g * 8 : 512 + (stl >> 2) * 16) + (lane < nf ? lane : 0)) * 128;
    const bool early = EP && slot >= 22 && slot < 30 && (EP == 2 || g < 3);
    u32 pf;
    if (PP == 2) ld32c(pf, early ? fo : OOB, rws, 0);
    w_issue<1>(s2, 6);
    if (PP == 1) ld32c(pf, early ? fo : OOB, rws, 0);
    w_issue<1>(s2, 7);
    TS(24);
    vmbar0<18>(w);
    w_round<1, 3, 8>(s2, a0, wr);
    bar();
    TS(25);
    *(volatile LAS u32*)(SG + w * 4) = 0;
    TS(8);
    asm volatile("s_waitcnt vmcnt(%2)" : "+v"(pf), "+v"(b2v) : "i"(4 * PP) : "memory");
    const bool ok = early && __builtin_amdgcn_ballot_w64(pf != tag) == 0;
    if (!ok) poll_bar(fo, rws, tag);
    TS(9);
    u32 gv;
    ld32c(gv, w == 0 ? lane * 4 : OOB, rws, WS_G + stl * 256);
    const u32 ub = WS_U + (dt * 2 + hf) * UDB;
    wr_gemm<1>(wr, a0, DSrc{srd(ws, g < 3 ? ub + 86 * 1024 : WS_END), (u32)lane * 16, g < 3 ? ub + g * 32768 : WS_O + (stl * 2 + hf) * 32768, s2.rot}, s2, SG + w * 4, ok, c0, c1);
    asm volatile("" : "+v"(gv));
    TS(10);
    if (w == 0) *(LAS u32*)(GT + lane * 4) = gv;
    for (int it = 0; it < SPIN_MAX * 16 && *(volatile LAS u32*)(SG + (w ^ 1) * 4) == 0; ++it) {}
    const u32 pb = L + g * 8192 + hf * 32768;
    if (g < 3) {
      asm volatile("s_mov_b32 m0, %0\n\ts_nop 15\n\ts_nop 7" ::"s"(pb + hf * 128) : "memory");
      put16<0, 0>(c0);
      asm volatile("s_mov_b32 m0, %0\n\ts_nop 0" ::"s"(pb + hf * 128 + 6144) : "memory");
      put16<65536 - 6144, 0>(c1);
    } else red_write<RSL>(pb + lane * 16, 65536, c0, c1);
    bar();
    TS(11);
    const int sl = lane >> 3, i = (sl & 3) + 4 * (w & 3);
    const u32 ya = L + (cq >> 2) * (32768 + 128) + (w >> 2) * 65536 + (i >> 1) * 768 + (i & 1) * 272 + (sl >> 2) * 128 + (cq & 3) * 32;
    const f32x4 y0 = (ldsf4(ya) + ldsf4(ya + 8192)) + ldsf4(ya + 16384), y1 = (ldsf4(ya + 16) + ldsf4(ya + 8208)) + ldsf4(ya + 16400);
    const u32 aa = L + 24576 + (w >> 2) * 32768 + (cq >> 2) * 65536 + (cq & 3) * RSL + (sr & 31) * 16;
    const f32x4 a0v = ldsf4(aa), a1v = ldsf4(aa + 512);
    const float gt = ldsf(GT + sr * 4), bb = blo(b2v), og = 1.f - gt;
    u32x4 ov;
    ov[0] = pk(gt * a0v[0] + og * (y0[0] + bb), gt * a0v[1] + og * (y0[1] + bb));
    ov[1] = pk(gt * a0v[2] + og * (y0[2] + bb), gt * a0v[3] + og * (y0[3] + bb));
    ov[2] = pk(gt * a1v[0] + og * (y1[0] + bb), gt * a1v[1] + og * (y1[1] + bb));
    ov[3] = pk(gt * a1v[2] + og * (y1[2] + bb), gt * a1v[3] + og * (y1[3] + bb));
    *(__attribute__((address_space(1))) u32x4*)(out + (size_t)(stl * 64 + sr) * 1024 + dt * 128 + cq * 16) = ov;
    TS(12); TS(26); TSEND;
    asm volatile(".Lfk_end:");
  }
}

extern "C" void kernel_launch(void* const* d_in, const int* in_sizes, int n_in, void* d_out, int out_size, void* d_ws, size_t ws_size, hipStream_t stream) {
  fused_kernel<<<256, 512, 0, stream>>>((const char*)d_in[0], (const char*)d_in[1], (const char*)d_in[2], (const char*)d_in[4], (const char*)d_in[5],
                                        (const char*)d_in[6], (const char*)d_in[7], (const char*)d_in[8], (char*)d_out, (char*)d_ws);
}
```

```cpp
#include <hip/hip_runtime.h>
#include <hip/hip_bf16.h>

#pragma clang diagnostic ignored "-Wint-to-pointer-cast"
#define DI __device__ __forceinline__
#define LAS __attribute__((address_space(3)))
#define SB() __builtin_amdgcn_sched_barrier(0)
typedef unsigned u32;
typedef u32 u32x4 __attribute__((ext_vector_type(4)));
typedef u32 u32x2 __attribute__((ext_vector_type(2)));
typedef float f32x2 __attribute__((ext_vector_type(2)));
typedef float f32x4 __attribute__((ext_vector_type(4)));
typedef float f32x16 __attribute__((ext_vector_type(16)));
typedef __bf16 bf16x8 __attribute__((ext_vector_type(8)));
typedef __bf16 bf16x2 __attribute__((ext_vector_type(2)));
typedef short s16x4 __attribute__((ext_vector_type(4)));
typedef short s16x2 __attribute__((ext_vector_type(2)));

constexpr u32 WS_E = 0, WS_G = 32768;
constexpr u32 WS_F = 7u << 20, FRS = 641 * 128;
constexpr u32 WS_KV = 65536, WS_Z = WS_KV + 262144, WS_H = 1u << 20, WS_U = 3u << 20, WS_O = 5u << 20, WS_P = 8u << 20, WS_END = 9u << 20, OOB = 1u << 30;
constexpr u32 UDB = 88 * 1024;
constexpr int SPIN_MAX = 20000;
#define DSC "sc1"
#define R1 7
#define R2 3
#define RA 6
#define NF 1
#define NE 1
#define QP 16
#define GI 0
#define QPR 3
#define QN -1
#define PM 0
#define SPR 1
#define PP 2
#define SO 0
#define EP 0
#define KA 1
#define CW 1
#define TL 18
#define NP1 8
#define NP2 8
#define SLP 4
#define MAP 0
#define AGAP {1, 3, 6, 9, 11, 14}
#define PRIO 3
#define WIN 10

#define TS(i)
#define TSP
#define TSA
#define TSK
#define TSBEG
#define TSEND

DI u32x4 srd(const void* p, u32 bytes) { const unsigned long long a = (unsigned long long)p; return u32x4{(u32)a, (u32)(a >> 32), bytes, 0x00020000u}; }
DI f32x16 mfma(u32x4 a, u32x4 b, f32x16 c) { return __builtin_amdgcn_mfma_f32_32x32x16_bf16(__builtin_bit_cast(bf16x8, a), __builtin_bit_cast(bf16x8, b), c, 0, 0, 0); }
DI f32x4 mfma16(u32x4 a, u32x4 b, f32x4 c) { return __builtin_amdgcn_mfma_f32_16x16x32_bf16(__builtin_bit_cast(bf16x8, a), __builtin_bit_cast(bf16x8, b), c, 0, 0, 0); }
DI u32x2 trd(u32 a) { return __builtin_bit_cast(u32x2, __builtin_amdgcn_ds_read_tr16_b64_v4i16((LAS s16x4*)a)); }
DI u32x4 trd2(u32 a, u32 b) { const u32x2 x = trd(a), y = trd(b); return u32x4{x[0], x[1], y[0], y[1]}; }
DI u32x4 lds128(u32 a) { return *(LAS u32x4*)a; }
DI f32x4 ldsf4(u32 a) { return *(LAS f32x4*)a; }
DI float ldsf(u32 a) { return *(LAS float*)a; }
DI u32 pk(float a, float b) { return __builtin_bit_cast(u32, __builtin_convertvector(f32x2{a, b}, bf16x2)); }
DI float blo(u32 v) { return __uint_as_float(v << 16); }
DI float bhi(u32 v) { return __uint_as_float(v & 0xffff0000u); }
DI u32x4 pack8(const f32x16& x, int o) { return u32x4{pk(x[o], x[o + 1]), pk(x[o + 2], x[o + 3]), pk(x[o + 4], x[o + 5]), pk(x[o + 6], x[o + 7])}; }
DI f32x16 splat(float v) { return f32x16{v, v, v, v, v, v, v, v, v, v, v, v, v, v, v, v}; }
DI float add1(float a, float b) { float r; asm("v_add_f32 %0, %1, %2" : "=v"(r) : "v"(a), "v"(b)); return r; }
DI f32x4 add4(f32x4 a, f32x4 b) { return f32x4{add1(a[0], b[0]), add1(a[1], b[1]), add1(a[2], b[2]), add1(a[3], b[3])}; }
DI u32 umin(u32 a, u32 b) { return a < b ? a : b; }
DI u32 umax(u32 a, u32 b) { return a > b ? a : b; }

DI void glds(u32 lds, u32 voff, u32x4 rs, u32 soff) {
  asm volatile("s_mov_b32 m0, %0\n\ts_nop 0\n\tbuffer_load_dwordx4 %1, %2, %3 offen lds" ::"s"(lds), "v"(voff), "s"(rs), "s"(soff) : "memory");
}
template <int OFF> DI void ld128c(u32x4& d, u32 voff, u32x4 rs, u32 soff) {
  asm volatile("buffer_load_dwordx4 %0, %1, %2, %3 offen offset:%4 " DSC : "=v"(d) : "v"(voff), "s"(rs), "s"(soff), "i"(OFF) : "memory");
}
DI void ld128(u32x4& d, u32 voff, u32x4 rs, u32 soff) { asm volatile("buffer_load_dwordx4 %0, %1, %2, %3 offen" : "=v"(d) : "v"(voff), "s"(rs), "s"(soff) : "memory"); }
template <int OFF = 0> DI void ld32c(u32& d, u32 voff, u32x4 rs, u32 soff) { asm volatile("buffer_load_dword %0, %1, %2, %3 offen offset:%4 sc1" : "=v"(d) : "v"(voff), "s"(rs), "s"(soff), "i"(OFF) : "memory"); }
DI void st128c(u32x4 v, u32 voff, u32x4 rs, u32 soff) { asm volatile("buffer_store_dwordx4 %0, %1, %2, %3 offen sc1\n\ts_nop 1" ::"v"(v), "v"(voff), "s"(rs), "s"(soff) : "memory"); }
DI void st32c(u32 v, u32 voff, u32x4 rs, u32 soff) { asm volatile("buffer_store_dword %0, %1, %2, %3 offen sc1" ::"v"(v), "v"(voff), "s"(rs), "s"(soff) : "memory"); }
template <int N> DI void vmwait() { asm volatile("s_waitcnt vmcnt(%0)" ::"i"(N) : "memory"); }
template <int N> DI void vmwait(u32x4& a) { asm volatile("s_waitcnt vmcnt(%1)" : "+v"(a) : "i"(N) : "memory"); }
DI void bar() { asm volatile("s_waitcnt lgkmcnt(0)\n\ts_barrier" ::: "memory"); }
template <int N> DI void vmbar() { asm volatile("s_waitcnt vmcnt(%0) lgkmcnt(0)\n\ts_barrier" ::"i"(N) : "memory"); }

DI void ev_load(u32& e, u32x4 ws) { asm volatile("buffer_load_dword %0, off, %1, 0" : "+v"(e) : "s"(ws) : "memory"); }
template <int N> DI u32 tag_wait(u32 e) {
  u32 t;
  asm volatile("s_waitcnt vmcnt(%2)\n\tv_readfirstlane_b32 %0, %1\n\ts_nop 1" : "=s"(t) : "v"(e), "i"(N) : "memory");
  return t * 1664525u + 1013904223u;
}
DI void poll(u32 voff, u32x4 ws, u32 tag) {
  for (int it = 0; it < SPIN_MAX; ++it) {
    u32 f;
    asm volatile("buffer_load_dword %0, %1, %2, 0 offen sc1\n\ts_waitcnt vmcnt(0)" : "=&v"(f) : "v"(voff), "s"(ws) : "memory");
    if (__builtin_amdgcn_ballot_w64(f != tag) == 0) break;
    __builtin_amdgcn_s_sleep(SLP);
  }
}
DI void poll_bar(u32 voff, u32x4 ws, u32 tag) {
  u32 f;
  asm volatile("buffer_load_dword %0, %1, %2, 0 offen sc1\n\ts_waitcnt vmcnt(0) lgkmcnt(0)\n\ts_barrier" : "=&v"(f) : "v"(voff), "s"(ws) : "memory");
  if (__builtin_amdgcn_ballot_w64(f != tag) != 0) poll(voff, ws, tag);
}
template <int C> DI float dppadd(float v) { return v + __int_as_float(__builtin_amdgcn_update_dpp(0, __float_as_int(v), C, 0xf, 0xf, true)); }
DI float rowsum(float v) { v = dppadd<0xB1>(v); v = dppadd<0x4E>(v); v = dppadd<0x141>(v); return dppadd<0x140>(v); }
DI float rdl(float v, int l) { return __int_as_float(__builtin_amdgcn_readlane(__float_as_int(v), l)); }
DI float fold32(float a, float b) { const auto r = __builtin_amdgcn_permlane32_swap(__float_as_uint(a), __float_as_uint(b), false, false); return __uint_as_float(r[0]) + __uint_as_float(r[1]); }
DI float fold16(float a, float b) { const auto r = __builtin_amdgcn_permlane16_swap(__float_as_uint(a), __float_as_uint(b), false, false); return __uint_as_float(r[0]) + __uint_as_float(r[1]); }
template <bool E> DI u32 phi2(float a, float b) {
  constexpr float l2e = 1.4426950408889634f, c = -l2e * (E ? 1.000001f : 1.f);
  float e0 = __builtin_amdgcn_fmed3f(__builtin_amdgcn_exp2f(__builtin_fmaf(a, l2e, c)), 0.f, 1.f), e1 = __builtin_amdgcn_fmed3f(__builtin_amdgcn_exp2f(__builtin_fmaf(b, l2e, c)), 0.f, 1.f);
  if (E) { e0 += 1e-6f; asm("" : "+v"(e0)); e1 += 1e-6f; }
  return pk(__builtin_amdgcn_fmed3f(a, e0, 3.4028234664e38f), __builtin_amdgcn_fmed3f(b, e1, 3.4028234664e38f));
}
template <bool E> DI u32x4 phi8(const f32x16& x, int o) { return u32x4{phi2<E>(x[o], x[o + 1]), phi2<E>(x[o + 2], x[o + 3]), phi2<E>(x[o + 4], x[o + 5]), phi2<E>(x[o + 6], x[o + 7])}; }
DI float gelu(float v) {
  const float t = v * (-2.3022081981f - 0.1029432397f * v * v);
  return v * __builtin_amdgcn_rcpf(1.f + __builtin_amdgcn_exp2f(t));
}

constexpr int wslot(int P, int c) { return (c + (P && c < 3 ? 4 : 1)) % 5; }
struct WSrc { u32x4 rs; u32 voff, sbase, stride, lds, rot; };
template <int P> DI void w_one(const WSrc& s, int c, int i) { glds(s.lds + wslot(P, c) * 32768 + i * 1024, s.voff, s.rs, s.sbase + (64 * ((c + s.rot) & 7) + 8 * i) * s.stride); }
template <int P> DI void w_issue(const WSrc& s, int c) {
#pragma unroll
  for (int i = 0; i < 4; ++i) w_one<P>(s, c, i);
}
struct WA { u32 a[3][2]; };
DI WA w_addr(u32 a0) { WA r;
#pragma unroll
  for (int i = 0; i < 3; ++i) { r.a[i][0] = a0 + i * 65536; r.a[i][1] = (a0 ^ 64) + i * 65536; asm volatile("" : "+v"(r.a[i][0]), "+v"(r.a[i][1])); }
  return r; }
template <int P> DI void w_frag(const WA& r, u32x4 (&f)[2], int c, int ks) {
#pragma unroll
  for (int b = 0; b < 2; ++b) { const u32 a = r.a[wslot(P, c) >> 1][b] + (wslot(P, c) & 1) * 32768 + ks * 2048; f[b] = trd2(a, a + 1024); }
}
template <int P, int C, int F> DI void w_round(const WSrc& s, const WA& a0, u32x4 (&wr)[16][2]) {
#pragma unroll
  for (int k = 0; k < 4; ++k) { w_frag<P>(a0, wr[4 * C + k], C, k); SB(); if constexpr (F < 8) w_one<P>(s, F, k); SB(); }
}
DI void w_stage(const WA& a0, u32x4 (&wr)[16][2], int k0, int k1) {
#pragma unroll
  for (int k = k0; k < k1; ++k) w_frag<1>(a0, wr[k], k >> 2, k & 3);
}
template <int C0> DI void w_flag(const WSrc& s, int w, u32 tag, u32 fv, u32x4 rws, u32 flag) {
  if constexpr (C0 < 6) w_issue<1>(s, C0);
  vmbar<(C0 < 6 ? 4 : 0)>();
  if (w == 0) st32c(tag, fv, rws, flag);
#pragma unroll
  for (int c = C0 + 1; c < 6; ++c) w_issue<1>(s, c);
}
template <int N> DI void vmbar0(int w) { if (w == 0) vmwait<N + 1>(); else vmwait<N>(); bar(); }
struct DSrc { u32x4 rs; u32 voff, sb, rot; };
template <int I> DI void d_issue(u32x4 (&d)[32], const DSrc& s) { ld128c<(I & 3) * 1024>(d[I], s.voff, s.rs, s.sb + (((I >> 2) + s.rot) & 7) * 4096); }
constexpr int KB = 32 - WIN;
template <int P, int K> DI void wr_steps(u32x4 (&d)[32], u32x4 (&wr)[16][2], const WA& a0, const DSrc& s, const WSrc& nx, u32 sig, bool late, f32x16& c0, f32x16& c1) {
  if constexpr (K < 32) {
    if constexpr (K == KB) {
      if constexpr (P == 0) bar();
      else { asm volatile("s_waitcnt lgkmcnt(0)" ::: "memory"); *(volatile LAS u32*)sig = 1; }
    }
    if constexpr (K % 8 == 0) __builtin_amdgcn_s_setprio(3 - K / 8);
    vmwait<(P == 0 || K < KB ? WIN - 1 : 31 - K)>(d[K]);
    c0 = mfma(wr[K & 15][0], d[K], c0); c1 = mfma(wr[K & 15][1], d[K], c1);
    asm volatile("" : "+v"(c0), "+v"(c1));
    if constexpr (K + WIN < 32) d_issue<K + WIN>(d, s);
    if constexpr (K == 1) { if (late) bar(); }
    if constexpr (P == 0 && K >= KB) w_one<1>(nx, (K - KB) >> 2, (K - KB) & 3);
    if constexpr (K >= 1 && K <= 16) w_frag<P>(a0, wr[K - 1], 4 + ((K - 1) >> 2), (K - 1) & 3);
    wr_steps<P, K + 1>(d, wr, a0, s, nx, sig, late, c0, c1);
  }
}
template <int I> DI void d_first(u32x4 (&d)[32], const DSrc& s) { if constexpr (I < WIN) { d_issue<I>(d, s); d_first<I + 1>(d, s); } }
template <int P> DI void wr_gemm(u32x4 (&wr)[16][2], const WA& a0, const DSrc& s, const WSrc& nx, u32 sig, bool late, f32x16& c0, f32x16& c1) {
  u32x4 d[32];
  d_first<0>(d, s);
  c0 = splat(0.f); c1 = splat(0.f);
  wr_steps<P, 0>(d, wr, a0, s, nx, sig, late, c0, c1);
  if constexpr (P == 0) {
#pragma unroll
    for (int i = WIN; i < 12; ++i) w_one<1>(nx, i >> 2, i & 3);
  }
}
constexpr u32 RSL = 1088;
template <u32 RS> DI void red_write(u32 a, u32 bs, const f32x16& c0, const f32x16& c1) {
#pragma unroll
  for (int q = 0; q < 4; ++q) {
    *(LAS f32x4*)(a + q * RS) = f32x4{c0[4 * q], c0[4 * q + 1], c0[4 * q + 2], c0[4 * q + 3]};
    *(LAS f32x4*)(a + bs + q * RS) = f32x4{c1[4 * q], c1[4 * q + 1], c1[4 * q + 2], c1[4 * q + 3]};
  }
}

template <int O, int I> DI void put16(const f32x16& c) {
  if constexpr (I < 16) { asm volatile("ds_write_addtid_b32 %0 offset:%1" ::"v"(c[I]), "i"(O + (I >> 1) * 768 + (I & 1) * 272) : "memory"); put16<O, I + 1>(c); }
}

constexpr u32 AST = 49152;
struct ASrc { u32x4 rx, rw; u32 vx0, vx1, vw, vq, sx, s0, s1, sq, rot, L; int w; };
DI void a_one(const ASrc& s, int c, int j) {
  const u32 st = s.L + (c % 3) * AST, cc = (c + s.rot) & 7;
  if (j < 4) glds(st + s.w * 4096 + j * 1024, (j & 1) ? s.vx1 : s.vx0, s.rx, s.sx + j * 8192 + cc * 128);
  else glds(st + 32768 + (j - 4) * 8192 + s.w * 1024, s.vw, s.rw, (j == 4 ? s.s0 : s.s1) + cc * (64 * 3072));
}
template <bool KV, int C, int J> DI void a_pref(const ASrc& s, const WSrc& nx) {
  if constexpr (J < 0) {}
  else if constexpr (C < 8) a_one(s, C, J);
  else if constexpr (C == 8) { if constexpr (KV && J < 4) glds(s.L + 98304 + s.w * 4096 + J * 1024, s.vq, s.rw, s.sq + J * (16 * 3072)); }
  else w_one<1>(nx, (6 * (C - 9) + J) >> 2, (6 * (C - 9) + J) & 3);
}
constexpr int a_gap(int C, int g) {
  const int t0[6] = {5, 6, 9, 10, 13, 14}, t1[6] = AGAP, t2[6] = {1, 2, -1, -1, -1, -1};
  for (int j = 0; j < 6; ++j) if ((C == 2 ? t0[j] : C == 10 ? t2[j] : t1[j]) == g) return j;
  return -1;
}
struct AMisc { u32x4 rws; u32 cfoff, tag, rdy, clr; int lane; };
struct AAd { u32 x0[4], x1[4], wa; };
template <bool KV, int T> DI void a_steps(const ASrc& s, const WSrc& nx, AAd& ad, u32x4 (&x0)[33], u32x4 (&f)[2][3], f32x16 (&c)[4], u32& cf, const AMisc& ms TSP) {
  if constexpr (T < 32) {
    constexpr int C = T >> 2, K = T & 3, P = T & 1, N = (T + 1) & 3;
    if constexpr (K == 3) {
      if constexpr (C < 6) vmbar<6 + ((KV && C == 1) || C == 3)>(); else if constexpr (C == 6) vmbar<KV ? 5 : 0>(); else vmbar<6>();
      if constexpr (C < 7) {
        constexpr u32 dl = (C + 1) % 3 ? AST : 0u - 2 * AST;
#pragma unroll
        for (int k = 0; k < 4; ++k) { ad.x0[k] += dl; ad.x1[k] += dl; }
        ad.wa += dl;
        asm volatile("" : "+v"(ad.x0[0]), "+v"(ad.x0[1]), "+v"(ad.x0[2]), "+v"(ad.x0[3]), "+v"(ad.x1[0]), "+v"(ad.x1[1]), "+v"(ad.x1[2]), "+v"(ad.x1[3]), "+v"(ad.wa));
      }
      if constexpr (C == 0 && KV) { if (s.w < 4) st128c(u32x4{~0u, ~0u, ~0u, ~0u}, ms.lane * 16, ms.rws, ms.clr); else st32c(~0u, (ms.lane & 31) * 4, ms.rws, ms.clr); }
      if constexpr (C == 2) { st32c(ms.tag, ms.rdy, ms.rws, 0); }
      if constexpr (C == 5 && KV) ld32c(cf, ms.cfoff, ms.rws, 0);
    }
    constexpr int PC = K == 3 ? C + 3 : C + 2, G0 = 4 * N;
    if (PRIO & 1) __builtin_amdgcn_s_setprio(3 - N);
    if constexpr (T < 31) { x0[T + 1] = lds128(ad.x0[N]); f[P ^ 1][1] = trd2(ad.wa + N * 2048, ad.wa + N * 2048 + 512); }
    SB();
    if constexpr (KV) c[0] = mfma(x0[T], f[P][1], c[0]); else c[0] = mfma(f[P][1], x0[T], c[0]);
    SB();
    if constexpr (T < 31) { f[P ^ 1][0] = lds128(ad.x1[N]); f[P ^ 1][2] = trd2(ad.wa + 8192 + N * 2048, ad.wa + 8192 + N * 2048 + 512); }
    a_pref<KV, PC, a_gap(PC, G0)>(s, nx);
    SB();
    if constexpr (KV) c[1] = mfma(f[P][0], f[P][1], c[1]); else c[1] = mfma(f[P][1], f[P][0], c[1]);
    SB();
    a_pref<KV, PC, a_gap(PC, G0 + 1)>(s, nx);
    SB();
    if constexpr (KV) c[2] = mfma(x0[T], f[P][2], c[2]); else c[2] = mfma(f[P][2], x0[T], c[2]);
    SB();
    a_pref<KV, PC, a_gap(PC, G0 + 2)>(s, nx);
    SB();
    if constexpr (KV) c[3] = mfma(f[P][0], f[P][2], c[3]); else c[3] = mfma(f[P][2], f[P][0], c[3]);
    SB();
    a_pref<KV, PC, a_gap(PC, G0 + 3)>(s, nx);
    SB();
    a_steps<KV, T + 1>(s, nx, ad, x0, f, c, cf, ms TSA);
  }
}

DI ASrc a_src(const char* x, const char* wqkv, u32 L, int lane, int w, int xcd, int slot) {
  const bool kv = slot < 30;
  const int ai = slot - 22, rc = (xcd & 6) + (kv ? ai >> 2 : slot - 30), hg = (xcd & 1) * 4 + (ai & 3), qj = xcd & 1;
  ASrc sa;
  sa.rx = srd(x, 2048 * 1024); sa.rw = srd(wqkv, 512 * 3072); sa.L = L; sa.w = w;
  sa.rot = xcd & RA;
  sa.vx0 = (lane >> 3) * 1024 + ((lane & 7) ^ (lane >> 4)) * 16; sa.vx1 = sa.vx0 ^ 64;
  sa.vw = (lane >> 3) * 3072 + ((lane & 7) ^ ((lane >> 2) & 4)) * 16;
  sa.vq = (lane >> 2) * 3072 + (lane & 3) * 16;
  sa.sx = (rc * 256 + w * 32) * 1024;
  sa.s0 = w * 8 * 3072 + (kv ? 1024 + hg * 128 : 512 + qj * 256);
  sa.s1 = sa.s0 + (kv ? 1024 : 128);
  sa.sq = ((w + sa.rot) & 7) * (64 * 3072) + hg * 64;
  return sa;
}
DI WSrc w1_src(const char* w1, u32 lds, int lane, int w, int xcd, int slot, int& ft, int& dt1) {
  const int gj = slot < 16 ? (xcd >> 1) * 16 + slot : 48 + (xcd >> 1) * 6 + slot;
  const u32 wch = (lane & 7) ^ ((lane >> 2) & 4);
  ft = gj >> 2; dt1 = (xcd & 1) * 4 + (gj & 3);
  WSrc s1;
  s1.rs = srd(w1, 2048 * 2752); s1.stride = 2752; s1.lds = lds;
  s1.voff = (lane >> 3) * 2752 + (ft == 21 ? (wch & 3) : wch) * 16;
  s1.sbase = ((w >> 1) * 512 + (w & 1) * 32) * 2752 + ft * 128;
  s1.rot = (2 * (dt1 + ft)) & R1;
  return s1;
}
template <bool KV> DI u32 attn(u32 ev, const char* x, const char* wqkv, u32x4 rws, const WSrc& s2, u32 L, int lane, int w, int xcd, int slot, u32 wa0, u32x4 (&wr)[16][2] TSP) {
  const int hh = lane >> 5, col = lane & 31, q4 = (lane & 15) >> 2, mi = w >> 1, ni = w & 1;
  const int ai = slot - 22, rc = (xcd & 6) + (KV ? ai >> 2 : slot - 30), hg = (xcd & 1) * 4 + (ai & 3), qj = xcd & 1;
  const ASrc sa = a_src(x, wqkv, L, lane, w, xcd, slot);
  AAd ad;
#pragma unroll
  for (int k = 0; k < 4; ++k) { ad.x0[k] = L + (mi * 64 + ni * 32 + col) * 128 + ((2 * k + hh) ^ ((col >> 1) & 7)) * 16; ad.x1[k] = ad.x0[k] ^ 4096; }
  ad.wa = L + 32768 + (8 * hh + q4) * 128 + 64 * ((q4 >> 1) ^ ni) + 32 * ((lane >> 4) & 1) + 8 * (lane & 3);
  const int pr = hg + (mi & 1) * 8;
  const bool ga = w < 4;
  const u32 cfoff = ga ? WS_F + xcd * FRS + (128 + (pr >> 1) * 8 + (lane & 7)) * 128 : OOB;
  asm volatile("" : "+v"(ad.x0[0]), "+v"(ad.x0[1]), "+v"(ad.x0[2]), "+v"(ad.x0[3]), "+v"(ad.x1[0]), "+v"(ad.x1[1]), "+v"(ad.x1[2]), "+v"(ad.x1[3]), "+v"(ad.wa)::"memory");
  const u32 tag = tag_wait<6>(ev);
  bar();
  TS(1);
  const AMisc ms = {rws, cfoff, tag, lane == 0 ? WS_F + w * FRS + (KV ? 128 + hg * 8 + rc : 192 + qj * 8 + rc) * 128 : OOB, w < 4 ? WS_KV + ((hg * 2 + (w >> 1)) * 8 + rc) * 2048 + (w & 1) * 1024 : WS_Z + ((hg * 2 + (w & 1)) * 8 + rc) * 128, lane};
  u32x4 x0[33], f[2][3]; f32x16 c[4]; u32 cf;
  c[0] = c[1] = splat(KV ? 1.f : 1.000001f); c[2] = c[3] = splat(KV ? 0.f : 1.000001f);
  x0[0] = lds128(ad.x0[0]); f[0][0] = lds128(ad.x1[0]); f[0][1] = trd2(ad.wa, ad.wa + 512); f[0][2] = trd2(ad.wa + 8192, ad.wa + 8192 + 512);
  a_steps<KV, 0>(sa, s2, ad, x0, f, c, cf, ms TSA);
  __builtin_amdgcn_s_setprio(0);
  TS(2);
  asm volatile("" : "+v"(c[0]), "+v"(c[1]), "+v"(c[2]), "+v"(c[3]));
  TS(3);
  const u32 pfl = WS_F + xcd * FRS + (512 + rc * 16 + 8 + qj) * 128;
  if constexpr (!KV) {
#pragma unroll
    for (int j = 0; j < 4; ++j)
#pragma unroll
      for (int h = 0; h < 2; ++h) { st128c(phi8<true>(c[j], 8 * h), lane * 16 + qj * 8192 + ni * 2048, rws, WS_P + (rc * 8 + (w ^ (j & 1))) * 16384 + ((j & 2) * 2 + h) * 1024); SB(); }
    TS(5);
    asm volatile("s_barrier" ::: "memory");
    w_issue<1>(s2, 2);
    vmbar<4>();
    if (w == 0) st32c(tag, lane == 0 ? 0 : OOB, rws, pfl);
    TS(4);
    if (blockIdx.x == 255 && w == 0) {
#pragma unroll 1
      for (int i = 0; i < 4; ++i) poll(WS_F + 7 * FRS + (i * 64 + lane) * 128, rws, tag);
      st32c(tag, lane == 0 ? 0 : OOB, rws, WS_E);
    }
    vmbar<0>();
    w_stage(w_addr(wa0), wr, 0, 12);
    bar();
    w_flag<3>(s2, w, tag, OOB, rws, 0);
    return tag;
  }
  const u32 S = L + 65536, KT = S + 14336, ZT = KT + 2048;
  if (w >= 4) w_issue<1>(s2, 2);
  const u32 on = ((lane >> 4) ^ (lane >> 2)) & 1 ? 0 : 0x3f803f80u;
  const u32x4 cs = {on, on, on, on};
  f32x16 d1 = splat(0.f); f32x4 d2 = {64e-6f, 64e-6f, 64e-6f, 64e-6f}, d3 = {0.f, 0.f, 0.f, 0.f};
  u32x4 p[4];
#pragma unroll
  for (int i = 0; i < 4; ++i) {
    const f32x16& k = c[i >> 1]; const int o = 8 * (i & 1);
    p[i][0] = phi2<false>(k[o], k[o + 1]); SB();
    if (i) d2 = mfma16(cs, p[i - 1], d2);
    const u32x4 v = pack8(c[2 + (i >> 1)], o);
    p[i][1] = phi2<false>(k[o + 2], k[o + 3]); SB();
    d3 = mfma16(cs, v, d3);
    p[i][2] = phi2<false>(k[o + 4], k[o + 5]); p[i][3] = phi2<false>(k[o + 6], k[o + 7]); SB();
    d1 = mfma(p[i], v, d1);
    SB(); if (w < 4) w_one<1>(s2, 2, i); SB();
  }
  d2 = mfma16(cs, p[3], d2);
  const bool lo = col < 16;
  f32x4 k0 = {lo ? d1[0] : d1[8], lo ? d1[1] : d1[9], lo ? d1[2] : d1[10], lo ? d1[3] : d1[11]};
  f32x4 k1 = {lo ? d1[4] : d1[12], lo ? d1[5] : d1[13], lo ? d1[6] : d1[14], lo ? d1[7] : d1[15]};
  const float ev6 = d3[0] * 1e-6f;
  k0 = add4(k0, f32x4{ev6, ev6, ev6, ev6}); k1 = add4(k1, f32x4{ev6, ev6, ev6, ev6});
  const float zp = d2[0];
  if (w >= 2) { *(LAS f32x4*)(S + (w - 2) * 2048 + lane * 16) = k0; *(LAS f32x4*)(S + (w - 2) * 2048 + 1024 + lane * 16) = k1; *(LAS float*)(S + 12288 + (w - 2) * 256 + lane * 4) = zp; }
  TS(13);
  vmbar<0>();
  TS(14);
  if (w < 2) {
    const u32 a = S + w * 2048 + lane * 16, z = S + 12288 + w * 256 + lane * 4;
    k0 = add4(add4(k0, ldsf4(a)), add4(ldsf4(a + 4096), ldsf4(a + 8192))); k1 = add4(add4(k1, ldsf4(a + 1024)), add4(ldsf4(a + 5120), ldsf4(a + 9216)));
    const float t = add1(add1(zp, ldsf(z)), add1(ldsf(z + 512), ldsf(z + 1024)));
    u32x4 p0 = __builtin_bit_cast(u32x4, k0), p1 = __builtin_bit_cast(u32x4, k1);
#pragma unroll
    for (int i = 0; i < 4; ++i) { p0[i] = umin(p0[i], ~1u); p1[i] = umin(p1[i], ~1u); }
    const u32 o = (hg * 2 + w) * 8 + rc;
    st128c(p0, lane * 16, rws, WS_KV + o * 2048); st128c(p1, lane * 16, rws, WS_KV + o * 2048 + 1024); st32c(umin(__float_as_uint(t), ~1u), col * 4, rws, WS_Z + o * 128);
  }
  TS(4);
  u32x4 pq[2][2], t[8]; u32x2 z; u32 pf;
  u32 qa = L + 98304 + (8 * hh + q4) * 64 + 32 * ((lane >> 4) & 1) + 8 * (lane & 3);
  asm volatile("" : "+v"(qa), "+v"(cf));
  u32x4 qf[4];
#pragma unroll
  for (int i = 0; i < 3; ++i) qf[i] = trd2(qa + i * 1024, qa + i * 1024 + 256);
  f32x16 cq = splat(1.000001f);
#pragma unroll
  for (int i = 0; i < 32; ++i) {
    SB();
    if (i < 29) qf[(i + 3) & 3] = trd2(qa + (i + 3) * 1024, qa + (i + 3) * 1024 + 256);
    if (QN >= 0) asm volatile("s_nop %0" ::"i"(QN < 0 ? 0 : QN));
    SB();
    cq = mfma(qf[i & 3], x0[i], cq);
  }
  SB();
  TS(18);
  auto sweep = [&]() {
#pragma unroll
    for (int r = 0; r < 8; ++r) ld128c<0>(t[r], lane * 16, rws, WS_KV + (pr * 8 + r) * 2048 + ni * 1024);
    asm volatile("buffer_load_dwordx2 %0, %1, %2, %3 offen sc1" : "=v"(z) : "v"((lane >> 3) * 128 + (lane & 7) * 8), "s"(rws), "s"(WS_Z + pr * 1024 + ni * 64) : "memory");
  };
  asm volatile("" : "=v"(t[0]), "=v"(t[1]), "=v"(t[2]), "=v"(t[3]), "=v"(t[4]), "=v"(t[5]), "=v"(t[6]), "=v"(t[7]), "=v"(z));
  if (ga && __builtin_amdgcn_ballot_w64(cf != tag) != 0) poll(cfoff, rws, tag);
  ld32c(pf, pfl, rws, 0);
  if (mi == 0) sweep();
  pq[0][0] = phi8<true>(cq, 0); pq[0][1] = phi8<true>(cq, 8);
  asm volatile("" : "+v"(pq[0][0]), "+v"(pq[0][1]));
  TS(5);
  w_stage(w_addr(wa0), wr, 0, 12);
  SB();
  if (mi == 1) sweep();
  if (!ga) vmwait<0>();
  asm volatile("s_waitcnt vmcnt(9)" : "+v"(pf)::"memory");
  if (__builtin_amdgcn_ballot_w64(pf != tag) != 0) poll(pfl, rws, tag);
  ld128c<0>(pq[1][0], lane * 16, rws, WS_P + (rc * 8 + w) * 16384 + hg * 2048); ld128c<1024>(pq[1][1], lane * 16, rws, WS_P + (rc * 8 + w) * 16384 + hg * 2048);
  TS(15);
#define GW(N) asm volatile("s_waitcnt vmcnt(%9)" : "+v"(t[0]), "+v"(t[1]), "+v"(t[2]), "+v"(t[3]), "+v"(t[4]), "+v"(t[5]), "+v"(t[6]), "+v"(t[7]), "+v"(z) : "i"(N) : "memory")
  GW(2);
  if (ga) {
    for (int it = 0; it < SPIN_MAX; ++it) {
      u32 m = umax(z[0], z[1]);
#pragma unroll
      for (int r = 0; r < 8; ++r) m = umax(umax(umax(m, t[r][0]), t[r][1]), umax(t[r][2], t[r][3]));
      if (__builtin_amdgcn_ballot_w64(m == ~0u) == 0) break;
      sweep();
      GW(0);
    }
    TS(16);
    f32x4 a = __builtin_bit_cast(f32x4, t[0]);
#pragma unroll
    for (int r = 1; r < 8; ++r) a += __builtin_bit_cast(f32x4, t[r]);
    float zs = fold32(__uint_as_float(z[0]), __uint_as_float(z[1]));
    zs = dppadd<0x128>(fold16(zs, zs));
    const int pl = mi, zf = 2 * (lane & 7) + hh;
    *(LAS u32x2*)(KT + pl * 1024 + lane * 16 + ni * 8) = u32x2{pk(a[0], a[1]), pk(a[2], a[3])};
    *(LAS unsigned short*)(ZT + pl * 64 + ni * 32 + ((zf >> 2) & 1) * 16 + ((zf & 3) + 4 * (zf >> 3)) * 2) = (unsigned short)pk(zs, zs);
  }
  bar();
  TS(6);
  f32x16 dd[2][2];
#pragma unroll
  for (int j = 0; j < 2; ++j) {
    if (j) asm volatile("s_waitcnt vmcnt(0)" : "+v"(pq[1][0]), "+v"(pq[1][1])::"memory");
#pragma unroll
    for (int h = 0; h < 2; ++h) dd[j][h] = mfma(lds128(((col < 16) == (h == 0)) ? KT + j * 1024 + lane * 16 : ZT + j * 64 + h * 32 + hh * 16), pq[j][h], splat(0.f));
  }
  SB();
#pragma unroll
  for (int c = 3; c < 3 + NE; ++c) w_issue<1>(s2, c);
  SB();
#pragma unroll
  for (int j = 0; j < 2; ++j)
#pragma unroll
    for (int h = 0; h < 2; ++h) {
      const int o = 8 * h;
      const f32x16& e = dd[j][h];
      const float rd = __builtin_amdgcn_rcpf(e[8 - o] + 1e-6f);
      const u32x4 ov = {pk(e[o] * rd, e[o + 1] * rd), pk(e[o + 2] * rd, e[o + 3] * rd), pk(e[o + 4] * rd, e[o + 5] * rd), pk(e[o + 6] * rd, e[o + 7] * rd)};
      st128c(ov, lane * 16, rws, WS_O + (rc * 8 + w) * 32768 + ((hg + 8 * j) * 2 + h) * 1024);
    }
  TS(17);
  bar();
  w_flag<3 + NE>(s2, w, tag, lane < 2 ? ((rc & 6) + lane) * FRS : OOB, rws, WS_F + (512 + rc * 16 + hg) * 128);
  return tag;
}

__global__ __launch_bounds__(512, 2) void fused_kernel(const char* __restrict__ x, const char* __restrict__ wqkv, const char* __restrict__ wo,
                                                        const char* __restrict__ w1, const char* __restrict__ b1, const char* __restrict__ w2,
                                                        const char* __restrict__ b2, const char* __restrict__ gw, char* __restrict__ out, char* ws TSK) {
  __shared__ __attribute__((aligned(1024))) char sm[163840];
  TSBEG; TS(0);
  u32 ev;
  {
    unsigned long long pc; u32 cn;
    asm volatile("s_getpc_b64 %0\n.Lfk_pc:\n\ts_mov_b32 %1, .Lfk_end-.Lfk_pc" : "=s"(pc), "=s"(cn));
    const u32 t = threadIdx.x, b = blockIdx.x >> 3;
    const u32 vo = CW == 1 ? (t < 64 ? (b * 64 + t) * 128 : OOB) : CW == 2 ? (t < 64 ? ((b - 16) * 64 + t) * 128 : OOB) : CW == 3 ? (t < 9 ? (b * 9 + t) * 128 : OOB) : (b == 0 || b == 16 ? t * 128 : OOB);
    asm volatile("buffer_load_dword %0, %1, %2, 0 offen" : "=v"(ev) : "v"(vo), "s"(srd((const void*)pc, cn)) : "memory");
    SB();
  }
  asm volatile("" : "+s"(x), "+s"(wqkv), "+s"(wo), "+s"(w1), "+s"(b1), "+s"(w2), "+s"(b2), "+s"(gw), "+s"(out), "+s"(ws));
  const u32 L = (u32)(size_t)(LAS char*)sm;
  const int lane = threadIdx.x & 63, w = __builtin_amdgcn_readfirstlane(threadIdx.x >> 6);
  int xcd = blockIdx.x & 7, slot = blockIdx.x >> 3;
  const int hh = lane >> 5;
  const u32x4 rws = srd(ws, WS_END);
  u32x4 xr[2], gr;
  asm volatile("s_nop 4" : "=v"(xr[0]), "=v"(xr[1]), "=v"(gr)::"memory");
  ev_load(ev, rws);
  if (slot < 22) {
    int ft, dt1;
    const WSrc s1 = w1_src(w1, L + w * 4096, lane, w, xcd, slot, ft, dt1);
    if (slot < 16) {
      const int r = (xcd * 16 + slot) * 16 + w;
      const u32x4 rx = srd(x, 2048 * 1024);
      ld128(xr[0], lane * 16, rx, r * 1024); ld128(xr[1], lane * 16, rx, (r + 8) * 1024);
      ld128(gr, lane * 16, srd(gw, 1024), 0);
      asm volatile("s_barrier" ::: "memory");
    }
    w_issue<0>(s1, 0);
  } else {
    const ASrc sa = a_src(x, wqkv, L, lane, w, xcd, slot);
#pragma unroll
    for (int j = 0; j < 6; ++j) a_one(sa, 0, j);
    asm volatile("s_barrier" ::: "memory");
#pragma unroll
    for (int j = 0; j < 6; ++j) a_one(sa, 1, j);
  }
  asm volatile("" : "+s"(slot), "+s"(xcd));
  const int g = w >> 1, hf = w & 1, q4 = (lane & 15) >> 2;
  const u32 wch = (lane & 7) ^ ((lane >> 2) & 4);
  const u32 wa0 = L + g * 8192 + (4 * hh + q4) * 128 + 64 * (q4 >> 1) + 32 * ((lane >> 4) & 1) + 8 * (lane & 3);

  const int dt = (xcd & 1) * 4 + (slot & 3), stl = (xcd >> 1) * 8 + (slot >> 2);
  WSrc s2;
  s2.rs = g < 3 ? srd(w2, 1376 * 4096) : srd(wo, 512 * 1024); s2.stride = g < 3 ? 4096 : 1024; s2.lds = L + g * 8192 + hf * 4096;
  s2.voff = (lane >> 3) * s2.stride + wch * 16;
  s2.sbase = g < 3 ? (g * 512 + hf * 32) * 4096 + stl * 128 : hf * 32 * 1024 + dt * 128;
  s2.rot = 2 * ((slot + (slot >> 2)) & R2);
  u32 tag;
  u32x4 wr[16][2];

  if (slot >= 30) tag = attn<false>(ev, x, wqkv, rws, s2, L, lane, w, xcd, slot, wa0, wr TSA);
  else if (slot >= 22) tag = attn<true>(ev, x, wqkv, rws, s2, L, lane, w, xcd, slot, wa0, wr TSA);
  else {
    const int gi = slot < 16 ? xcd * 16 + slot : 112 + xcd * 6 + slot;
    int ft, dt1;
    const WSrc s1 = w1_src(w1, s2.lds, lane, w, xcd, slot, ft, dt1);
    if (slot < 16) {
      const int sb = xcd * 16 + slot;
      tag = tag_wait<4>(ev);
      asm volatile("" : "+v"(xr[0]), "+v"(xr[1]), "+v"(gr)::"memory");
      TS(1);
      f32x2 v[2][4], t; float s[2], gd[2], q[2];
#pragma unroll
      for (int j = 0; j < 2; ++j) {
#pragma unroll
        for (int i = 0; i < 4; ++i) v[j][i] = f32x2{blo(xr[j][i]), bhi(xr[j][i])};
        t = (v[j][0] + v[j][1]) + (v[j][2] + v[j][3]); s[j] = t[0] + t[1];
      }
      const float t0 = fold32(s[0], s[1]), t1 = rowsum(fold16(t0, t0)) * (1.f / 512.f);
#pragma unroll
      for (int j = 0; j < 2; ++j) {
        t = f32x2{0.f, 0.f};
#pragma unroll
        for (int i = 0; i < 4; ++i) t = __builtin_elementwise_fma(v[j][i], f32x2{blo(gr[i]), bhi(gr[i])}, t);
        gd[j] = t[0] + t[1];
        const float m = rdl(t1, 32 * j);
        t = f32x2{0.f, 0.f};
#pragma unroll
        for (int i = 0; i < 4; ++i) { v[j][i] -= m; t = __builtin_elementwise_fma(v[j][i], v[j][i], t); }
        q[j] = t[0] + t[1];
      }
      const float t2 = rowsum(fold16(fold32(q[0], q[1]), fold32(gd[0], gd[1])));
      const float rs = __builtin_amdgcn_rsqf(t2 * (1.f / 512.f) + 1e-5f);
#pragma unroll
      for (int j = 0; j < 2; ++j) {
        const float r = rdl(rs, 32 * j);
#pragma unroll
        for (int i = 0; i < 4; ++i) v[j][i] *= r;
        *(LAS u32x4*)(L + (w + 8 * j) * 1088 + lane * 16) = u32x4{pk(v[j][0][0], v[j][0][1]), pk(v[j][1][0], v[j][1][1]), pk(v[j][2][0], v[j][2][1]), pk(v[j][3][0], v[j][3][1])};
      }
      const float gt = __builtin_amdgcn_rcpf(1.f + __builtin_amdgcn_exp2f(t2 * -1.4426950408889634f));
      TS(13);
      bar();
      const u32 ta = L + (4 * hh + q4) * 1088 + w * 128 + 32 * ((lane >> 4) & 1) + 8 * (lane & 3);
      const u32x4 h0 = trd2(ta, ta + 8 * 1088), h1 = trd2(ta + 64, ta + 64 + 8 * 1088);
      st32c(__float_as_uint(gt), (lane & 31) == 16 ? lane & 32 : OOB, rws, WS_G + (sb * 16 + w) * 4);
      st128c(h0, lane * 16, rws, WS_H + w * 262144 + sb * 1024);
      st128c(h1, lane * 16, rws, WS_H + w * 262144 + 131072 + sb * 1024);
      asm volatile("s_barrier" ::: "memory");
      TS(14);
      w_issue<0>(s1, 1);
      TS(15);
      vmbar<4>();
    } else {
      u32 tl;
      asm volatile("buffer_load_dword %0, %1, %2, 0 offen" : "=v"(tl) : "v"((lane << TL) + 2048),
                   "s"(w == 0 ? srd(w2, 1376 * 4096) : w == 1 ? srd(wo, 512 * 1024) : w == 2 ? srd(out, 2048 * 1024) : w == 3 ? srd(b1, 2752) : w == 4 ? srd(b2, 4096) : srd(ws, w == 5 ? WS_END : 0)) : "memory");
      w_issue<0>(s1, 1);
      tag = tag_wait<4>(ev);
      asm volatile("" ::"v"(tl));

      bar();
    }
    TS(2);
    const WA a0 = w_addr(wa0);
    if (w == 0) st32c(tag, lane < 8 ? lane * FRS : OOB, rws, WS_F + (slot < 16 ? gi : gi + 80) * 128);
    w_round<0, 0, 2>(s1, a0, wr); w_issue<0>(s1, 3);
    TS(18);
    vmbar0<8>(w);
    TS(19);
    w_round<0, 1, 4>(s1, a0, wr);
    vmbar<8>();
    TS(20);
    w_round<0, 2, 5>(s1, a0, wr);
    vmbar<8>();
    TS(21);
    const u32 hfl = hf ? WS_F + xcd * FRS + (g * 32 + (lane & 31)) * 128 : OOB;
    u32 pf; u32x2 bb0, bb1;
    ld32c(pf, hfl, rws, 0);
    asm volatile("buffer_load_dwordx2 %0, %2, %3, %4 offen\n\tbuffer_load_dwordx2 %1, %2, %3, %4 offen offset:16" : "=&v"(bb0), "=&v"(bb1) : "v"(hh * 8), "s"(srd(b1, 2752)), "s"(ft * 128 + g * 32) : "memory");
    w_round<0, 3, 6>(s1, a0, wr); w_issue<0>(s1, 7);
    TS(3);
    asm volatile("s_waitcnt vmcnt(8)" : "+v"(pf), "+v"(bb0), "+v"(bb1)::"memory");
    if (hf && __builtin_amdgcn_ballot_w64(pf != tag) != 0) poll(hfl, rws, tag);
    bar();
    TS(4);
    f32x16 c0, c1;
    wr_gemm<0>(wr, a0, DSrc{rws, (u32)lane * 16, WS_H + (dt1 * 2 + hf) * 131072 + g * 32768, s1.rot}, s2, 0, true, c0, c1);
    TS(5);
    const u32 R = L + 65536 + hf * 32768 + lane * 16;
    f32x4 lo = {blo(bb0[0]), bhi(bb0[0]), blo(bb0[1]), bhi(bb0[1])}, hi = {blo(bb1[0]), bhi(bb1[0]), blo(bb1[1]), bhi(bb1[1])};
#pragma unroll
    for (int q = 0; q < 4; ++q) {
      const f32x16& c = q < 2 ? c0 : c1; const int o = (q & 1) * 8;
      const f32x4 a = {c[o], c[o + 1], c[o + 2], c[o + 3]}, b = {c[o + 4], c[o + 5], c[o + 6], c[o + 7]};
      if (q == g) { lo += a; hi += b; }
      else { const u32 t = R + q * 8192 + ((g - q) & 3) * 2048; *(LAS f32x4*)t = a; *(LAS f32x4*)(t + 1024) = b; }
    }
    TS(16);
    vmbar<0>();
    TS(17);
    if (SO && w >= 4) { w_stage(a0, wr, 0, 12); SB(); }
#pragma unroll
    for (int k = 1; k < 4; ++k) { lo += ldsf4(R + g * 8192 + k * 2048); hi += ldsf4(R + g * 8192 + k * 2048 + 1024); }
    const u32x4 uv = {pk(gelu(lo[0]), gelu(lo[1])), pk(gelu(lo[2]), gelu(lo[3])), pk(gelu(hi[0]), gelu(hi[1])), pk(gelu(hi[2]), gelu(hi[3]))};
    st128c(uv, lane * 16, rws, WS_U + (dt1 * 2 + hf) * UDB + (ft * 4 + g) * 1024);
    TS(6);
    if (!SO || w < 4) w_stage(a0, wr, 0, 12);
    bar();
    TS(22);
    w_flag<3>(s2, w, tag, lane < 4 ? (lane * 2 + (dt1 >> 2)) * FRS : OOB, rws, WS_F + (256 + dt1 * 32 + ft) * 128);
  }

  {
    TS(7);
    asm volatile("" : "+s"(s2.lds), "+s"(s2.rot), "+s"(s2.sbase));
    const int sr = w * 8 + (lane >> 3), cq = lane & 7;
    u32 b2v;
    asm volatile("buffer_load_ushort %0, %1, %2, %3 offen" : "=v"(b2v) : "v"(sr * 2), "s"(srd(b2, 4096)), "s"(stl * 128) : "memory");
    const u32 GT = L + 131072, SG = GT + 256;
    const WA a0 = w_addr(wa0);
    f32x16 c0, c1;
    const int nf = g == 2 ? 6 : 8;
    const u32 fo = WS_F + xcd * FRS + ((g < 3 ? 256 + dt * 32 + g * 8 : 512 + (stl >> 2) * 16) + (lane < nf ? lane : 0)) * 128;
    const bool early = EP && slot >= 22 && slot < 30 && (EP == 2 || g < 3);
    u32 pf;
    if (PP == 2) ld32c(pf, early ? fo : OOB, rws, 0);
    w_issue<1>(s2, 6);
    if (PP == 1) ld32c(pf, early ? fo : OOB, rws, 0);
    w_issue<1>(s2, 7);
    TS(24);
    vmbar0<18>(w);
    w_round<1, 3, 8>(s2, a0, wr);
    bar();
    TS(25);
    *(volatile LAS u32*)(SG + w * 4) = 0;
    TS(8);
    asm volatile("s_waitcnt vmcnt(%2)" : "+v"(pf), "+v"(b2v) : "i"(4 * PP) : "memory");
    const bool ok = early && __builtin_amdgcn_ballot_w64(pf != tag) == 0;
    if (!ok) poll_bar(fo, rws, tag);
    TS(9);
    u32 gv;
    ld32c(gv, w == 0 ? lane * 4 : OOB, rws, WS_G + stl * 256);
    const u32 ub = WS_U + (dt * 2 + hf) * UDB;
    wr_gemm<1>(wr, a0, DSrc{srd(ws, g < 3 ? ub + 86 * 1024 : WS_END), (u32)lane * 16, g < 3 ? ub + g * 32768 : WS_O + (stl * 2 + hf) * 32768, s2.rot}, s2, SG + w * 4, ok, c0, c1);
    asm volatile("" : "+v"(gv));
    TS(10);
    if (w == 0) *(LAS u32*)(GT + lane * 4) = gv;
    for (int it = 0; it < SPIN_MAX * 16 && *(volatile LAS u32*)(SG + (w ^ 1) * 4) == 0; ++it) {}
    const u32 pb = L + g * 8192 + hf * 32768;
    if (g < 3) {
      asm volatile("s_mov_b32 m0, %0\n\ts_nop 15\n\ts_nop 7" ::"s"(pb + hf * 128) : "memory");
      put16<0, 0>(c0);
      asm volatile("s_mov_b32 m0, %0\n\ts_nop 0" ::"s"(pb + hf * 128 + 6144) : "memory");
      put16<65536 - 6144, 0>(c1);
    } else red_write<RSL>(pb + lane * 16, 65536, c0, c1);
    bar();
    TS(11);
    const int sl = lane >> 3, i = (sl & 3) + 4 * (w & 3);
    const u32 ya = L + (cq >> 2) * (32768 + 128) + (w >> 2) * 65536 + (i >> 1) * 768 + (i & 1) * 272 + (sl >> 2) * 128 + (cq & 3) * 32;
    const f32x4 y0 = (ldsf4(ya) + ldsf4(ya + 8192)) + ldsf4(ya + 16384), y1 = (ldsf4(ya + 16) + ldsf4(ya + 8208)) + ldsf4(ya + 16400);
    const u32 aa = L + 24576 + (w >> 2) * 32768 + (cq >> 2) * 65536 + (cq & 3) * RSL + (sr & 31) * 16;
    const f32x4 a0v = ldsf4(aa), a1v = ldsf4(aa + 512);
    const float gt = ldsf(GT + sr * 4), bb = blo(b2v), og = 1.f - gt;
    u32x4 ov;
    ov[0] = pk(gt * a0v[0] + og * (y0[0] + bb), gt * a0v[1] + og * (y0[1] + bb));
    ov[1] = pk(gt * a0v[2] + og * (y0[2] + bb), gt * a0v[3] + og * (y0[3] + bb));
    ov[2] = pk(gt * a1v[0] + og * (y1[0] + bb), gt * a1v[1] + og * (y1[1] + bb));
    ov[3] = pk(gt * a1v[2] + og * (y1[2] + bb), gt * a1v[3] + og * (y1[3] + bb));
    *(__attribute__((address_space(1))) u32x4*)(out + (size_t)(stl * 64 + sr) * 1024 + dt * 128 + cq * 16) = ov;
    TS(12); TS(26); TSEND;
    asm volatile(".Lfk_end:");
  }
}

extern "C" void kernel_launch(void* const* d_in, const int* in_sizes, int n_in, void* d_out, int out_size, void* d_ws, size_t ws_size, hipStream_t stream) {
  fused_kernel<<<256, 512, 0, stream>>>((const char*)d_in[0], (const char*)d_in[1], (const char*)d_in[2], (const char*)d_in[4], (const char*)d_in[5],
                                        (const char*)d_in[6], (const char*)d_in[7], (const char*)d_in[8], (char*)d_out, (char*)d_ws);
}
```

```cpp
#include <hip/hip_runtime.h>
#include <hip/hip_bf16.h>

#pragma clang diagnostic ignored "-Wint-to-pointer-cast"
#define DI __device__ __forceinline__
#define LAS __attribute__((address_space(3)))
#define SB() __builtin_amdgcn_sched_barrier(0)
typedef unsigned u32;
typedef u32 u32x4 __attribute__((ext_vector_type(4)));
typedef u32 u32x2 __attribute__((ext_vector_type(2)));
typedef float f32x2 __attribute__((ext_vector_type(2)));
typedef float f32x4 __attribute__((ext_vector_type(4)));
typedef float f32x16 __attribute__((ext_vector_type(16)));
typedef __bf16 bf16x8 __attribute__((ext_vector_type(8)));
typedef __bf16 bf16x2 __attribute__((ext_vector_type(2)));
typedef short s16x4 __attribute__((ext_vector_type(4)));
typedef short s16x2 __attribute__((ext_vector_type(2)));

constexpr u32 WS_E = 0, WS_G = 32768;
constexpr u32 WS_F = 7u << 20, FRS = 641 * 128;
constexpr u32 WS_KV = 65536, WS_Z = WS_KV + 262144, WS_H = 1u << 20, WS_U = 3u << 20, WS_O = 5u << 20, WS_P = 8u << 20, WS_END = 9u << 20, OOB = 1u << 30;
constexpr u32 UDB = 88 * 1024;
constexpr int SPIN_MAX = 20000;
#define DSC "sc1"
#define R1 7
#define R2 3
#define RA 6
#define NF 1
#define NE 1
#define QP 16
#define GI 0
#define QPR 3
#define QN -1
#define PM 0
#define SPR 1
#define PP 2
#define SO 0
#define EP 0
#define KA 1
#define CW 4
#define OS 2
#define OSM "nt"
#define TL 18
#define NP1 8
#define NP2 8
#define SLP 4
#define MAP 0
#define AGAP {1, 3, 6, 9, 11, 14}
#define PRIO 3
#define WIN 10

#define TS(i)
#define TSP
#define TSA
#define TSK
#define TSBEG
#define TSEND

DI u32x4 srd(const void* p, u32 bytes) { const unsigned long long a = (unsigned long long)p; return u32x4{(u32)a, (u32)(a >> 32), bytes, 0x00020000u}; }
DI f32x16 mfma(u32x4 a, u32x4 b, f32x16 c) { return __builtin_amdgcn_mfma_f32_32x32x16_bf16(__builtin_bit_cast(bf16x8, a), __builtin_bit_cast(bf16x8, b), c, 0, 0, 0); }
DI f32x4 mfma16(u32x4 a, u32x4 b, f32x4 c) { return __builtin_amdgcn_mfma_f32_16x16x32_bf16(__builtin_bit_cast(bf16x8, a), __builtin_bit_cast(bf16x8, b), c, 0, 0, 0); }
DI u32x2 trd(u32 a) { return __builtin_bit_cast(u32x2, __builtin_amdgcn_ds_read_tr16_b64_v4i16((LAS s16x4*)a)); }
DI u32x4 trd2(u32 a, u32 b) { const u32x2 x = trd(a), y = trd(b); return u32x4{x[0], x[1], y[0], y[1]}; }
DI u32x4 lds128(u32 a) { return *(LAS u32x4*)a; }
DI f32x4 ldsf4(u32 a) { return *(LAS f32x4*)a; }
DI float ldsf(u32 a) { return *(LAS float*)a; }
DI u32 pk(float a, float b) { return __builtin_bit_cast(u32, __builtin_convertvector(f32x2{a, b}, bf16x2)); }
DI float blo(u32 v) { return __uint_as_float(v << 16); }
DI float bhi(u32 v) { return __uint_as_float(v & 0xffff0000u); }
DI u32x4 pack8(const f32x16& x, int o) { return u32x4{pk(x[o], x[o + 1]), pk(x[o + 2], x[o + 3]), pk(x[o + 4], x[o + 5]), pk(x[o + 6], x[o + 7])}; }
DI f32x16 splat(float v) { return f32x16{v, v, v, v, v, v, v, v, v, v, v, v, v, v, v, v}; }
DI float add1(float a, float b) { float r; asm("v_add_f32 %0, %1, %2" : "=v"(r) : "v"(a), "v"(b)); return r; }
DI f32x4 add4(f32x4 a, f32x4 b) { return f32x4{add1(a[0], b[0]), add1(a[1], b[1]), add1(a[2], b[2]), add1(a[3], b[3])}; }
DI u32 umin(u32 a, u32 b) { return a < b ? a : b; }
DI u32 umax(u32 a, u32 b) { return a > b ? a : b; }

DI void glds(u32 lds, u32 voff, u32x4 rs, u32 soff) {
  asm volatile("s_mov_b32 m0, %0\n\ts_nop 0\n\tbuffer_load_dwordx4 %1, %2, %3 offen lds" ::"s"(lds), "v"(voff), "s"(rs), "s"(soff) : "memory");
}
template <int OFF> DI void ld128c(u32x4& d, u32 voff, u32x4 rs, u32 soff) {
  asm volatile("buffer_load_dwordx4 %0, %1, %2, %3 offen offset:%4 " DSC : "=v"(d) : "v"(voff), "s"(rs), "s"(soff), "i"(OFF) : "memory");
}
DI void ld128(u32x4& d, u32 voff, u32x4 rs, u32 soff) { asm volatile("buffer_load_dwordx4 %0, %1, %2, %3 offen" : "=v"(d) : "v"(voff), "s"(rs), "s"(soff) : "memory"); }
template <int OFF = 0> DI void ld32c(u32& d, u32 voff, u32x4 rs, u32 soff) { asm volatile("buffer_load_dword %0, %1, %2, %3 offen offset:%4 sc1" : "=v"(d) : "v"(voff), "s"(rs), "s"(soff), "i"(OFF) : "memory"); }
DI void st128c(u32x4 v, u32 voff, u32x4 rs, u32 soff) { asm volatile("buffer_store_dwordx4 %0, %1, %2, %3 offen sc1\n\ts_nop 1" ::"v"(v), "v"(voff), "s"(rs), "s"(soff) : "memory"); }
DI void st32c(u32 v, u32 voff, u32x4 rs, u32 soff) { asm volatile("buffer_store_dword %0, %1, %2, %3 offen sc1" ::"v"(v), "v"(voff), "s"(rs), "s"(soff) : "memory"); }
template <int N> DI void vmwait() { asm volatile("s_waitcnt vmcnt(%0)" ::"i"(N) : "memory"); }
template <int N> DI void vmwait(u32x4& a) { asm volatile("s_waitcnt vmcnt(%1)" : "+v"(a) : "i"(N) : "memory"); }
DI void bar() { asm volatile("s_waitcnt lgkmcnt(0)\n\ts_barrier" ::: "memory"); }
template <int N> DI void vmbar() { asm volatile("s_waitcnt vmcnt(%0) lgkmcnt(0)\n\ts_barrier" ::"i"(N) : "memory"); }

DI void ev_load(u32& e, u32x4 ws) { asm volatile("buffer_load_dword %0, off, %1, 0" : "+v"(e) : "s"(ws) : "memory"); }
template <int N> DI u32 tag_wait(u32 e) {
  u32 t;
  asm volatile("s_waitcnt vmcnt(%2)\n\tv_readfirstlane_b32 %0, %1\n\ts_nop 1" : "=s"(t) : "v"(e), "i"(N) : "memory");
  return t * 1664525u + 1013904223u;
}
DI void poll(u32 voff, u32x4 ws, u32 tag) {
  for (int it = 0; it < SPIN_MAX; ++it) {
    u32 f;
    asm volatile("buffer_load_dword %0, %1, %2, 0 offen sc1\n\ts_waitcnt vmcnt(0)" : "=&v"(f) : "v"(voff), "s"(ws) : "memory");
    if (__builtin_amdgcn_ballot_w64(f != tag) == 0) break;
    __builtin_amdgcn_s_sleep(SLP);
  }
}
DI void poll_bar(u32 voff, u32x4 ws, u32 tag) {
  u32 f;
  asm volatile("buffer_load_dword %0, %1, %2, 0 offen sc1\n\ts_waitcnt vmcnt(0) lgkmcnt(0)\n\ts_barrier" : "=&v"(f) : "v"(voff), "s"(ws) : "memory");
  if (__builtin_amdgcn_ballot_w64(f != tag) != 0) poll(voff, ws, tag);
}
template <int C> DI float dppadd(float v) { return v + __int_as_float(__builtin_amdgcn_update_dpp(0, __float_as_int(v), C, 0xf, 0xf, true)); }
DI float rowsum(float v) { v = dppadd<0xB1>(v); v = dppadd<0x4E>(v); v = dppadd<0x141>(v); return dppadd<0x140>(v); }
DI float rdl(float v, int l) { return __int_as_float(__builtin_amdgcn_readlane(__float_as_int(v), l)); }
DI float fold32(float a, float b) { const auto r = __builtin_amdgcn_permlane32_swap(__float_as_uint(a), __float_as_uint(b), false, false); return __uint_as_float(r[0]) + __uint_as_float(r[1]); }
DI float fold16(float a, float b) { const auto r = __builtin_amdgcn_permlane16_swap(__float_as_uint(a), __float_as_uint(b), false, false); return __uint_as_float(r[0]) + __uint_as_float(r[1]); }
template <bool E> DI u32 phi2(float a, float b) {
  constexpr float l2e = 1.4426950408889634f, c = -l2e * (E ? 1.000001f : 1.f);
  float e0 = __builtin_amdgcn_fmed3f(__builtin_amdgcn_exp2f(__builtin_fmaf(a, l2e, c)), 0.f, 1.f), e1 = __builtin_amdgcn_fmed3f(__builtin_amdgcn_exp2f(__builtin_fmaf(b, l2e, c)), 0.f, 1.f);
  if (E) { e0 += 1e-6f; asm("" : "+v"(e0)); e1 += 1e-6f; }
  return pk(__builtin_amdgcn_fmed3f(a, e0, 3.4028234664e38f), __builtin_amdgcn_fmed3f(b, e1, 3.4028234664e38f));
}
template <bool E> DI u32x4 phi8(const f32x16& x, int o) { return u32x4{phi2<E>(x[o], x[o + 1]), phi2<E>(x[o + 2], x[o + 3]), phi2<E>(x[o + 4], x[o + 5]), phi2<E>(x[o + 6], x[o + 7])}; }
DI float gelu(float v) {
  const float t = v * (-2.3022081981f - 0.1029432397f * v * v);
  return v * __builtin_amdgcn_rcpf(1.f + __builtin_amdgcn_exp2f(t));
}

constexpr int wslot(int P, int c) { return (c + (P && c < 3 ? 4 : 1)) % 5; }
struct WSrc { u32x4 rs; u32 voff, sbase, stride, lds, rot; };
template <int P> DI void w_one(const WSrc& s, int c, int i) { glds(s.lds + wslot(P, c) * 32768 + i * 1024, s.voff, s.rs, s.sbase + (64 * ((c + s.rot) & 7) + 8 * i) * s.stride); }
template <int P> DI void w_issue(const WSrc& s, int c) {
#pragma unroll
  for (int i = 0; i < 4; ++i) w_one<P>(s, c, i);
}
struct WA { u32 a[3][2]; };
DI WA w_addr(u32 a0) { WA r;
#pragma unroll
  for (int i = 0; i < 3; ++i) { r.a[i][0] = a0 + i * 65536; r.a[i][1] = (a0 ^ 64) + i * 65536; asm volatile("" : "+v"(r.a[i][0]), "+v"(r.a[i][1])); }
  return r; }
template <int P> DI void w_frag(const WA& r, u32x4 (&f)[2], int c, int ks) {
#pragma unroll
  for (int b = 0; b < 2; ++b) { const u32 a = r.a[wslot(P, c) >> 1][b] + (wslot(P, c) & 1) * 32768 + ks * 2048; f[b] = trd2(a, a + 1024); }
}
template <int P, int C, int F> DI void w_round(const WSrc& s, const WA& a0, u32x4 (&wr)[16][2]) {
#pragma unroll
  for (int k = 0; k < 4; ++k) { w_frag<P>(a0, wr[4 * C + k], C, k); SB(); if constexpr (F < 8) w_one<P>(s, F, k); SB(); }
}
DI void w_stage(const WA& a0, u32x4 (&wr)[16][2], int k0, int k1) {
#pragma unroll
  for (int k = k0; k < k1; ++k) w_frag<1>(a0, wr[k], k >> 2, k & 3);
}
template <int C0> DI void w_flag(const WSrc& s, int w, u32 tag, u32 fv, u32x4 rws, u32 flag) {
  if constexpr (C0 < 6) w_issue<1>(s, C0);
  vmbar<(C0 < 6 ? 4 : 0)>();
  if (w == 0) st32c(tag, fv, rws, flag);
#pragma unroll
  for (int c = C0 + 1; c < 6; ++c) w_issue<1>(s, c);
}
template <int N> DI void vmbar0(int w) { if (w == 0) vmwait<N + 1>(); else vmwait<N>(); bar(); }
struct DSrc { u32x4 rs; u32 voff, sb, rot; };
template <int I> DI void d_issue(u32x4 (&d)[32], const DSrc& s) { ld128c<(I & 3) * 1024>(d[I], s.voff, s.rs, s.sb + (((I >> 2) + s.rot) & 7) * 4096); }
constexpr int KB = 32 - WIN;
template <int P, int K> DI void wr_steps(u32x4 (&d)[32], u32x4 (&wr)[16][2], const WA& a0, const DSrc& s, const WSrc& nx, u32 sig, bool late, f32x16& c0, f32x16& c1) {
  if constexpr (K < 32) {
    if constexpr (K == KB) {
      if constexpr (P == 0) bar();
      else { asm volatile("s_waitcnt lgkmcnt(0)" ::: "memory"); *(volatile LAS u32*)sig = 1; }
    }
    if constexpr (K % 8 == 0) __builtin_amdgcn_s_setprio(3 - K / 8);
    vmwait<(P == 0 || K < KB ? WIN - 1 : 31 - K)>(d[K]);
    c0 = mfma(wr[K & 15][0], d[K], c0); c1 = mfma(wr[K & 15][1], d[K], c1);
    asm volatile("" : "+v"(c0), "+v"(c1));
    if constexpr (K + WIN < 32) d_issue<K + WIN>(d, s);
    if constexpr (K == 1) { if (late) bar(); }
    if constexpr (P == 0 && K >= KB) w_one<1>(nx, (K - KB) >> 2, (K - KB) & 3);
    if constexpr (K >= 1 && K <= 16) w_frag<P>(a0, wr[K - 1], 4 + ((K - 1) >> 2), (K - 1) & 3);
    wr_steps<P, K + 1>(d, wr, a0, s, nx, sig, late, c0, c1);
  }
}
template <int I> DI void d_first(u32x4 (&d)[32], const DSrc& s) { if constexpr (I < WIN) { d_issue<I>(d, s); d_first<I + 1>(d, s); } }
template <int P> DI void wr_gemm(u32x4 (&wr)[16][2], const WA& a0, const DSrc& s, const WSrc& nx, u32 sig, bool late, f32x16& c0, f32x16& c1) {
  u32x4 d[32];
  d_first<0>(d, s);
  c0 = splat(0.f); c1 = splat(0.f);
  wr_steps<P, 0>(d, wr, a0, s, nx, sig, late, c0, c1);
  if constexpr (P == 0) {
#pragma unroll
    for (int i = WIN; i < 12; ++i) w_one<1>(nx, i >> 2, i & 3);
  }
}
constexpr u32 RSL = 1088;
template <u32 RS> DI void red_write(u32 a, u32 bs, const f32x16& c0, const f32x16& c1) {
#pragma unroll
  for (int q = 0; q < 4; ++q) {
    *(LAS f32x4*)(a + q * RS) = f32x4{c0[4 * q], c0[4 * q + 1], c0[4 * q + 2], c0[4 * q + 3]};
    *(LAS f32x4*)(a + bs + q * RS) = f32x4{c1[4 * q], c1[4 * q + 1], c1[4 * q + 2], c1[4 * q + 3]};
  }
}

template <int O, int I> DI void put16(const f32x16& c) {
  if constexpr (I < 16) { asm volatile("ds_write_addtid_b32 %0 offset:%1" ::"v"(c[I]), "i"(O + (I >> 1) * 768 + (I & 1) * 272) : "memory"); put16<O, I + 1>(c); }
}

constexpr u32 AST = 49152;
struct ASrc { u32x4 rx, rw; u32 vx0, vx1, vw, vq, sx, s0, s1, sq, rot, L; int w; };
DI void a_one(const ASrc& s, int c, int j) {
  const u32 st = s.L + (c % 3) * AST, cc = (c + s.rot) & 7;
  if (j < 4) glds(st + s.w * 4096 + j * 1024, (j & 1) ? s.vx1 : s.vx0, s.rx, s.sx + j * 8192 + cc * 128);
  else glds(st + 32768 + (j - 4) * 8192 + s.w * 1024, s.vw, s.rw, (j == 4 ? s.s0 : s.s1) + cc * (64 * 3072));
}
template <bool KV, int C, int J> DI void a_pref(const ASrc& s, const WSrc& nx) {
  if constexpr (J < 0) {}
  else if constexpr (C < 8) a_one(s, C, J);
  else if constexpr (C == 8) { if constexpr (KV && J < 4) glds(s.L + 98304 + s.w * 4096 + J * 1024, s.vq, s.rw, s.sq + J * (16 * 3072)); }
  else w_one<1>(nx, (6 * (C - 9) + J) >> 2, (6 * (C - 9) + J) & 3);
}
constexpr int a_gap(int C, int g) {
  const int t0[6] = {5, 6, 9, 10, 13, 14}, t1[6] = AGAP, t2[6] = {1, 2, -1, -1, -1, -1};
  for (int j = 0; j < 6; ++j) if ((C == 2 ? t0[j] : C == 10 ? t2[j] : t1[j]) == g) return j;
  return -1;
}
struct AMisc { u32x4 rws; u32 cfoff, tag, rdy, clr; int lane; };
struct AAd { u32 x0[4], x1[4], wa; };
template <bool KV, int T> DI void a_steps(const ASrc& s, const WSrc& nx, AAd& ad, u32x4 (&x0)[33], u32x4 (&f)[2][3], f32x16 (&c)[4], u32& cf, const AMisc& ms TSP) {
  if constexpr (T < 32) {
    constexpr int C = T >> 2, K = T & 3, P = T & 1, N = (T + 1) & 3;
    if constexpr (K == 3) {
      if constexpr (C < 6) vmbar<6 + ((KV && C == 1) || C == 3)>(); else if constexpr (C == 6) vmbar<KV ? 5 : 0>(); else vmbar<6>();
      if constexpr (C < 7) {
        constexpr u32 dl = (C + 1) % 3 ? AST : 0u - 2 * AST;
#pragma unroll
        for (int k = 0; k < 4; ++k) { ad.x0[k] += dl; ad.x1[k] += dl; }
        ad.wa += dl;
        asm volatile("" : "+v"(ad.x0[0]), "+v"(ad.x0[1]), "+v"(ad.x0[2]), "+v"(ad.x0[3]), "+v"(ad.x1[0]), "+v"(ad.x1[1]), "+v"(ad.x1[2]), "+v"(ad.x1[3]), "+v"(ad.wa));
      }
      if constexpr (C == 0 && KV) { if (s.w < 4) st128c(u32x4{~0u, ~0u, ~0u, ~0u}, ms.lane * 16, ms.rws, ms.clr); else st32c(~0u, (ms.lane & 31) * 4, ms.rws, ms.clr); }
      if constexpr (C == 2) { st32c(ms.tag, ms.rdy, ms.rws, 0); }
      if constexpr (C == 5 && KV) ld32c(cf, ms.cfoff, ms.rws, 0);
    }
    constexpr int PC = K == 3 ? C + 3 : C + 2, G0 = 4 * N;
    if (PRIO & 1) __builtin_amdgcn_s_setprio(3 - N);
    if constexpr (T < 31) { x0[T + 1] = lds128(ad.x0[N]); f[P ^ 1][1] = trd2(ad.wa + N * 2048, ad.wa + N * 2048 + 512); }
    SB();
    if constexpr (KV) c[0] = mfma(x0[T], f[P][1], c[0]); else c[0] = mfma(f[P][1], x0[T], c[0]);
    SB();
    if constexpr (T < 31) { f[P ^ 1][0] = lds128(ad.x1[N]); f[P ^ 1][2] = trd2(ad.wa + 8192 + N * 2048, ad.wa + 8192 + N * 2048 + 512); }
    a_pref<KV, PC, a_gap(PC, G0)>(s, nx);
    SB();
    if constexpr (KV) c[1] = mfma(f[P][0], f[P][1], c[1]); else c[1] = mfma(f[P][1], f[P][0], c[1]);
    SB();
    a_pref<KV, PC, a_gap(PC, G0 + 1)>(s, nx);
    SB();
    if constexpr (KV) c[2] = mfma(x0[T], f[P][2], c[2]); else c[2] = mfma(f[P][2], x0[T], c[2]);
    SB();
    a_pref<KV, PC, a_gap(PC, G0 + 2)>(s, nx);
    SB();
    if constexpr (KV) c[3] = mfma(f[P][0], f[P][2], c[3]); else c[3] = mfma(f[P][2], f[P][0], c[3]);
    SB();
    a_pref<KV, PC, a_gap(PC, G0 + 3)>(s, nx);
    SB();
    a_steps<KV, T + 1>(s, nx, ad, x0, f, c, cf, ms TSA);
  }
}

DI ASrc a_src(const char* x, const char* wqkv, u32 L, int lane, int w, int xcd, int slot) {
  const bool kv = slot < 30;
  const int ai = slot - 22, rc = (xcd & 6) + (kv ? ai >> 2 : slot - 30), hg = (xcd & 1) * 4 + (ai & 3), qj = xcd & 1;
  ASrc sa;
  sa.rx = srd(x, 2048 * 1024); sa.rw = srd(wqkv, 512 * 3072); sa.L = L; sa.w = w;
  sa.rot = xcd & RA;
  sa.vx0 = (lane >> 3) * 1024 + ((lane & 7) ^ (lane >> 4)) * 16; sa.vx1 = sa.vx0 ^ 64;
  sa.vw = (lane >> 3) * 3072 + ((lane & 7) ^ ((lane >> 2) & 4)) * 16;
  sa.vq = (lane >> 2) * 3072 + (lane & 3) * 16;
  sa.sx = (rc * 256 + w * 32) * 1024;
  sa.s0 = w * 8 * 3072 + (kv ? 1024 + hg * 128 : 512 + qj * 256);
  sa.s1 = sa.s0 + (kv ? 1024 : 128);
  sa.sq = ((w + sa.rot) & 7) * (64 * 3072) + hg * 64;
  return sa;
}
DI WSrc w1_src(const char* w1, u32 lds, int lane, int w, int xcd, int slot, int& ft, int& dt1) {
  const int gj = slot < 16 ? (xcd >> 1) * 16 + slot : 48 + (xcd >> 1) * 6 + slot;
  const u32 wch = (lane & 7) ^ ((lane >> 2) & 4);
  ft = gj >> 2; dt1 = (xcd & 1) * 4 + (gj & 3);
  WSrc s1;
  s1.rs = srd(w1, 2048 * 2752); s1.stride = 2752; s1.lds = lds;
  s1.voff = (lane >> 3) * 2752 + (ft == 21 ? (wch & 3) : wch) * 16;
  s1.sbase = ((w >> 1) * 512 + (w & 1) * 32) * 2752 + ft * 128;
  s1.rot = (2 * (dt1 + ft)) & R1;
  return s1;
}
template <bool KV> DI u32 attn(u32 ev, const char* x, const char* wqkv, u32x4 rws, const WSrc& s2, u32 L, int lane, int w, int xcd, int slot, u32 wa0, u32x4 (&wr)[16][2] TSP) {
  const int hh = lane >> 5, col = lane & 31, q4 = (lane & 15) >> 2, mi = w >> 1, ni = w & 1;
  const int ai = slot - 22, rc = (xcd & 6) + (KV ? ai >> 2 : slot - 30), hg = (xcd & 1) * 4 + (ai & 3), qj = xcd & 1;
  const ASrc sa = a_src(x, wqkv, L, lane, w, xcd, slot);
  AAd ad;
#pragma unroll
  for (int k = 0; k < 4; ++k) { ad.x0[k] = L + (mi * 64 + ni * 32 + col) * 128 + ((2 * k + hh) ^ ((col >> 1) & 7)) * 16; ad.x1[k] = ad.x0[k] ^ 4096; }
  ad.wa = L + 32768 + (8 * hh + q4) * 128 + 64 * ((q4 >> 1) ^ ni) + 32 * ((lane >> 4) & 1) + 8 * (lane & 3);
  const int pr = hg + (mi & 1) * 8;
  const bool ga = w < 4;
  const u32 cfoff = ga ? WS_F + xcd * FRS + (128 + (pr >> 1) * 8 + (lane & 7)) * 128 : OOB;
  asm volatile("" : "+v"(ad.x0[0]), "+v"(ad.x0[1]), "+v"(ad.x0[2]), "+v"(ad.x0[3]), "+v"(ad.x1[0]), "+v"(ad.x1[1]), "+v"(ad.x1[2]), "+v"(ad.x1[3]), "+v"(ad.wa)::"memory");
  const u32 tag = tag_wait<6>(ev);
  bar();
  TS(1);
  const AMisc ms = {rws, cfoff, tag, lane == 0 ? WS_F + w * FRS + (KV ? 128 + hg * 8 + rc : 192 + qj * 8 + rc) * 128 : OOB, w < 4 ? WS_KV + ((hg * 2 + (w >> 1)) * 8 + rc) * 2048 + (w & 1) * 1024 : WS_Z + ((hg * 2 + (w & 1)) * 8 + rc) * 128, lane};
  u32x4 x0[33], f[2][3]; f32x16 c[4]; u32 cf;
  c[0] = c[1] = splat(KV ? 1.f : 1.000001f); c[2] = c[3] = splat(KV ? 0.f : 1.000001f);
  x0[0] = lds128(ad.x0[0]); f[0][0] = lds128(ad.x1[0]); f[0][1] = trd2(ad.wa, ad.wa + 512); f[0][2] = trd2(ad.wa + 8192, ad.wa + 8192 + 512);
  a_steps<KV, 0>(sa, s2, ad, x0, f, c, cf, ms TSA);
  __builtin_amdgcn_s_setprio(0);
  TS(2);
  asm volatile("" : "+v"(c[0]), "+v"(c[1]), "+v"(c[2]), "+v"(c[3]));
  TS(3);
  const u32 pfl = WS_F + xcd * FRS + (512 + rc * 16 + 8 + qj) * 128;
  if constexpr (!KV) {
#pragma unroll
    for (int j = 0; j < 4; ++j)
#pragma unroll
      for (int h = 0; h < 2; ++h) { st128c(phi8<true>(c[j], 8 * h), lane * 16 + qj * 8192 + ni * 2048, rws, WS_P + (rc * 8 + (w ^ (j & 1))) * 16384 + ((j & 2) * 2 + h) * 1024); SB(); }
    TS(5);
    asm volatile("s_barrier" ::: "memory");
    w_issue<1>(s2, 2);
    vmbar<4>();
    if (w == 0) st32c(tag, lane == 0 ? 0 : OOB, rws, pfl);
    TS(4);
    if (blockIdx.x == 255 && w == 0) {
#pragma unroll 1
      for (int i = 0; i < 4; ++i) poll(WS_F + 7 * FRS + (i * 64 + lane) * 128, rws, tag);
      st32c(tag, lane == 0 ? 0 : OOB, rws, WS_E);
    }
    vmbar<0>();
    w_stage(w_addr(wa0), wr, 0, 12);
    bar();
    w_flag<3>(s2, w, tag, OOB, rws, 0);
    return tag;
  }
  const u32 S = L + 65536, KT = S + 14336, ZT = KT + 2048;
  if (w >= 4) w_issue<1>(s2, 2);
  const u32 on = ((lane >> 4) ^ (lane >> 2)) & 1 ? 0 : 0x3f803f80u;
  const u32x4 cs = {on, on, on, on};
  f32x16 d1 = splat(0.f); f32x4 d2 = {64e-6f, 64e-6f, 64e-6f, 64e-6f}, d3 = {0.f, 0.f, 0.f, 0.f};
  u32x4 p[4];
#pragma unroll
  for (int i = 0; i < 4; ++i) {
    const f32x16& k = c[i >> 1]; const int o = 8 * (i & 1);
    p[i][0] = phi2<false>(k[o], k[o + 1]); SB();
    if (i) d2 = mfma16(cs, p[i - 1], d2);
    const u32x4 v = pack8(c[2 + (i >> 1)], o);
    p[i][1] = phi2<false>(k[o + 2], k[o + 3]); SB();
    d3 = mfma16(cs, v, d3);
    p[i][2] = phi2<false>(k[o + 4], k[o + 5]); p[i][3] = phi2<false>(k[o + 6], k[o + 7]); SB();
    d1 = mfma(p[i], v, d1);
    SB(); if (w < 4) w_one<1>(s2, 2, i); SB();
  }
  d2 = mfma16(cs, p[3], d2);
  const bool lo = col < 16;
  f32x4 k0 = {lo ? d1[0] : d1[8], lo ? d1[1] : d1[9], lo ? d1[2] : d1[10], lo ? d1[3] : d1[11]};
  f32x4 k1 = {lo ? d1[4] : d1[12], lo ? d1[5] : d1[13], lo ? d1[6] : d1[14], lo ? d1[7] : d1[15]};
  const float ev6 = d3[0] * 1e-6f;
  k0 = add4(k0, f32x4{ev6, ev6, ev6, ev6}); k1 = add4(k1, f32x4{ev6, ev6, ev6, ev6});
  const float zp = d2[0];
  if (w >= 2) { *(LAS f32x4*)(S + (w - 2) * 2048 + lane * 16) = k0; *(LAS f32x4*)(S + (w - 2) * 2048 + 1024 + lane * 16) = k1; *(LAS float*)(S + 12288 + (w - 2) * 256 + lane * 4) = zp; }
  TS(13);
  vmbar<0>();
  TS(14);
  if (w < 2) {
    const u32 a = S + w * 2048 + lane * 16, z = S + 12288 + w * 256 + lane * 4;
    k0 = add4(add4(k0, ldsf4(a)), add4(ldsf4(a + 4096), ldsf4(a + 8192))); k1 = add4(add4(k1, ldsf4(a + 1024)), add4(ldsf4(a + 5120), ldsf4(a + 9216)));
    const float t = add1(add1(zp, ldsf(z)), add1(ldsf(z + 512), ldsf(z + 1024)));
    u32x4 p0 = __builtin_bit_cast(u32x4, k0), p1 = __builtin_bit_cast(u32x4, k1);
#pragma unroll
    for (int i = 0; i < 4; ++i) { p0[i] = umin(p0[i], ~1u); p1[i] = umin(p1[i], ~1u); }
    const u32 o = (hg * 2 + w) * 8 + rc;
    st128c(p0, lane * 16, rws, WS_KV + o * 2048); st128c(p1, lane * 16, rws, WS_KV + o * 2048 + 1024); st32c(umin(__float_as_uint(t), ~1u), col * 4, rws, WS_Z + o * 128);
  }
  TS(4);
  u32x4 pq[2][2], t[8]; u32x2 z; u32 pf;
  u32 qa = L + 98304 + (8 * hh + q4) * 64 + 32 * ((lane >> 4) & 1) + 8 * (lane & 3);
  asm volatile("" : "+v"(qa), "+v"(cf));
  u32x4 qf[4];
#pragma unroll
  for (int i = 0; i < 3; ++i) qf[i] = trd2(qa + i * 1024, qa + i * 1024 + 256);
  f32x16 cq = splat(1.000001f);
#pragma unroll
  for (int i = 0; i < 32; ++i) {
    SB();
    if (i < 29) qf[(i + 3) & 3] = trd2(qa + (i + 3) * 1024, qa + (i + 3) * 1024 + 256);
    if (QN >= 0) asm volatile("s_nop %0" ::"i"(QN < 0 ? 0 : QN));
    SB();
    cq = mfma(qf[i & 3], x0[i], cq);
  }
  SB();
  TS(18);
  auto sweep = [&]() {
#pragma unroll
    for (int r = 0; r < 8; ++r) ld128c<0>(t[r], lane * 16, rws, WS_KV + (pr * 8 + r) * 2048 + ni * 1024);
    asm volatile("buffer_load_dwordx2 %0, %1, %2, %3 offen sc1" : "=v"(z) : "v"((lane >> 3) * 128 + (lane & 7) * 8), "s"(rws), "s"(WS_Z + pr * 1024 + ni * 64) : "memory");
  };
  asm volatile("" : "=v"(t[0]), "=v"(t[1]), "=v"(t[2]), "=v"(t[3]), "=v"(t[4]), "=v"(t[5]), "=v"(t[6]), "=v"(t[7]), "=v"(z));
  if (ga && __builtin_amdgcn_ballot_w64(cf != tag) != 0) poll(cfoff, rws, tag);
  ld32c(pf, pfl, rws, 0);
  if (mi == 0) sweep();
  pq[0][0] = phi8<true>(cq, 0); pq[0][1] = phi8<true>(cq, 8);
  asm volatile("" : "+v"(pq[0][0]), "+v"(pq[0][1]));
  TS(5);
  w_stage(w_addr(wa0), wr, 0, 12);
  SB();
  if (mi == 1) sweep();
  if (!ga) vmwait<0>();
  asm volatile("s_waitcnt vmcnt(9)" : "+v"(pf)::"memory");
  if (__builtin_amdgcn_ballot_w64(pf != tag) != 0) poll(pfl, rws, tag);
  ld128c<0>(pq[1][0], lane * 16, rws, WS_P + (rc * 8 + w) * 16384 + hg * 2048); ld128c<1024>(pq[1][1], lane * 16, rws, WS_P + (rc * 8 + w) * 16384 + hg * 2048);
  TS(15);
#define GW(N) asm volatile("s_waitcnt vmcnt(%9)" : "+v"(t[0]), "+v"(t[1]), "+v"(t[2]), "+v"(t[3]), "+v"(t[4]), "+v"(t[5]), "+v"(t[6]), "+v"(t[7]), "+v"(z) : "i"(N) : "memory")
  GW(2);
  if (ga) {
    for (int it = 0; it < SPIN_MAX; ++it) {
      u32 m = umax(z[0], z[1]);
#pragma unroll
      for (int r = 0; r < 8; ++r) m = umax(umax(umax(m, t[r][0]), t[r][1]), umax(t[r][2], t[r][3]));
      if (__builtin_amdgcn_ballot_w64(m == ~0u) == 0) break;
      sweep();
      GW(0);
    }
    TS(16);
    f32x4 a = __builtin_bit_cast(f32x4, t[0]);
#pragma unroll
    for (int r = 1; r < 8; ++r) a += __builtin_bit_cast(f32x4, t[r]);
    float zs = fold32(__uint_as_float(z[0]), __uint_as_float(z[1]));
    zs = dppadd<0x128>(fold16(zs, zs));
    const int pl = mi, zf = 2 * (lane & 7) + hh;
    *(LAS u32x2*)(KT + pl * 1024 + lane * 16 + ni * 8) = u32x2{pk(a[0], a[1]), pk(a[2], a[3])};
    *(LAS unsigned short*)(ZT + pl * 64 + ni * 32 + ((zf >> 2) & 1) * 16 + ((zf & 3) + 4 * (zf >> 3)) * 2) = (unsigned short)pk(zs, zs);
  }
  bar();
  TS(6);
  f32x16 dd[2][2];
#pragma unroll
  for (int j = 0; j < 2; ++j) {
    if (j) asm volatile("s_waitcnt vmcnt(0)" : "+v"(pq[1][0]), "+v"(pq[1][1])::"memory");
#pragma unroll
    for (int h = 0; h < 2; ++h) dd[j][h] = mfma(lds128(((col < 16) == (h == 0)) ? KT + j * 1024 + lane * 16 : ZT + j * 64 + h * 32 + hh * 16), pq[j][h], splat(0.f));
  }
  SB();
#pragma unroll
  for (int c = 3; c < 3 + NE; ++c) w_issue<1>(s2, c);
  SB();
#pragma unroll
  for (int j = 0; j < 2; ++j)
#pragma unroll
    for (int h = 0; h < 2; ++h) {
      const int o = 8 * h;
      const f32x16& e = dd[j][h];
      const float rd = __builtin_amdgcn_rcpf(e[8 - o] + 1e-6f);
      const u32x4 ov = {pk(e[o] * rd, e[o + 1] * rd), pk(e[o + 2] * rd, e[o + 3] * rd), pk(e[o + 4] * rd, e[o + 5] * rd), pk(e[o + 6] * rd, e[o + 7] * rd)};
      st128c(ov, lane * 16, rws, WS_O + (rc * 8 + w) * 32768 + ((hg + 8 * j) * 2 + h) * 1024);
    }
  TS(17);
  bar();
  w_flag<3 + NE>(s2, w, tag, lane < 2 ? ((rc & 6) + lane) * FRS : OOB, rws, WS_F + (512 + rc * 16 + hg) * 128);
  return tag;
}

__global__ __launch_bounds__(512, 2) void fused_kernel(const char* __restrict__ x, const char* __restrict__ wqkv, const char* __restrict__ wo,
                                                        const char* __restrict__ w1, const char* __restrict__ b1, const char* __restrict__ w2,
                                                        const char* __restrict__ b2, const char* __restrict__ gw, char* __restrict__ out, char* ws TSK) {
  __shared__ __attribute__((aligned(1024))) char sm[163840];
  TSBEG; TS(0);
  u32 ev;
  {
    unsigned long long pc; u32 cn;
    asm volatile("s_getpc_b64 %0\n.Lfk_pc:\n\ts_mov_b32 %1, .Lfk_end-.Lfk_pc" : "=s"(pc), "=s"(cn));
    const u32 t = threadIdx.x, b = blockIdx.x >> 3;
    const u32 vo = CW == 1 ? (t < 64 ? (b * 64 + t) * 128 : OOB) : CW == 2 ? (t < 64 ? ((b - 16) * 64 + t) * 128 : OOB) : CW == 3 ? (t < 9 ? (b * 9 + t) * 128 : OOB) : CW == 4 ? (b == 0 || b == 16 ? t * 128 : OOB) : CW == 5 ? ((b & 7) == 0 ? t * 128 : OOB) : CW == 6 ? (b == 0 ? t * 128 : OOB) : (b < 2 ? t * 128 : OOB);
    asm volatile("buffer_load_dword %0, %1, %2, 0 offen" : "=v"(ev) : "v"(vo), "s"(srd((const void*)pc, cn)) : "memory");
    SB();
  }
  asm volatile("" : "+s"(x), "+s"(wqkv), "+s"(wo), "+s"(w1), "+s"(b1), "+s"(w2), "+s"(b2), "+s"(gw), "+s"(out), "+s"(ws));
  const u32 L = (u32)(size_t)(LAS char*)sm;
  const int lane = threadIdx.x & 63, w = __builtin_amdgcn_readfirstlane(threadIdx.x >> 6);
  int xcd = blockIdx.x & 7, slot = blockIdx.x >> 3;
  const int hh = lane >> 5;
  const u32x4 rws = srd(ws, WS_END);
  u32x4 xr[2], gr;
  asm volatile("s_nop 4" : "=v"(xr[0]), "=v"(xr[1]), "=v"(gr)::"memory");
  ev_load(ev, rws);
  if (slot < 22) {
    int ft, dt1;
    const WSrc s1 = w1_src(w1, L + w * 4096, lane, w, xcd, slot, ft, dt1);
    if (slot < 16) {
      const int r = (xcd * 16 + slot) * 16 + w;
      const u32x4 rx = srd(x, 2048 * 1024);
      ld128(xr[0], lane * 16, rx, r * 1024); ld128(xr[1], lane * 16, rx, (r + 8) * 1024);
      ld128(gr, lane * 16, srd(gw, 1024), 0);
      asm volatile("s_barrier" ::: "memory");
    }
    w_issue<0>(s1, 0);
  } else {
    const ASrc sa = a_src(x, wqkv, L, lane, w, xcd, slot);
#pragma unroll
    for (int j = 0; j < 6; ++j) a_one(sa, 0, j);
    asm volatile("s_barrier" ::: "memory");
#pragma unroll
    for (int j = 0; j < 6; ++j) a_one(sa, 1, j);
  }
  asm volatile("" : "+s"(slot), "+s"(xcd));
  const int g = w >> 1, hf = w & 1, q4 = (lane & 15) >> 2;
  const u32 wch = (lane & 7) ^ ((lane >> 2) & 4);
  const u32 wa0 = L + g * 8192 + (4 * hh + q4) * 128 + 64 * (q4 >> 1) + 32 * ((lane >> 4) & 1) + 8 * (lane & 3);

  const int dt = (xcd & 1) * 4 + (slot & 3), stl = (xcd >> 1) * 8 + (slot >> 2);
  WSrc s2;
  s2.rs = g < 3 ? srd(w2, 1376 * 4096) : srd(wo, 512 * 1024); s2.stride = g < 3 ? 4096 : 1024; s2.lds = L + g * 8192 + hf * 4096;
  s2.voff = (lane >> 3) * s2.stride + wch * 16;
  s2.sbase = g < 3 ? (g * 512 + hf * 32) * 4096 + stl * 128 : hf * 32 * 1024 + dt * 128;
  s2.rot = 2 * ((slot + (slot >> 2)) & R2);
  u32 tag;
  u32x4 wr[16][2];

  if (slot >= 30) tag = attn<false>(ev, x, wqkv, rws, s2, L, lane, w, xcd, slot, wa0, wr TSA);
  else if (slot >= 22) tag = attn<true>(ev, x, wqkv, rws, s2, L, lane, w, xcd, slot, wa0, wr TSA);
  else {
    const int gi = slot < 16 ? xcd * 16 + slot : 112 + xcd * 6 + slot;
    int ft, dt1;
    const WSrc s1 = w1_src(w1, s2.lds, lane, w, xcd, slot, ft, dt1);
    if (slot < 16) {
      const int sb = xcd * 16 + slot;
      tag = tag_wait<4>(ev);
      asm volatile("" : "+v"(xr[0]), "+v"(xr[1]), "+v"(gr)::"memory");
      TS(1);
      f32x2 v[2][4], t; float s[2], gd[2], q[2];
#pragma unroll
      for (int j = 0; j < 2; ++j) {
#pragma unroll
        for (int i = 0; i < 4; ++i) v[j][i] = f32x2{blo(xr[j][i]), bhi(xr[j][i])};
        t = (v[j][0] + v[j][1]) + (v[j][2] + v[j][3]); s[j] = t[0] + t[1];
      }
      const float t0 = fold32(s[0], s[1]), t1 = rowsum(fold16(t0, t0)) * (1.f / 512.f);
#pragma unroll
      for (int j = 0; j < 2; ++j) {
        t = f32x2{0.f, 0.f};
#pragma unroll
        for (int i = 0; i < 4; ++i) t = __builtin_elementwise_fma(v[j][i], f32x2{blo(gr[i]), bhi(gr[i])}, t);
        gd[j] = t[0] + t[1];
        const float m = rdl(t1, 32 * j);
        t = f32x2{0.f, 0.f};
#pragma unroll
        for (int i = 0; i < 4; ++i) { v[j][i] -= m; t = __builtin_elementwise_fma(v[j][i], v[j][i], t); }
        q[j] = t[0] + t[1];
      }
      const float t2 = rowsum(fold16(fold32(q[0], q[1]), fold32(gd[0], gd[1])));
      const float rs = __builtin_amdgcn_rsqf(t2 * (1.f / 512.f) + 1e-5f);
#pragma unroll
      for (int j = 0; j < 2; ++j) {
        const float r = rdl(rs, 32 * j);
#pragma unroll
        for (int i = 0; i < 4; ++i) v[j][i] *= r;
        *(LAS u32x4*)(L + (w + 8 * j) * 1088 + lane * 16) = u32x4{pk(v[j][0][0], v[j][0][1]), pk(v[j][1][0], v[j][1][1]), pk(v[j][2][0], v[j][2][1]), pk(v[j][3][0], v[j][3][1])};
      }
      const float gt = __builtin_amdgcn_rcpf(1.f + __builtin_amdgcn_exp2f(t2 * -1.4426950408889634f));
      TS(13);
      bar();
      const u32 ta = L + (4 * hh + q4) * 1088 + w * 128 + 32 * ((lane >> 4) & 1) + 8 * (lane & 3);
      const u32x4 h0 = trd2(ta, ta + 8 * 1088), h1 = trd2(ta + 64, ta + 64 + 8 * 1088);
      st32c(__float_as_uint(gt), (lane & 31) == 16 ? lane & 32 : OOB, rws, WS_G + (sb * 16 + w) * 4);
      st128c(h0, lane * 16, rws, WS_H + w * 262144 + sb * 1024);
      st128c(h1, lane * 16, rws, WS_H + w * 262144 + 131072 + sb * 1024);
      asm volatile("s_barrier" ::: "memory");
      TS(14);
      w_issue<0>(s1, 1);
      TS(15);
      vmbar<4>();
    } else {
      u32 tl;
      asm volatile("buffer_load_dword %0, %1, %2, 0 offen" : "=v"(tl) : "v"((lane << TL) + 2048),
                   "s"(w == 0 ? srd(w2, 1376 * 4096) : w == 1 ? srd(wo, 512 * 1024) : w == 2 ? srd(out, 2048 * 1024) : w == 3 ? srd(b1, 2752) : w == 4 ? srd(b2, 4096) : srd(ws, w == 5 ? WS_END : 0)) : "memory");
      w_issue<0>(s1, 1);
      tag = tag_wait<4>(ev);
      asm volatile("" ::"v"(tl));

      bar();
    }
    TS(2);
    const WA a0 = w_addr(wa0);
    if (w == 0) st32c(tag, lane < 8 ? lane * FRS : OOB, rws, WS_F + (slot < 16 ? gi : gi + 80) * 128);
    w_round<0, 0, 2>(s1, a0, wr); w_issue<0>(s1, 3);
    TS(18);
    vmbar0<8>(w);
    TS(19);
    w_round<0, 1, 4>(s1, a0, wr);
    vmbar<8>();
    TS(20);
    w_round<0, 2, 5>(s1, a0, wr);
    vmbar<8>();
    TS(21);
    const u32 hfl = hf ? WS_F + xcd * FRS + (g * 32 + (lane & 31)) * 128 : OOB;
    u32 pf; u32x2 bb0, bb1;
    ld32c(pf, hfl, rws, 0);
    asm volatile("buffer_load_dwordx2 %0, %2, %3, %4 offen\n\tbuffer_load_dwordx2 %1, %2, %3, %4 offen offset:16" : "=&v"(bb0), "=&v"(bb1) : "v"(hh * 8), "s"(srd(b1, 2752)), "s"(ft * 128 + g * 32) : "memory");
    w_round<0, 3, 6>(s1, a0, wr); w_issue<0>(s1, 7);
    TS(3);
    asm volatile("s_waitcnt vmcnt(8)" : "+v"(pf), "+v"(bb0), "+v"(bb1)::"memory");
    if (hf && __builtin_amdgcn_ballot_w64(pf != tag) != 0) poll(hfl, rws, tag);
    bar();
    TS(4);
    f32x16 c0, c1;
    wr_gemm<0>(wr, a0, DSrc{rws, (u32)lane * 16, WS_H + (dt1 * 2 + hf) * 131072 + g * 32768, s1.rot}, s2, 0, true, c0, c1);
    TS(5);
    const u32 R = L + 65536 + hf * 32768 + lane * 16;
    f32x4 lo = {blo(bb0[0]), bhi(bb0[0]), blo(bb0[1]), bhi(bb0[1])}, hi = {blo(bb1[0]), bhi(bb1[0]), blo(bb1[1]), bhi(bb1[1])};
#pragma unroll
    for (int q = 0; q < 4; ++q) {
      const f32x16& c = q < 2 ? c0 : c1; const int o = (q & 1) * 8;
      const f32x4 a = {c[o], c[o + 1], c[o + 2], c[o + 3]}, b = {c[o + 4], c[o + 5], c[o + 6], c[o + 7]};
      if (q == g) { lo += a; hi += b; }
      else { const u32 t = R + q * 8192 + ((g - q) & 3) * 2048; *(LAS f32x4*)t = a; *(LAS f32x4*)(t + 1024) = b; }
    }
    TS(16);
    vmbar<0>();
    TS(17);
    if (SO && w >= 4) { w_stage(a0, wr, 0, 12); SB(); }
#pragma unroll
    for (int k = 1; k < 4; ++k) { lo += ldsf4(R + g * 8192 + k * 2048); hi += ldsf4(R + g * 8192 + k * 2048 + 1024); }
    const u32x4 uv = {pk(gelu(lo[0]), gelu(lo[1])), pk(gelu(lo[2]), gelu(lo[3])), pk(gelu(hi[0]), gelu(hi[1])), pk(gelu(hi[2]), gelu(hi[3]))};
    st128c(uv, lane * 16, rws, WS_U + (dt1 * 2 + hf) * UDB + (ft * 4 + g) * 1024);
    TS(6);
    if (!SO || w < 4) w_stage(a0, wr, 0, 12);
    bar();
    TS(22);
    w_flag<3>(s2, w, tag, lane < 4 ? (lane * 2 + (dt1 >> 2)) * FRS : OOB, rws, WS_F + (256 + dt1 * 32 + ft) * 128);
  }

  {
    TS(7);
    asm volatile("" : "+s"(s2.lds), "+s"(s2.rot), "+s"(s2.sbase));
    const int sr = w * 8 + (lane >> 3), cq = lane & 7;
    u32 b2v;
    asm volatile("buffer_load_ushort %0, %1, %2, %3 offen" : "=v"(b2v) : "v"(sr * 2), "s"(srd(b2, 4096)), "s"(stl * 128) : "memory");
    const u32 GT = L + 131072, SG = GT + 256;
    const WA a0 = w_addr(wa0);
    f32x16 c0, c1;
    const int nf = g == 2 ? 6 : 8;
    const u32 fo = WS_F + xcd * FRS + ((g < 3 ? 256 + dt * 32 + g * 8 : 512 + (stl >> 2) * 16) + (lane < nf ? lane : 0)) * 128;
    const bool early = EP && slot >= 22 && slot < 30 && (EP == 2 || g < 3);
    u32 pf;
    if (PP == 2) ld32c(pf, early ? fo : OOB, rws, 0);
    w_issue<1>(s2, 6);
    if (PP == 1) ld32c(pf, early ? fo : OOB, rws, 0);
    w_issue<1>(s2, 7);
    TS(24);
    vmbar0<18>(w);
    w_round<1, 3, 8>(s2, a0, wr);
    bar();
    TS(25);
    *(volatile LAS u32*)(SG + w * 4) = 0;
    TS(8);
    asm volatile("s_waitcnt vmcnt(%2)" : "+v"(pf), "+v"(b2v) : "i"(4 * PP) : "memory");
    const bool ok = early && __builtin_amdgcn_ballot_w64(pf != tag) == 0;
    if (!ok) poll_bar(fo, rws, tag);
    TS(9);
    u32 gv;
    ld32c(gv, w == 0 ? lane * 4 : OOB, rws, WS_G + stl * 256);
    const u32 ub = WS_U + (dt * 2 + hf) * UDB;
    wr_gemm<1>(wr, a0, DSrc{srd(ws, g < 3 ? ub + 86 * 1024 : WS_END), (u32)lane * 16, g < 3 ? ub + g * 32768 : WS_O + (stl * 2 + hf) * 32768, s2.rot}, s2, SG + w * 4, ok, c0, c1);
    asm volatile("" : "+v"(gv));
    TS(10);
    if (w == 0) *(LAS u32*)(GT + lane * 4) = gv;
    for (int it = 0; it < SPIN_MAX * 16 && *(volatile LAS u32*)(SG + (w ^ 1) * 4) == 0; ++it) {}
    const u32 pb = L + g * 8192 + hf * 32768;
    if (g < 3) {
      asm volatile("s_mov_b32 m0, %0\n\ts_nop 15\n\ts_nop 7" ::"s"(pb + hf * 128) : "memory");
      put16<0, 0>(c0);
      asm volatile("s_mov_b32 m0, %0\n\ts_nop 0" ::"s"(pb + hf * 128 + 6144) : "memory");
      put16<65536 - 6144, 0>(c1);
    } else red_write<RSL>(pb + lane * 16, 65536, c0, c1);
    bar();
    TS(11);
    const int sl = lane >> 3, i = (sl & 3) + 4 * (w & 3);
    const u32 ya = L + (cq >> 2) * (32768 + 128) + (w >> 2) * 65536 + (i >> 1) * 768 + (i & 1) * 272 + (sl >> 2) * 128 + (cq & 3) * 32;
    const f32x4 y0 = (ldsf4(ya) + ldsf4(ya + 8192)) + ldsf4(ya + 16384), y1 = (ldsf4(ya + 16) + ldsf4(ya + 8208)) + ldsf4(ya + 16400);
    const u32 aa = L + 24576 + (w >> 2) * 32768 + (cq >> 2) * 65536 + (cq & 3) * RSL + (sr & 31) * 16;
    const f32x4 a0v = ldsf4(aa), a1v = ldsf4(aa + 512);
    const float gt = ldsf(GT + sr * 4), bb = blo(b2v), og = 1.f - gt;
    u32x4 ov;
    ov[0] = pk(gt * a0v[0] + og * (y0[0] + bb), gt * a0v[1] + og * (y0[1] + bb));
    ov[1] = pk(gt * a0v[2] + og * (y0[2] + bb), gt * a0v[3] + og * (y0[3] + bb));
    ov[2] = pk(gt * a1v[0] + og * (y1[0] + bb), gt * a1v[1] + og * (y1[1] + bb));
    ov[3] = pk(gt * a1v[2] + og * (y1[2] + bb), gt * a1v[3] + og * (y1[3] + bb));
    asm volatile("buffer_store_dwordx4 %0, %1, %2, %3 offen " OSM ::"v"(ov), "v"(sr * 1024 + cq * 16), "s"(srd(out, 2048 * 1024)), "s"(stl * 65536 + dt * 128) : "memory");
    TS(12); TS(26); TSEND;
    asm volatile(".Lfk_end:");
  }
}

extern "C" void kernel_launch(void* const* d_in, const int* in_sizes, int n_in, void* d_out, int out_size, void* d_ws, size_t ws_size, hipStream_t stream) {
  fused_kernel<<<256, 512, 0, stream>>>((const char*)d_in[0], (const char*)d_in[1], (const char*)d_in[2], (const char*)d_in[4], (const char*)d_in[5],
                                        (const char*)d_in[6], (const char*)d_in[7], (const char*)d_in[8], (char*)d_out, (char*)d_ws);
}
```

```cpp
#include <hip/hip_runtime.h>
#include <hip/hip_bf16.h>

#pragma clang diagnostic ignored "-Wint-to-pointer-cast"
#define DI __device__ __forceinline__
#define LAS __attribute__((address_space(3)))
#define SB() __builtin_amdgcn_sched_barrier(0)
typedef unsigned u32;
typedef u32 u32x4 __attribute__((ext_vector_type(4)));
typedef u32 u32x2 __attribute__((ext_vector_type(2)));
typedef float f32x2 __attribute__((ext_vector_type(2)));
typedef float f32x4 __attribute__((ext_vector_type(4)));
typedef float f32x16 __attribute__((ext_vector_type(16)));
typedef __bf16 bf16x8 __attribute__((ext_vector_type(8)));
typedef __bf16 bf16x2 __attribute__((ext_vector_type(2)));
typedef short s16x4 __attribute__((ext_vector_type(4)));
typedef short s16x2 __attribute__((ext_vector_type(2)));

constexpr u32 WS_E = 0, WS_G = 32768;
constexpr u32 WS_F = 7u << 20, FRS = 641 * 128;
constexpr u32 WS_KV = 65536, WS_Z = WS_KV + 262144, WS_H = 1u << 20, WS_U = 3u << 20, WS_O = 5u << 20, WS_P = 8u << 20, WS_END = 9u << 20, OOB = 1u << 30;
constexpr u32 UDB = 88 * 1024;
constexpr int SPIN_MAX = 20000;
#define DSC "sc1"
#define R1 7
#define R2 3
#define RA 6
#define NF 1
#define NE 1
#define QP 16
#define GI 0
#define QPR 3
#define QN -1
#define PM 0
#define SPR 1
#define PP 2
#define SO 0
#define EP 0
#define KA 1
#define CW 4
#define OS 2
#define OSM "nt"
#define PFW 0
#define VB 0
#define KP 0
#define TL 18
#define NP1 8
#define NP2 8
#define SLP 4
#define MAP 0
#define AGAP {1, 3, 6, 9, 11, 14}
#define PRIO 3
#define WIN 10

#define TS(i)
#define TSP
#define TSA
#define TSK
#define TSBEG
#define TSEND

DI u32x4 srd(const void* p, u32 bytes) { const unsigned long long a = (unsigned long long)p; return u32x4{(u32)a, (u32)(a >> 32), bytes, 0x00020000u}; }
DI f32x16 mfma(u32x4 a, u32x4 b, f32x16 c) { return __builtin_amdgcn_mfma_f32_32x32x16_bf16(__builtin_bit_cast(bf16x8, a), __builtin_bit_cast(bf16x8, b), c, 0, 0, 0); }
DI f32x4 mfma16(u32x4 a, u32x4 b, f32x4 c) { return __builtin_amdgcn_mfma_f32_16x16x32_bf16(__builtin_bit_cast(bf16x8, a), __builtin_bit_cast(bf16x8, b), c, 0, 0, 0); }
DI u32x2 trd(u32 a) { return __builtin_bit_cast(u32x2, __builtin_amdgcn_ds_read_tr16_b64_v4i16((LAS s16x4*)a)); }
DI u32x4 trd2(u32 a, u32 b) { const u32x2 x = trd(a), y = trd(b); return u32x4{x[0], x[1], y[0], y[1]}; }
DI u32x4 lds128(u32 a) { return *(LAS u32x4*)a; }
DI f32x4 ldsf4(u32 a) { return *(LAS f32x4*)a; }
DI float ldsf(u32 a) { return *(LAS float*)a; }
DI u32 pk(float a, float b) { return __builtin_bit_cast(u32, __builtin_convertvector(f32x2{a, b}, bf16x2)); }
DI float blo(u32 v) { return __uint_as_float(v << 16); }
DI float bhi(u32 v) { return __uint_as_float(v & 0xffff0000u); }
DI u32x4 pack8(const f32x16& x, int o) { return u32x4{pk(x[o], x[o + 1]), pk(x[o + 2], x[o + 3]), pk(x[o + 4], x[o + 5]), pk(x[o + 6], x[o + 7])}; }
DI f32x16 splat(float v) { return f32x16{v, v, v, v, v, v, v, v, v, v, v, v, v, v, v, v}; }
DI float add1(float a, float b) { float r; asm("v_add_f32 %0, %1, %2" : "=v"(r) : "v"(a), "v"(b)); return r; }
DI f32x4 add4(f32x4 a, f32x4 b) { return f32x4{add1(a[0], b[0]), add1(a[1], b[1]), add1(a[2], b[2]), add1(a[3], b[3])}; }
DI u32 umin(u32 a, u32 b) { return a < b ? a : b; }
DI u32 umax(u32 a, u32 b) { return a > b ? a : b; }

DI void glds(u32 lds, u32 voff, u32x4 rs, u32 soff) {
  asm volatile("s_mov_b32 m0, %0\n\ts_nop 0\n\tbuffer_load_dwordx4 %1, %2, %3 offen lds" ::"s"(lds), "v"(voff), "s"(rs), "s"(soff) : "memory");
}
template <int OFF> DI void ld128c(u32x4& d, u32 voff, u32x4 rs, u32 soff) {
  asm volatile("buffer_load_dwordx4 %0, %1, %2, %3 offen offset:%4 " DSC : "=v"(d) : "v"(voff), "s"(rs), "s"(soff), "i"(OFF) : "memory");
}
DI void ld128(u32x4& d, u32 voff, u32x4 rs, u32 soff) { asm volatile("buffer_load_dwordx4 %0, %1, %2, %3 offen" : "=v"(d) : "v"(voff), "s"(rs), "s"(soff) : "memory"); }
template <int OFF = 0> DI void ld32c(u32& d, u32 voff, u32x4 rs, u32 soff) { asm volatile("buffer_load_dword %0, %1, %2, %3 offen offset:%4 sc1" : "=v"(d) : "v"(voff), "s"(rs), "s"(soff), "i"(OFF) : "memory"); }
DI void st128c(u32x4 v, u32 voff, u32x4 rs, u32 soff) { asm volatile("buffer_store_dwordx4 %0, %1, %2, %3 offen sc1\n\ts_nop 1" ::"v"(v), "v"(voff), "s"(rs), "s"(soff) : "memory"); }
DI void st32c(u32 v, u32 voff, u32x4 rs, u32 soff) { asm volatile("buffer_store_dword %0, %1, %2, %3 offen sc1" ::"v"(v), "v"(voff), "s"(rs), "s"(soff) : "memory"); }
template <int N> DI void vmwait() { asm volatile("s_waitcnt vmcnt(%0)" ::"i"(N) : "memory"); }
template <int N> DI void vmwait(u32x4& a) { asm volatile("s_waitcnt vmcnt(%1)" : "+v"(a) : "i"(N) : "memory"); }
DI void bar() { asm volatile("s_waitcnt lgkmcnt(0)\n\ts_barrier" ::: "memory"); }
template <int N> DI void vmbar() { asm volatile("s_waitcnt vmcnt(%0) lgkmcnt(0)\n\ts_barrier" ::"i"(N) : "memory"); }

DI void ev_load(u32& e, u32x4 ws) { asm volatile("buffer_load_dword %0, off, %1, 0" : "+v"(e) : "s"(ws) : "memory"); }
template <int N> DI u32 tag_wait(u32 e) {
  u32 t;
  asm volatile("s_waitcnt vmcnt(%2)\n\tv_readfirstlane_b32 %0, %1\n\ts_nop 1" : "=s"(t) : "v"(e), "i"(N) : "memory");
  return t * 1664525u + 1013904223u;
}
DI void poll(u32 voff, u32x4 ws, u32 tag) {
  for (int it = 0; it < SPIN_MAX; ++it) {
    u32 f;
    asm volatile("buffer_load_dword %0, %1, %2, 0 offen sc1\n\ts_waitcnt vmcnt(0)" : "=&v"(f) : "v"(voff), "s"(ws) : "memory");
    if (__builtin_amdgcn_ballot_w64(f != tag) == 0) break;
    __builtin_amdgcn_s_sleep(SLP);
  }
}
DI void poll_bar(u32 voff, u32x4 ws, u32 tag) {
  u32 f;
  asm volatile("buffer_load_dword %0, %1, %2, 0 offen sc1\n\ts_waitcnt vmcnt(0) lgkmcnt(0)\n\ts_barrier" : "=&v"(f) : "v"(voff), "s"(ws) : "memory");
  if (__builtin_amdgcn_ballot_w64(f != tag) != 0) poll(voff, ws, tag);
}
template <int C> DI float dppadd(float v) { return v + __int_as_float(__builtin_amdgcn_update_dpp(0, __float_as_int(v), C, 0xf, 0xf, true)); }
DI float rowsum(float v) { v = dppadd<0xB1>(v); v = dppadd<0x4E>(v); v = dppadd<0x141>(v); return dppadd<0x140>(v); }
DI float rdl(float v, int l) { return __int_as_float(__builtin_amdgcn_readlane(__float_as_int(v), l)); }
DI float fold32(float a, float b) { const auto r = __builtin_amdgcn_permlane32_swap(__float_as_uint(a), __float_as_uint(b), false, false); return __uint_as_float(r[0]) + __uint_as_float(r[1]); }
DI float fold16(float a, float b) { const auto r = __builtin_amdgcn_permlane16_swap(__float_as_uint(a), __float_as_uint(b), false, false); return __uint_as_float(r[0]) + __uint_as_float(r[1]); }
template <bool E> DI u32 phi2(float a, float b) {
  constexpr float l2e = 1.4426950408889634f, c = -l2e * (E ? 1.000001f : 1.f);
  float e0 = __builtin_amdgcn_fmed3f(__builtin_amdgcn_exp2f(__builtin_fmaf(a, l2e, c)), 0.f, 1.f), e1 = __builtin_amdgcn_fmed3f(__builtin_amdgcn_exp2f(__builtin_fmaf(b, l2e, c)), 0.f, 1.f);
  if (E) { e0 += 1e-6f; asm("" : "+v"(e0)); e1 += 1e-6f; }
  return pk(__builtin_amdgcn_fmed3f(a, e0, 3.4028234664e38f), __builtin_amdgcn_fmed3f(b, e1, 3.4028234664e38f));
}
template <bool E> DI u32x4 phi8(const f32x16& x, int o) { return u32x4{phi2<E>(x[o], x[o + 1]), phi2<E>(x[o + 2], x[o + 3]), phi2<E>(x[o + 4], x[o + 5]), phi2<E>(x[o + 6], x[o + 7])}; }
DI float gelu(float v) {
  const float t = v * (-2.3022081981f - 0.1029432397f * v * v);
  return v * __builtin_amdgcn_rcpf(1.f + __builtin_amdgcn_exp2f(t));
}

constexpr int wslot(int P, int c) { return (c + (P && c < 3 ? 4 : 1)) % 5; }
struct WSrc { u32x4 rs; u32 voff, sbase, stride, lds, rot; };
template <int P> DI void w_one(const WSrc& s, int c, int i) { glds(s.lds + wslot(P, c) * 32768 + i * 1024, s.voff, s.rs, s.sbase + (64 * ((c + s.rot) & 7) + 8 * i) * s.stride); }
template <int P> DI void w_issue(const WSrc& s, int c) {
#pragma unroll
  for (int i = 0; i < 4; ++i) w_one<P>(s, c, i);
}
struct WA { u32 a[3][2]; };
DI WA w_addr(u32 a0) { WA r;
#pragma unroll
  for (int i = 0; i < 3; ++i) { r.a[i][0] = a0 + i * 65536; r.a[i][1] = (a0 ^ 64) + i * 65536; asm volatile("" : "+v"(r.a[i][0]), "+v"(r.a[i][1])); }
  return r; }
template <int P> DI void w_frag(const WA& r, u32x4 (&f)[2], int c, int ks) {
#pragma unroll
  for (int b = 0; b < 2; ++b) { const u32 a = r.a[wslot(P, c) >> 1][b] + (wslot(P, c) & 1) * 32768 + ks * 2048; f[b] = trd2(a, a + 1024); }
}
template <int P, int C, int F> DI void w_round(const WSrc& s, const WA& a0, u32x4 (&wr)[16][2]) {
#pragma unroll
  for (int k = 0; k < 4; ++k) { w_frag<P>(a0, wr[4 * C + k], C, k); SB(); if constexpr (F < 8) w_one<P>(s, F, k); SB(); }
}
DI void w_stage(const WA& a0, u32x4 (&wr)[16][2], int k0, int k1) {
#pragma unroll
  for (int k = k0; k < k1; ++k) w_frag<1>(a0, wr[k], k >> 2, k & 3);
}
template <int C0> DI void w_flag(const WSrc& s, int w, u32 tag, u32 fv, u32x4 rws, u32 flag) {
  if constexpr (C0 < 6) w_issue<1>(s, C0);
  vmbar<(C0 < 6 ? 4 : 0)>();
  if (w == 0) st32c(tag, fv, rws, flag);
#pragma unroll
  for (int c = C0 + 1; c < 6; ++c) w_issue<1>(s, c);
}
template <int N> DI void vmbar0(int w) { if (w == 0) vmwait<N + 1>(); else vmwait<N>(); bar(); }
struct DSrc { u32x4 rs; u32 voff, sb, rot; };
template <int I> DI void d_issue(u32x4 (&d)[32], const DSrc& s) { ld128c<(I & 3) * 1024>(d[I], s.voff, s.rs, s.sb + (((I >> 2) + s.rot) & 7) * 4096); }
constexpr int KB = 32 - WIN;
template <int P, int K> DI void wr_steps(u32x4 (&d)[32], u32x4 (&wr)[16][2], const WA& a0, const DSrc& s, const WSrc& nx, u32 sig, bool late, f32x16& c0, f32x16& c1) {
  if constexpr (K < 32) {
    if constexpr (K == KB) {
      if constexpr (P == 0) bar();
      else { asm volatile("s_waitcnt lgkmcnt(0)" ::: "memory"); *(volatile LAS u32*)sig = 1; }
    }
    if constexpr (K % 8 == 0) __builtin_amdgcn_s_setprio(3 - K / 8);
    vmwait<(P == 0 || K < KB ? WIN - 1 : 31 - K)>(d[K]);
    c0 = mfma(wr[K & 15][0], d[K], c0); c1 = mfma(wr[K & 15][1], d[K], c1);
    asm volatile("" : "+v"(c0), "+v"(c1));
    if constexpr (K + WIN < 32) d_issue<K + WIN>(d, s);
    if constexpr (K == 1) { if (late) bar(); }
    if constexpr (P == 0 && K >= KB) w_one<1>(nx, (K - KB) >> 2, (K - KB) & 3);
    if constexpr (K >= 1 && K <= 16) w_frag<P>(a0, wr[K - 1], 4 + ((K - 1) >> 2), (K - 1) & 3);
    wr_steps<P, K + 1>(d, wr, a0, s, nx, sig, late, c0, c1);
  }
}
template <int I> DI void d_first(u32x4 (&d)[32], const DSrc& s) { if constexpr (I < WIN) { d_issue<I>(d, s); d_first<I + 1>(d, s); } }
template <int P> DI void wr_gemm(u32x4 (&wr)[16][2], const WA& a0, const DSrc& s, const WSrc& nx, u32 sig, bool late, f32x16& c0, f32x16& c1) {
  u32x4 d[32];
  d_first<0>(d, s);
  c0 = splat(0.f); c1 = splat(0.f);
  wr_steps<P, 0>(d, wr, a0, s, nx, sig, late, c0, c1);
  if constexpr (P == 0) {
#pragma unroll
    for (int i = WIN; i < 12; ++i) w_one<1>(nx, i >> 2, i & 3);
  }
}
constexpr u32 RSL = 1088;
template <u32 RS> DI void red_write(u32 a, u32 bs, const f32x16& c0, const f32x16& c1) {
#pragma unroll
  for (int q = 0; q < 4; ++q) {
    *(LAS f32x4*)(a + q * RS) = f32x4{c0[4 * q], c0[4 * q + 1], c0[4 * q + 2], c0[4 * q + 3]};
    *(LAS f32x4*)(a + bs + q * RS) = f32x4{c1[4 * q], c1[4 * q + 1], c1[4 * q + 2], c1[4 * q + 3]};
  }
}

template <int O, int I> DI void put16(const f32x16& c) {
  if constexpr (I < 16) { asm volatile("ds_write_addtid_b32 %0 offset:%1" ::"v"(c[I]), "i"(O + (I >> 1) * 768 + (I & 1) * 272) : "memory"); put16<O, I + 1>(c); }
}

constexpr u32 AST = 49152;
struct ASrc { u32x4 rx, rw; u32 vx0, vx1, vw, vq, sx, s0, s1, sq, rot, L; int w; };
DI void a_one(const ASrc& s, int c, int j) {
  const u32 st = s.L + (c % 3) * AST, cc = (c + s.rot) & 7;
  if (j < 4) glds(st + s.w * 4096 + j * 1024, (j & 1) ? s.vx1 : s.vx0, s.rx, s.sx + j * 8192 + cc * 128);
  else glds(st + 32768 + (j - 4) * 8192 + s.w * 1024, s.vw, s.rw, (j == 4 ? s.s0 : s.s1) + cc * (64 * 3072));
}
template <bool KV, int C, int J> DI void a_pref(const ASrc& s, const WSrc& nx) {
  if constexpr (J < 0) {}
  else if constexpr (C < 8) a_one(s, C, J);
  else if constexpr (C == 8) { if constexpr (KV && J < 4) glds(s.L + 98304 + s.w * 4096 + J * 1024, s.vq, s.rw, s.sq + J * (16 * 3072)); }
  else w_one<1>(nx, (6 * (C - 9) + J) >> 2, (6 * (C - 9) + J) & 3);
}
constexpr int a_gap(int C, int g) {
  const int t0[6] = {5, 6, 9, 10, 13, 14}, t1[6] = AGAP, t2[6] = {1, 2, -1, -1, -1, -1};
  for (int j = 0; j < 6; ++j) if ((C == 2 ? t0[j] : C == 10 ? t2[j] : t1[j]) == g) return j;
  return -1;
}
struct AMisc { u32x4 rws; u32 cfoff, tag, rdy, clr; int lane; };
struct AAd { u32 x0[4], x1[4], wa; };
template <bool KV, int T> DI void a_steps(const ASrc& s, const WSrc& nx, AAd& ad, u32x4 (&x0)[33], u32x4 (&f)[2][3], f32x16 (&c)[4], u32& cf, const AMisc& ms TSP) {
  if constexpr (T < 32) {
    constexpr int C = T >> 2, K = T & 3, P = T & 1, N = (T + 1) & 3;
    if constexpr (K == 3) {
      if constexpr (C < 6) vmbar<6 + ((KV && C == 1) || C == 3)>(); else if constexpr (C == 6) vmbar<KV ? 5 : 0>(); else vmbar<6>();
      if constexpr (C < 7) {
        constexpr u32 dl = (C + 1) % 3 ? AST : 0u - 2 * AST;
#pragma unroll
        for (int k = 0; k < 4; ++k) { ad.x0[k] += dl; ad.x1[k] += dl; }
        ad.wa += dl;
        asm volatile("" : "+v"(ad.x0[0]), "+v"(ad.x0[1]), "+v"(ad.x0[2]), "+v"(ad.x0[3]), "+v"(ad.x1[0]), "+v"(ad.x1[1]), "+v"(ad.x1[2]), "+v"(ad.x1[3]), "+v"(ad.wa));
      }
      if constexpr (C == 0 && KV) { if (s.w < 4) st128c(u32x4{~0u, ~0u, ~0u, ~0u}, ms.lane * 16, ms.rws, ms.clr); else st32c(~0u, (ms.lane & 31) * 4, ms.rws, ms.clr); }
      if constexpr (C == 2) { st32c(ms.tag, ms.rdy, ms.rws, 0); }
      if constexpr (C == 5 && KV) ld32c(cf, ms.cfoff, ms.rws, 0);
    }
    constexpr int PC = K == 3 ? C + 3 : C + 2, G0 = 4 * N;
    if (PRIO & 1) __builtin_amdgcn_s_setprio(3 - N);
    if constexpr (T < 31) { x0[T + 1] = lds128(ad.x0[N]); f[P ^ 1][1] = trd2(ad.wa + N * 2048, ad.wa + N * 2048 + 512); }
    SB();
    if constexpr (KV) c[0] = mfma(x0[T], f[P][1], c[0]); else c[0] = mfma(f[P][1], x0[T], c[0]);
    SB();
    if constexpr (T < 31) { f[P ^ 1][0] = lds128(ad.x1[N]); f[P ^ 1][2] = trd2(ad.wa + 8192 + N * 2048, ad.wa + 8192 + N * 2048 + 512); }
    a_pref<KV, PC, a_gap(PC, G0)>(s, nx);
    SB();
    if constexpr (KV) c[1] = mfma(f[P][0], f[P][1], c[1]); else c[1] = mfma(f[P][1], f[P][0], c[1]);
    SB();
    a_pref<KV, PC, a_gap(PC, G0 + 1)>(s, nx);
    SB();
    if constexpr (KV) c[2] = mfma(x0[T], f[P][2], c[2]); else c[2] = mfma(f[P][2], x0[T], c[2]);
    SB();
    a_pref<KV, PC, a_gap(PC, G0 + 2)>(s, nx);
    SB();
    if constexpr (KV) c[3] = mfma(f[P][0], f[P][2], c[3]); else c[3] = mfma(f[P][2], f[P][0], c[3]);
    SB();
    a_pref<KV, PC, a_gap(PC, G0 + 3)>(s, nx);
    SB();
    a_steps<KV, T + 1>(s, nx, ad, x0, f, c, cf, ms TSA);
  }
}

DI ASrc a_src(const char* x, const char* wqkv, u32 L, int lane, int w, int xcd, int slot) {
  const bool kv = slot < 30;
  const int ai = slot - 22, rc = (xcd & 6) + (kv ? ai >> 2 : slot - 30), hg = (xcd & 1) * 4 + (ai & 3), qj = xcd & 1;
  ASrc sa;
  sa.rx = srd(x, 2048 * 1024); sa.rw = srd(wqkv, 512 * 3072); sa.L = L; sa.w = w;
  sa.rot = xcd & RA;
  sa.vx0 = (lane >> 3) * 1024 + ((lane & 7) ^ (lane >> 4)) * 16; sa.vx1 = sa.vx0 ^ 64;
  sa.vw = (lane >> 3) * 3072 + ((lane & 7) ^ ((lane >> 2) & 4)) * 16;
  sa.vq = (lane >> 2) * 3072 + (lane & 3) * 16;
  sa.sx = (rc * 256 + w * 32) * 1024;
  sa.s0 = w * 8 * 3072 + (kv ? 1024 + hg * 128 : 512 + qj * 256);
  sa.s1 = sa.s0 + (kv ? 1024 : 128);
  sa.sq = ((w + sa.rot) & 7) * (64 * 3072) + hg * 64;
  return sa;
}
DI WSrc w1_src(const char* w1, u32 lds, int lane, int w, int xcd, int slot, int& ft, int& dt1) {
  const int gj = slot < 16 ? (xcd >> 1) * 16 + slot : 48 + (xcd >> 1) * 6 + slot;
  const u32 wch = (lane & 7) ^ ((lane >> 2) & 4);
  ft = gj >> 2; dt1 = (xcd & 1) * 4 + (gj & 3);
  WSrc s1;
  s1.rs = srd(w1, 2048 * 2752); s1.stride = 2752; s1.lds = lds;
  s1.voff = (lane >> 3) * 2752 + (ft == 21 ? (wch & 3) : wch) * 16;
  s1.sbase = ((w >> 1) * 512 + (w & 1) * 32) * 2752 + ft * 128;
  s1.rot = (2 * (dt1 + ft)) & R1;
  return s1;
}
template <bool KV> DI u32 attn(u32 ev, const char* x, const char* wqkv, u32x4 rws, const WSrc& s2, u32 L, int lane, int w, int xcd, int slot, u32 wa0, u32x4 (&wr)[16][2] TSP) {
  const int hh = lane >> 5, col = lane & 31, q4 = (lane & 15) >> 2, mi = w >> 1, ni = w & 1;
  const int ai = slot - 22, rc = (xcd & 6) + (KV ? ai >> 2 : slot - 30), hg = (xcd & 1) * 4 + (ai & 3), qj = xcd & 1;
  const ASrc sa = a_src(x, wqkv, L, lane, w, xcd, slot);
  AAd ad;
#pragma unroll
  for (int k = 0; k < 4; ++k) { ad.x0[k] = L + (mi * 64 + ni * 32 + col) * 128 + ((2 * k + hh) ^ ((col >> 1) & 7)) * 16; ad.x1[k] = ad.x0[k] ^ 4096; }
  ad.wa = L + 32768 + (8 * hh + q4) * 128 + 64 * ((q4 >> 1) ^ ni) + 32 * ((lane >> 4) & 1) + 8 * (lane & 3);
  const int pr = hg + (mi & 1) * 8;
  const bool ga = w < 4;
  const u32 cfoff = ga ? WS_F + xcd * FRS + (128 + (pr >> 1) * 8 + (lane & 7)) * 128 : OOB;
  asm volatile("" : "+v"(ad.x0[0]), "+v"(ad.x0[1]), "+v"(ad.x0[2]), "+v"(ad.x0[3]), "+v"(ad.x1[0]), "+v"(ad.x1[1]), "+v"(ad.x1[2]), "+v"(ad.x1[3]), "+v"(ad.wa)::"memory");
  const u32 tag = tag_wait<6>(ev);
  bar();
  TS(1);
  const AMisc ms = {rws, cfoff, tag, lane == 0 ? WS_F + w * FRS + (KV ? 128 + hg * 8 + rc : 192 + qj * 8 + rc) * 128 : OOB, w < 4 ? WS_KV + ((hg * 2 + (w >> 1)) * 8 + rc) * 2048 + (w & 1) * 1024 : WS_Z + ((hg * 2 + (w & 1)) * 8 + rc) * 128, lane};
  u32x4 x0[33], f[2][3]; f32x16 c[4]; u32 cf;
  c[0] = c[1] = splat(KV ? 1.f : 1.000001f); c[2] = c[3] = splat(KV ? 0.f : 1.000001f);
  x0[0] = lds128(ad.x0[0]); f[0][0] = lds128(ad.x1[0]); f[0][1] = trd2(ad.wa, ad.wa + 512); f[0][2] = trd2(ad.wa + 8192, ad.wa + 8192 + 512);
  a_steps<KV, 0>(sa, s2, ad, x0, f, c, cf, ms TSA);
  __builtin_amdgcn_s_setprio(0);
  TS(2);
  asm volatile("" : "+v"(c[0]), "+v"(c[1]), "+v"(c[2]), "+v"(c[3]));
  TS(3);
  const u32 pfl = WS_F + xcd * FRS + (512 + rc * 16 + 8 + qj) * 128;
  if constexpr (!KV) {
#pragma unroll
    for (int j = 0; j < 4; ++j)
#pragma unroll
      for (int h = 0; h < 2; ++h) { st128c(phi8<true>(c[j], 8 * h), lane * 16 + qj * 8192 + ni * 2048, rws, WS_P + (rc * 8 + (w ^ (j & 1))) * 16384 + ((j & 2) * 2 + h) * 1024); SB(); }
    TS(5);
    asm volatile("s_barrier" ::: "memory");
    w_issue<1>(s2, 2);
    vmbar<4>();
    if (w == 0) st32c(tag, lane == 0 ? 0 : OOB, rws, pfl);
    TS(4);
    if (xcd == 7 && slot == 31 && w == 0) {
#pragma unroll 1
      for (int i = 0; i < 4; ++i) poll(WS_F + 7 * FRS + (i * 64 + lane) * 128, rws, tag);
      st32c(tag, lane == 0 ? 0 : OOB, rws, WS_E);
    }
    vmbar<0>();
    w_stage(w_addr(wa0), wr, 0, 12);
    bar();
    w_flag<3>(s2, w, tag, OOB, rws, 0);
    return tag;
  }
  const u32 S = L + 65536, KT = S + 14336, ZT = KT + 2048;
  if (w >= 4) w_issue<1>(s2, 2);
  const u32 on = ((lane >> 4) ^ (lane >> 2)) & 1 ? 0 : 0x3f803f80u;
  const u32x4 cs = {on, on, on, on};
  f32x16 d1 = splat(0.f); f32x4 d2 = {64e-6f, 64e-6f, 64e-6f, 64e-6f}, d3 = {0.f, 0.f, 0.f, 0.f};
  u32x4 p[4];
#pragma unroll
  for (int i = 0; i < 4; ++i) {
    const f32x16& k = c[i >> 1]; const int o = 8 * (i & 1);
    p[i][0] = phi2<false>(k[o], k[o + 1]); SB();
    if (i) d2 = mfma16(cs, p[i - 1], d2);
    const u32x4 v = pack8(c[2 + (i >> 1)], o);
    p[i][1] = phi2<false>(k[o + 2], k[o + 3]); SB();
    d3 = mfma16(cs, v, d3);
    p[i][2] = phi2<false>(k[o + 4], k[o + 5]); p[i][3] = phi2<false>(k[o + 6], k[o + 7]); SB();
    d1 = mfma(p[i], v, d1);
    SB(); if (w < 4) w_one<1>(s2, 2, i); SB();
  }
  d2 = mfma16(cs, p[3], d2);
  const bool lo = col < 16;
  f32x4 k0 = {lo ? d1[0] : d1[8], lo ? d1[1] : d1[9], lo ? d1[2] : d1[10], lo ? d1[3] : d1[11]};
  f32x4 k1 = {lo ? d1[4] : d1[12], lo ? d1[5] : d1[13], lo ? d1[6] : d1[14], lo ? d1[7] : d1[15]};
  const float ev6 = d3[0] * 1e-6f;
  k0 = add4(k0, f32x4{ev6, ev6, ev6, ev6}); k1 = add4(k1, f32x4{ev6, ev6, ev6, ev6});
  const float zp = d2[0];
  if (w >= 2) { *(LAS f32x4*)(S + (w - 2) * 2048 + lane * 16) = k0; *(LAS f32x4*)(S + (w - 2) * 2048 + 1024 + lane * 16) = k1; *(LAS float*)(S + 12288 + (w - 2) * 256 + lane * 4) = zp; }
  TS(13);
  vmbar<0>();
  TS(14);
  if (w < 2) {
    const u32 a = S + w * 2048 + lane * 16, z = S + 12288 + w * 256 + lane * 4;
    k0 = add4(add4(k0, ldsf4(a)), add4(ldsf4(a + 4096), ldsf4(a + 8192))); k1 = add4(add4(k1, ldsf4(a + 1024)), add4(ldsf4(a + 5120), ldsf4(a + 9216)));
    const float t = add1(add1(zp, ldsf(z)), add1(ldsf(z + 512), ldsf(z + 1024)));
    u32x4 p0 = __builtin_bit_cast(u32x4, k0), p1 = __builtin_bit_cast(u32x4, k1);
#pragma unroll
    for (int i = 0; i < 4; ++i) { p0[i] = umin(p0[i], ~1u); p1[i] = umin(p1[i], ~1u); }
    const u32 o = (hg * 2 + w) * 8 + rc;
    st128c(p0, lane * 16, rws, WS_KV + o * 2048); st128c(p1, lane * 16, rws, WS_KV + o * 2048 + 1024); st32c(umin(__float_as_uint(t), ~1u), col * 4, rws, WS_Z + o * 128);
  }
  TS(4);
  u32x4 pq[2][2], t[8]; u32x2 z; u32 pf;
  u32 qa = L + 98304 + (8 * hh + q4) * 64 + 32 * ((lane >> 4) & 1) + 8 * (lane & 3);
  asm volatile("" : "+v"(qa), "+v"(cf));
  u32x4 qf[4];
#pragma unroll
  for (int i = 0; i < 3; ++i) qf[i] = trd2(qa + i * 1024, qa + i * 1024 + 256);
  f32x16 cq = splat(1.000001f);
#pragma unroll
  for (int i = 0; i < 32; ++i) {
    SB();
    if (i < 29) qf[(i + 3) & 3] = trd2(qa + (i + 3) * 1024, qa + (i + 3) * 1024 + 256);
    if (QN >= 0) asm volatile("s_nop %0" ::"i"(QN < 0 ? 0 : QN));
    SB();
    cq = mfma(qf[i & 3], x0[i], cq);
  }
  SB();
  TS(18);
  auto sweep = [&]() {
#pragma unroll
    for (int r = 0; r < 8; ++r) ld128c<0>(t[r], lane * 16, rws, WS_KV + (pr * 8 + r) * 2048 + ni * 1024);
    asm volatile("buffer_load_dwordx2 %0, %1, %2, %3 offen sc1" : "=v"(z) : "v"((lane >> 3) * 128 + (lane & 7) * 8), "s"(rws), "s"(WS_Z + pr * 1024 + ni * 64) : "memory");
  };
  asm volatile("" : "=v"(t[0]), "=v"(t[1]), "=v"(t[2]), "=v"(t[3]), "=v"(t[4]), "=v"(t[5]), "=v"(t[6]), "=v"(t[7]), "=v"(z));
  if (ga && __builtin_amdgcn_ballot_w64(cf != tag) != 0) poll(cfoff, rws, tag);
  ld32c(pf, pfl, rws, 0);
  if (mi == 0) sweep();
  pq[0][0] = phi8<true>(cq, 0); pq[0][1] = phi8<true>(cq, 8);
  asm volatile("" : "+v"(pq[0][0]), "+v"(pq[0][1]));
  TS(5);
  w_stage(w_addr(wa0), wr, 0, 12);
  SB();
  if (mi == 1) sweep();
  if (!ga) vmwait<0>();
  asm volatile("s_waitcnt vmcnt(9)" : "+v"(pf)::"memory");
  if (__builtin_amdgcn_ballot_w64(pf != tag) != 0) poll(pfl, rws, tag);
  ld128c<0>(pq[1][0], lane * 16, rws, WS_P + (rc * 8 + w) * 16384 + hg * 2048); ld128c<1024>(pq[1][1], lane * 16, rws, WS_P + (rc * 8 + w) * 16384 + hg * 2048);
  TS(15);
#define GW(N) asm volatile("s_waitcnt vmcnt(%9)" : "+v"(t[0]), "+v"(t[1]), "+v"(t[2]), "+v"(t[3]), "+v"(t[4]), "+v"(t[5]), "+v"(t[6]), "+v"(t[7]), "+v"(z) : "i"(N) : "memory")
  GW(2);
  if (ga) {
    for (int it = 0; it < SPIN_MAX; ++it) {
      u32 m = umax(z[0], z[1]);
#pragma unroll
      for (int r = 0; r < 8; ++r) m = umax(umax(umax(m, t[r][0]), t[r][1]), umax(t[r][2], t[r][3]));
      if (__builtin_amdgcn_ballot_w64(m == ~0u) == 0) break;
      sweep();
      GW(0);
    }
    TS(16);
    f32x4 a = __builtin_bit_cast(f32x4, t[0]);
#pragma unroll
    for (int r = 1; r < 8; ++r) a += __builtin_bit_cast(f32x4, t[r]);
    float zs = fold32(__uint_as_float(z[0]), __uint_as_float(z[1]));
    zs = dppadd<0x128>(fold16(zs, zs));
    const int pl = mi, zf = 2 * (lane & 7) + hh;
    *(LAS u32x2*)(KT + pl * 1024 + lane * 16 + ni * 8) = u32x2{pk(a[0], a[1]), pk(a[2], a[3])};
    *(LAS unsigned short*)(ZT + pl * 64 + ni * 32 + ((zf >> 2) & 1) * 16 + ((zf & 3) + 4 * (zf >> 3)) * 2) = (unsigned short)pk(zs, zs);
  }
  bar();
  TS(6);
  f32x16 dd[2][2];
#pragma unroll
  for (int j = 0; j < 2; ++j) {
    if (j) asm volatile("s_waitcnt vmcnt(0)" : "+v"(pq[1][0]), "+v"(pq[1][1])::"memory");
#pragma unroll
    for (int h = 0; h < 2; ++h) dd[j][h] = mfma(lds128(((col < 16) == (h == 0)) ? KT + j * 1024 + lane * 16 : ZT + j * 64 + h * 32 + hh * 16), pq[j][h], splat(0.f));
  }
  SB();
#pragma unroll
  for (int c = 3; c < 3 + NE; ++c) w_issue<1>(s2, c);
  SB();
#pragma unroll
  for (int j = 0; j < 2; ++j)
#pragma unroll
    for (int h = 0; h < 2; ++h) {
      const int o = 8 * h;
      const f32x16& e = dd[j][h];
      const float rd = __builtin_amdgcn_rcpf(e[8 - o] + 1e-6f);
      const u32x4 ov = {pk(e[o] * rd, e[o + 1] * rd), pk(e[o + 2] * rd, e[o + 3] * rd), pk(e[o + 4] * rd, e[o + 5] * rd), pk(e[o + 6] * rd, e[o + 7] * rd)};
      st128c(ov, lane * 16, rws, WS_O + (rc * 8 + w) * 32768 + ((hg + 8 * j) * 2 + h) * 1024);
    }
  TS(17);
  bar();
  w_flag<3 + NE>(s2, w, tag, lane < 2 ? ((rc & 6) + lane) * FRS : OOB, rws, WS_F + (512 + rc * 16 + hg) * 128);
  return tag;
}

__global__ __launch_bounds__(512, 2) void fused_kernel(const char* __restrict__ x, const char* __restrict__ wqkv, const char* __restrict__ wo,
                                                        const char* __restrict__ w1, const char* __restrict__ b1, const char* __restrict__ w2,
                                                        const char* __restrict__ b2, const char* __restrict__ gw, char* __restrict__ out, char* ws TSK) {
  const u32 bid = blockIdx.x;
  __shared__ __attribute__((aligned(1024))) char sm[163840];
  TSBEG; TS(0);
  u32 ev;
  {
    unsigned long long pc; u32 cn;
    asm volatile("s_getpc_b64 %0\n.Lfk_pc:\n\ts_mov_b32 %1, .Lfk_end-.Lfk_pc" : "=s"(pc), "=s"(cn));
    const u32 t = threadIdx.x, b = bid >> 3;
    const u32 vo = CW == 1 ? (t < 64 ? (b * 64 + t) * 128 : OOB) : CW == 2 ? (t < 64 ? ((b - 16) * 64 + t) * 128 : OOB) : CW == 3 ? (t < 9 ? (b * 9 + t) * 128 : OOB) : CW == 4 ? (b == 0 || b == 16 ? t * 128 : OOB) : CW == 5 ? ((b & 7) == 0 ? t * 128 : OOB) : CW == 6 ? (b == 0 ? t * 128 : OOB) : (b < 2 ? t * 128 : OOB);
    asm volatile("buffer_load_dword %0, %1, %2, 0 offen" : "=v"(ev) : "v"(vo), "s"(srd((const void*)pc, cn)) : "memory");
    SB();
  }
  asm volatile("" : "+s"(x), "+s"(wqkv), "+s"(wo), "+s"(w1), "+s"(b1), "+s"(w2), "+s"(b2), "+s"(gw), "+s"(out), "+s"(ws));
  const u32 L = (u32)(size_t)(LAS char*)sm;
  const int lane = threadIdx.x & 63, w = __builtin_amdgcn_readfirstlane(threadIdx.x >> 6);
  int xcd = bid & 7, slot = bid >> 3;
  const int hh = lane >> 5;
  const u32x4 rws = srd(ws, WS_END);
  u32x4 xr[2], gr;
  asm volatile("s_nop 4" : "=v"(xr[0]), "=v"(xr[1]), "=v"(gr)::"memory");
  ev_load(ev, rws);
  if (slot < 22) {
    int ft, dt1;
    const WSrc s1 = w1_src(w1, L + w * 4096, lane, w, xcd, slot, ft, dt1);
    if (slot < 16) {
      const int r = (xcd * 16 + slot) * 16 + w;
      const u32x4 rx = srd(x, 2048 * 1024);
      ld128(xr[0], lane * 16, rx, r * 1024); ld128(xr[1], lane * 16, rx, (r + 8) * 1024);
      ld128(gr, lane * 16, srd(gw, 1024), 0);
      asm volatile("s_barrier" ::: "memory");
    }
    w_issue<0>(s1, 0);
  } else {
    const ASrc sa = a_src(x, wqkv, L, lane, w, xcd, slot);
#pragma unroll
    for (int j = 0; j < 6; ++j) a_one(sa, 0, j);
    asm volatile("s_barrier" ::: "memory");
#pragma unroll
    for (int j = 0; j < 6; ++j) a_one(sa, 1, j);
  }
  asm volatile("" : "+s"(slot), "+s"(xcd));
  const int g = w >> 1, hf = w & 1, q4 = (lane & 15) >> 2;
  const u32 wch = (lane & 7) ^ ((lane >> 2) & 4);
  const u32 wa0 = L + g * 8192 + (4 * hh + q4) * 128 + 64 * (q4 >> 1) + 32 * ((lane >> 4) & 1) + 8 * (lane & 3);

  const int dt = (xcd & 1) * 4 + (slot & 3), stl = (xcd >> 1) * 8 + (slot >> 2);
  WSrc s2;
  s2.rs = g < 3 ? srd(w2, 1376 * 4096) : srd(wo, 512 * 1024); s2.stride = g < 3 ? 4096 : 1024; s2.lds = L + g * 8192 + hf * 4096;
  s2.voff = (lane >> 3) * s2.stride + wch * 16;
  s2.sbase = g < 3 ? (g * 512 + hf * 32) * 4096 + stl * 128 : hf * 32 * 1024 + dt * 128;
  s2.rot = 2 * ((slot + (slot >> 2)) & R2);
  u32 tag;
  u32x4 wr[16][2];

  if (slot >= 30) tag = attn<false>(ev, x, wqkv, rws, s2, L, lane, w, xcd, slot, wa0, wr TSA);
  else if (slot >= 22) tag = attn<true>(ev, x, wqkv, rws, s2, L, lane, w, xcd, slot, wa0, wr TSA);
  else {
    const int gi = slot < 16 ? xcd * 16 + slot : 112 + xcd * 6 + slot;
    int ft, dt1;
    const WSrc s1 = w1_src(w1, s2.lds, lane, w, xcd, slot, ft, dt1);
    u32 pw = 0;
    if (slot < 16) {
      const int sb = xcd * 16 + slot;
      tag = tag_wait<4>(ev);
      asm volatile("" : "+v"(xr[0]), "+v"(xr[1]), "+v"(gr)::"memory");
      TS(1);
      f32x2 v[2][4], t; float s[2], gd[2], q[2];
#pragma unroll
      for (int j = 0; j < 2; ++j) {
#pragma unroll
        for (int i = 0; i < 4; ++i) v[j][i] = f32x2{blo(xr[j][i]), bhi(xr[j][i])};
        t = (v[j][0] + v[j][1]) + (v[j][2] + v[j][3]); s[j] = t[0] + t[1];
      }
      const float t0 = fold32(s[0], s[1]), t1 = rowsum(fold16(t0, t0)) * (1.f / 512.f);
#pragma unroll
      for (int j = 0; j < 2; ++j) {
        t = f32x2{0.f, 0.f};
#pragma unroll
        for (int i = 0; i < 4; ++i) t = __builtin_elementwise_fma(v[j][i], f32x2{blo(gr[i]), bhi(gr[i])}, t);
        gd[j] = t[0] + t[1];
        const float m = rdl(t1, 32 * j);
        t = f32x2{0.f, 0.f};
#pragma unroll
        for (int i = 0; i < 4; ++i) { v[j][i] -= m; t = __builtin_elementwise_fma(v[j][i], v[j][i], t); }
        q[j] = t[0] + t[1];
      }
      const float t2 = rowsum(fold16(fold32(q[0], q[1]), fold32(gd[0], gd[1])));
      const float rs = __builtin_amdgcn_rsqf(t2 * (1.f / 512.f) + 1e-5f);
#pragma unroll
      for (int j = 0; j < 2; ++j) {
        const float r = rdl(rs, 32 * j);
#pragma unroll
        for (int i = 0; i < 4; ++i) v[j][i] *= r;
        *(LAS u32x4*)(L + (w + 8 * j) * 1088 + lane * 16) = u32x4{pk(v[j][0][0], v[j][0][1]), pk(v[j][1][0], v[j][1][1]), pk(v[j][2][0], v[j][2][1]), pk(v[j][3][0], v[j][3][1])};
      }
      const float gt = __builtin_amdgcn_rcpf(1.f + __builtin_amdgcn_exp2f(t2 * -1.4426950408889634f));
      TS(13);
      bar();
      const u32 ta = L + (4 * hh + q4) * 1088 + w * 128 + 32 * ((lane >> 4) & 1) + 8 * (lane & 3);
      const u32x4 h0 = trd2(ta, ta + 8 * 1088), h1 = trd2(ta + 64, ta + 64 + 8 * 1088);
      st32c(__float_as_uint(gt), (lane & 31) == 16 ? lane & 32 : OOB, rws, WS_G + (sb * 16 + w) * 4);
      st128c(h0, lane * 16, rws, WS_H + w * 262144 + sb * 1024);
      st128c(h1, lane * 16, rws, WS_H + w * 262144 + 131072 + sb * 1024);
      asm volatile("s_barrier" ::: "memory");
      TS(14);
      w_issue<0>(s1, 1);
      TS(15);
      vmbar<4>();
    } else {
      u32 tl;
      asm volatile("buffer_load_dword %0, %1, %2, 0 offen" : "=v"(tl) : "v"((lane << TL) + 2048),
                   "s"(w == 0 ? srd(w2, 1376 * 4096) : w == 1 ? srd(wo, 512 * 1024) : w == 2 ? srd(out, 2048 * 1024) : w == 3 ? srd(b1, 2752) : w == 4 ? srd(b2, 4096) : srd(ws, w == 5 ? WS_END : 0)) : "memory");
      w_issue<0>(s1, 1);
      tag = tag_wait<4>(ev);
      asm volatile("" ::"v"(tl));

      bar();
    }
    TS(2);
    const WA a0 = w_addr(wa0);
    if (w == 0) st32c(tag, lane < 8 ? lane * FRS : OOB, rws, WS_F + (slot < 16 ? gi : gi + 80) * 128);
    w_round<0, 0, 2>(s1, a0, wr); w_issue<0>(s1, 3);
    TS(18);
    vmbar0<8>(w);
    if (PFW) asm volatile("" ::"v"(pw));
    TS(19);
    w_round<0, 1, 4>(s1, a0, wr);
    vmbar<8>();
    TS(20);
    w_round<0, 2, 5>(s1, a0, wr);
    vmbar<8>();
    TS(21);
    const u32 hfl = hf ? WS_F + xcd * FRS + (g * 32 + (lane & 31)) * 128 : OOB;
    u32 pf; u32x2 bb0, bb1;
    ld32c(pf, hfl, rws, 0);
    asm volatile("buffer_load_dwordx2 %0, %2, %3, %4 offen\n\tbuffer_load_dwordx2 %1, %2, %3, %4 offen offset:16" : "=&v"(bb0), "=&v"(bb1) : "v"(hh * 8), "s"(srd(b1, 2752)), "s"(ft * 128 + g * 32) : "memory");
    w_round<0, 3, 6>(s1, a0, wr); w_issue<0>(s1, 7);
    TS(3);
    asm volatile("s_waitcnt vmcnt(8)" : "+v"(pf), "+v"(bb0), "+v"(bb1)::"memory");
    if (hf && __builtin_amdgcn_ballot_w64(pf != tag) != 0) poll(hfl, rws, tag);
    bar();
    TS(4);
    f32x16 c0, c1;
    wr_gemm<0>(wr, a0, DSrc{rws, (u32)lane * 16, WS_H + (dt1 * 2 + hf) * 131072 + g * 32768, s1.rot}, s2, 0, true, c0, c1);
    TS(5);
    const u32 R = L + 65536 + hf * 32768 + lane * 16;
    f32x4 lo = {blo(bb0[0]), bhi(bb0[0]), blo(bb0[1]), bhi(bb0[1])}, hi = {blo(bb1[0]), bhi(bb1[0]), blo(bb1[1]), bhi(bb1[1])};
#pragma unroll
    for (int q = 0; q < 4; ++q) {
      const f32x16& c = q < 2 ? c0 : c1; const int o = (q & 1) * 8;
      const f32x4 a = {c[o], c[o + 1], c[o + 2], c[o + 3]}, b = {c[o + 4], c[o + 5], c[o + 6], c[o + 7]};
      if (q == g) { lo += a; hi += b; }
      else { const u32 t = R + q * 8192 + ((g - q) & 3) * 2048; *(LAS f32x4*)t = a; *(LAS f32x4*)(t + 1024) = b; }
    }
    TS(16);
    if (VB) bar(); else vmbar<0>();
    TS(17);
    if (SO && w >= 4) { w_stage(a0, wr, 0, 12); SB(); }
#pragma unroll
    for (int k = 1; k < 4; ++k) { lo += ldsf4(R + g * 8192 + k * 2048); hi += ldsf4(R + g * 8192 + k * 2048 + 1024); }
    const u32x4 uv = {pk(gelu(lo[0]), gelu(lo[1])), pk(gelu(lo[2]), gelu(lo[3])), pk(gelu(hi[0]), gelu(hi[1])), pk(gelu(hi[2]), gelu(hi[3]))};
    st128c(uv, lane * 16, rws, WS_U + (dt1 * 2 + hf) * UDB + (ft * 4 + g) * 1024);
    TS(6);
    if (VB) vmbar<1>();
    if (!SO || w < 4) w_stage(a0, wr, 0, 12);
    bar();
    TS(22);
    w_flag<3>(s2, w, tag, lane < 4 ? (lane * 2 + (dt1 >> 2)) * FRS : OOB, rws, WS_F + (256 + dt1 * 32 + ft) * 128);
  }

  {
    TS(7);
    asm volatile("" : "+s"(s2.lds), "+s"(s2.rot), "+s"(s2.sbase));
    const int sr = w * 8 + (lane >> 3), cq = lane & 7;
    u32 b2v;
    asm volatile("buffer_load_ushort %0, %1, %2, %3 offen" : "=v"(b2v) : "v"(sr * 2), "s"(srd(b2, 4096)), "s"(stl * 128) : "memory");
    const u32 GT = L + 131072, SG = GT + 256;
    const WA a0 = w_addr(wa0);
    f32x16 c0, c1;
    const int nf = g == 2 ? 6 : 8;
    const u32 fo = WS_F + xcd * FRS + ((g < 3 ? 256 + dt * 32 + g * 8 : 512 + (stl >> 2) * 16) + (lane < nf ? lane : 0)) * 128;
    const bool early = EP && slot >= 22 && slot < 30 && (EP == 2 || g < 3);
    u32 pf;
    if (PP == 2) ld32c(pf, early ? fo : OOB, rws, 0);
    w_issue<1>(s2, 6);
    if (PP == 1) ld32c(pf, early ? fo : OOB, rws, 0);
    w_issue<1>(s2, 7);
    TS(24);
    vmbar0<18>(w);
    w_round<1, 3, 8>(s2, a0, wr);
    bar();
    TS(25);
    *(volatile LAS u32*)(SG + w * 4) = 0;
    TS(8);
    asm volatile("s_waitcnt vmcnt(%2)" : "+v"(pf), "+v"(b2v) : "i"(4 * PP) : "memory");
    const bool ok = early && __builtin_amdgcn_ballot_w64(pf != tag) == 0;
    if (!ok) poll_bar(fo, rws, tag);
    TS(9);
    u32 gv;
    ld32c(gv, w == 0 ? lane * 4 : OOB, rws, WS_G + stl * 256);
    const u32 ub = WS_U + (dt * 2 + hf) * UDB;
    wr_gemm<1>(wr, a0, DSrc{srd(ws, g < 3 ? ub + 86 * 1024 : WS_END), (u32)lane * 16, g < 3 ? ub + g * 32768 : WS_O + (stl * 2 + hf) * 32768, s2.rot}, s2, SG + w * 4, ok, c0, c1);
    asm volatile("" : "+v"(gv));
    TS(10);
    if (w == 0) *(LAS u32*)(GT + lane * 4) = gv;
    for (int it = 0; it < SPIN_MAX * 16 && *(volatile LAS u32*)(SG + (w ^ 1) * 4) == 0; ++it) {}
    const u32 pb = L + g * 8192 + hf * 32768;
    if (g < 3) {
      asm volatile("s_mov_b32 m0, %0\n\ts_nop 15\n\ts_nop 7" ::"s"(pb + hf * 128) : "memory");
      put16<0, 0>(c0);
      asm volatile("s_mov_b32 m0, %0\n\ts_nop 0" ::"s"(pb + hf * 128 + 6144) : "memory");
      put16<65536 - 6144, 0>(c1);
    } else red_write<RSL>(pb + lane * 16, 65536, c0, c1);
    bar();
    TS(11);
    const int sl = lane >> 3, i = (sl & 3) + 4 * (w & 3);
    const u32 ya = L + (cq >> 2) * (32768 + 128) + (w >> 2) * 65536 + (i >> 1) * 768 + (i & 1) * 272 + (sl >> 2) * 128 + (cq & 3) * 32;
    const f32x4 y0 = (ldsf4(ya) + ldsf4(ya + 8192)) + ldsf4(ya + 16384), y1 = (ldsf4(ya + 16) + ldsf4(ya + 8208)) + ldsf4(ya + 16400);
    const u32 aa = L + 24576 + (w >> 2) * 32768 + (cq >> 2) * 65536 + (cq & 3) * RSL + (sr & 31) * 16;
    const f32x4 a0v = ldsf4(aa), a1v = ldsf4(aa + 512);
    const float gt = ldsf(GT + sr * 4), bb = blo(b2v), og = 1.f - gt;
    u32x4 ov;
    ov[0] = pk(gt * a0v[0] + og * (y0[0] + bb), gt * a0v[1] + og * (y0[1] + bb));
    ov[1] = pk(gt * a0v[2] + og * (y0[2] + bb), gt * a0v[3] + og * (y0[3] + bb));
    ov[2] = pk(gt * a1v[0] + og * (y1[0] + bb), gt * a1v[1] + og * (y1[1] + bb));
    ov[3] = pk(gt * a1v[2] + og * (y1[2] + bb), gt * a1v[3] + og * (y1[3] + bb));
    asm volatile("buffer_store_dwordx4 %0, %1, %2, %3 offen " OSM ::"v"(ov), "v"(sr * 1024 + cq * 16), "s"(srd(out, 2048 * 1024)), "s"(stl * 65536 + dt * 128) : "memory");
    TS(12); TS(26); TSEND;
    asm volatile(".Lfk_end:");
  }
}

extern "C" void kernel_launch(void* const* d_in, const int* in_sizes, int n_in, void* d_out, int out_size, void* d_ws, size_t ws_size, hipStream_t stream) {
  fused_kernel<<<256, 512, 0, stream>>>((const char*)d_in[0], (const char*)d_in[1], (const char*)d_in[2], (const char*)d_in[4], (const char*)d_in[5],
                                        (const char*)d_in[6], (const char*)d_in[7], (const char*)d_in[8], (char*)d_out, (char*)d_ws);
}
```

```cpp
#include <hip/hip_runtime.h>
#include <hip/hip_bf16.h>


#pragma clang diagnostic ignored "-Wint-to-pointer-cast"
#define DI __device__ __forceinline__
#define LAS __attribute__((address_space(3)))
#define SB() __builtin_amdgcn_sched_barrier(0)
typedef unsigned u32;
typedef u32 u32x4 __attribute__((ext_vector_type(4)));
typedef u32 u32x2 __attribute__((ext_vector_type(2)));
typedef float f32x2 __attribute__((ext_vector_type(2)));
typedef float f32x4 __attribute__((ext_vector_type(4)));
typedef float f32x16 __attribute__((ext_vector_type(16)));
typedef __bf16 bf16x8 __attribute__((ext_vector_type(8)));
typedef __bf16 bf16x2 __attribute__((ext_vector_type(2)));
typedef short s16x4 __attribute__((ext_vector_type(4)));

constexpr u32 WS_E = 0, WS_G = 32768;
constexpr u32 WS_F = 7u << 20, FRS = 641 * 128;
constexpr u32 WS_KV = 65536, WS_Z = WS_KV + 262144, WS_H = 1u << 20, WS_U = 3u << 20, WS_O = 5u << 20, WS_P = 8u << 20, WS_END = 9u << 20, OOB = 1u << 30;
constexpr u32 UDB = 88 * 1024;
constexpr int SPIN_MAX = 20000;
constexpr int WIN = 10;

DI u32x4 srd(const void* p, u32 bytes) { const unsigned long long a = (unsigned long long)p; return u32x4{(u32)a, (u32)(a >> 32), bytes, 0x00020000u}; }
DI f32x16 mfma(u32x4 a, u32x4 b, f32x16 c) { return __builtin_amdgcn_mfma_f32_32x32x16_bf16(__builtin_bit_cast(bf16x8, a), __builtin_bit_cast(bf16x8, b), c, 0, 0, 0); }
DI f32x4 mfma16(u32x4 a, u32x4 b, f32x4 c) { return __builtin_amdgcn_mfma_f32_16x16x32_bf16(__builtin_bit_cast(bf16x8, a), __builtin_bit_cast(bf16x8, b), c, 0, 0, 0); }
DI u32x2 trd(u32 a) { return __builtin_bit_cast(u32x2, __builtin_amdgcn_ds_read_tr16_b64_v4i16((LAS s16x4*)a)); }
DI u32x4 trd2(u32 a, u32 b) { const u32x2 x = trd(a), y = trd(b); return u32x4{x[0], x[1], y[0], y[1]}; }
DI u32x4 lds128(u32 a) { return *(LAS u32x4*)a; }
DI f32x4 ldsf4(u32 a) { return *(LAS f32x4*)a; }
DI float ldsf(u32 a) { return *(LAS float*)a; }
DI u32 pk(float a, float b) { return __builtin_bit_cast(u32, __builtin_convertvector(f32x2{a, b}, bf16x2)); }
DI float blo(u32 v) { return __uint_as_float(v << 16); }
DI float bhi(u32 v) { return __uint_as_float(v & 0xffff0000u); }
DI u32x4 pack8(const f32x16& x, int o) { return u32x4{pk(x[o], x[o + 1]), pk(x[o + 2], x[o + 3]), pk(x[o + 4], x[o + 5]), pk(x[o + 6], x[o + 7])}; }
DI f32x16 splat(float v) { return f32x16{v, v, v, v, v, v, v, v, v, v, v, v, v, v, v, v}; }
DI float add1(float a, float b) { float r; asm("v_add_f32 %0, %1, %2" : "=v"(r) : "v"(a), "v"(b)); return r; }
DI f32x4 add4(f32x4 a, f32x4 b) { return f32x4{add1(a[0], b[0]), add1(a[1], b[1]), add1(a[2], b[2]), add1(a[3], b[3])}; }
DI u32 umin(u32 a, u32 b) { return a < b ? a : b; }
DI u32 umax(u32 a, u32 b) { return a > b ? a : b; }

DI void glds(u32 lds, u32 voff, u32x4 rs, u32 soff) {
  asm volatile("s_mov_b32 m0, %0\n\ts_nop 0\n\tbuffer_load_dwordx4 %1, %2, %3 offen lds" ::"s"(lds), "v"(voff), "s"(rs), "s"(soff) : "memory");
}
template <int OFF> DI void ld128c(u32x4& d, u32 voff, u32x4 rs, u32 soff) {
  asm volatile("buffer_load_dwordx4 %0, %1, %2, %3 offen offset:%4 sc1" : "=v"(d) : "v"(voff), "s"(rs), "s"(soff), "i"(OFF) : "memory");
}
DI void ld128(u32x4& d, u32 voff, u32x4 rs, u32 soff) { asm volatile("buffer_load_dwordx4 %0, %1, %2, %3 offen" : "=v"(d) : "v"(voff), "s"(rs), "s"(soff) : "memory"); }
template <int OFF = 0> DI void ld32c(u32& d, u32 voff, u32x4 rs, u32 soff) { asm volatile("buffer_load_dword %0, %1, %2, %3 offen offset:%4 sc1" : "=v"(d) : "v"(voff), "s"(rs), "s"(soff), "i"(OFF) : "memory"); }
DI void st128c(u32x4 v, u32 voff, u32x4 rs, u32 soff) { asm volatile("buffer_store_dwordx4 %0, %1, %2, %3 offen sc1\n\ts_nop 1" ::"v"(v), "v"(voff), "s"(rs), "s"(soff) : "memory"); }
DI void st32c(u32 v, u32 voff, u32x4 rs, u32 soff) { asm volatile("buffer_store_dword %0, %1, %2, %3 offen sc1" ::"v"(v), "v"(voff), "s"(rs), "s"(soff) : "memory"); }
template <int N> DI void vmwait() { asm volatile("s_waitcnt vmcnt(%0)" ::"i"(N) : "memory"); }
template <int N> DI void vmwait(u32x4& a) { asm volatile("s_waitcnt vmcnt(%1)" : "+v"(a) : "i"(N) : "memory"); }
DI void bar() { asm volatile("s_waitcnt lgkmcnt(0)\n\ts_barrier" ::: "memory"); }
template <int N> DI void vmbar() { asm volatile("s_waitcnt vmcnt(%0) lgkmcnt(0)\n\ts_barrier" ::"i"(N) : "memory"); }

DI void ev_load(u32& e, u32x4 ws) { asm volatile("buffer_load_dword %0, off, %1, 0" : "+v"(e) : "s"(ws) : "memory"); }
template <int N> DI u32 tag_wait(u32 e) {
  u32 t;
  asm volatile("s_waitcnt vmcnt(%2)\n\tv_readfirstlane_b32 %0, %1\n\ts_nop 1" : "=s"(t) : "v"(e), "i"(N) : "memory");
  return t * 1664525u + 1013904223u;
}
DI void poll(u32 voff, u32x4 ws, u32 tag) {
  for (int it = 0; it < SPIN_MAX; ++it) {
    u32 f;
    asm volatile("buffer_load_dword %0, %1, %2, 0 offen sc1\n\ts_waitcnt vmcnt(0)" : "=&v"(f) : "v"(voff), "s"(ws) : "memory");
    if (__builtin_amdgcn_ballot_w64(f != tag) == 0) break;
    __builtin_amdgcn_s_sleep(4);
  }
}
DI void poll_bar(u32 voff, u32x4 ws, u32 tag) {
  u32 f;
  asm volatile("buffer_load_dword %0, %1, %2, 0 offen sc1\n\ts_waitcnt vmcnt(0) lgkmcnt(0)\n\ts_barrier" : "=&v"(f) : "v"(voff), "s"(ws) : "memory");
  if (__builtin_amdgcn_ballot_w64(f != tag) != 0) poll(voff, ws, tag);
}
template <int C> DI float dppadd(float v) { return v + __int_as_float(__builtin_amdgcn_update_dpp(0, __float_as_int(v), C, 0xf, 0xf, true)); }
DI float rowsum(float v) { v = dppadd<0xB1>(v); v = dppadd<0x4E>(v); v = dppadd<0x141>(v); return dppadd<0x140>(v); }
DI float rdl(float v, int l) { return __int_as_float(__builtin_amdgcn_readlane(__float_as_int(v), l)); }
DI float fold32(float a, float b) { const auto r = __builtin_amdgcn_permlane32_swap(__float_as_uint(a), __float_as_uint(b), false, false); return __uint_as_float(r[0]) + __uint_as_float(r[1]); }
DI float fold16(float a, float b) { const auto r = __builtin_amdgcn_permlane16_swap(__float_as_uint(a), __float_as_uint(b), false, false); return __uint_as_float(r[0]) + __uint_as_float(r[1]); }
template <bool E> DI u32 phi2(float a, float b) {
  constexpr float l2e = 1.4426950408889634f, c = -l2e * (E ? 1.000001f : 1.f);
  float e0 = __builtin_amdgcn_fmed3f(__builtin_amdgcn_exp2f(__builtin_fmaf(a, l2e, c)), 0.f, 1.f), e1 = __builtin_amdgcn_fmed3f(__builtin_amdgcn_exp2f(__builtin_fmaf(b, l2e, c)), 0.f, 1.f);
  if (E) { e0 += 1e-6f; asm("" : "+v"(e0)); e1 += 1e-6f; }
  return pk(__builtin_amdgcn_fmed3f(a, e0, 3.4028234664e38f), __builtin_amdgcn_fmed3f(b, e1, 3.4028234664e38f));
}
template <bool E> DI u32x4 phi8(const f32x16& x, int o) { return u32x4{phi2<E>(x[o], x[o + 1]), phi2<E>(x[o + 2], x[o + 3]), phi2<E>(x[o + 4], x[o + 5]), phi2<E>(x[o + 6], x[o + 7])}; }
DI float gelu(float v) {
  const float t = v * (-2.3022081981f - 0.1029432397f * v * v);
  return v * __builtin_amdgcn_rcpf(1.f + __builtin_amdgcn_exp2f(t));
}

constexpr int wslot(int P, int c) { return (c + (P && c < 3 ? 4 : 1)) % 5; }
struct WSrc { u32x4 rs; u32 voff, sbase, stride, lds, rot; };
template <int P> DI void w_one(const WSrc& s, int c, int i) { glds(s.lds + wslot(P, c) * 32768 + i * 1024, s.voff, s.rs, s.sbase + (64 * ((c + s.rot) & 7) + 8 * i) * s.stride); }
template <int P> DI void w_issue(const WSrc& s, int c) {
#pragma unroll
  for (int i = 0; i < 4; ++i) w_one<P>(s, c, i);
}
struct WA { u32 a[3][2]; };
DI WA w_addr(u32 a0) { WA r;
#pragma unroll
  for (int i = 0; i < 3; ++i) { r.a[i][0] = a0 + i * 65536; r.a[i][1] = (a0 ^ 64) + i * 65536; asm volatile("" : "+v"(r.a[i][0]), "+v"(r.a[i][1])); }
  return r; }
template <int P> DI void w_frag(const WA& r, u32x4 (&f)[2], int c, int ks) {
#pragma unroll
  for (int b = 0; b < 2; ++b) { const u32 a = r.a[wslot(P, c) >> 1][b] + (wslot(P, c) & 1) * 32768 + ks * 2048; f[b] = trd2(a, a + 1024); }
}
template <int P, int C, int F> DI void w_round(const WSrc& s, const WA& a0, u32x4 (&wr)[16][2]) {
#pragma unroll
  for (int k = 0; k < 4; ++k) { w_frag<P>(a0, wr[4 * C + k], C, k); SB(); if constexpr (F < 8) w_one<P>(s, F, k); SB(); }
}
DI void w_stage(const WA& a0, u32x4 (&wr)[16][2], int k0, int k1) {
#pragma unroll
  for (int k = k0; k < k1; ++k) w_frag<1>(a0, wr[k], k >> 2, k & 3);
}
template <int C0> DI void w_flag(const WSrc& s, int w, u32 tag, u32 fv, u32x4 rws, u32 flag) {
  if constexpr (C0 < 6) w_issue<1>(s, C0);
  vmbar<(C0 < 6 ? 4 : 0)>();
  if (w == 0) st32c(tag, fv, rws, flag);
#pragma unroll
  for (int c = C0 + 1; c < 6; ++c) w_issue<1>(s, c);
}
template <int N> DI void vmbar0(int w) { if (w == 0) vmwait<N + 1>(); else vmwait<N>(); bar(); }
struct DSrc { u32x4 rs; u32 voff, sb, rot; };
template <int I> DI void d_issue(u32x4 (&d)[32], const DSrc& s) { ld128c<(I & 3) * 1024>(d[I], s.voff, s.rs, s.sb + (((I >> 2) + s.rot) & 7) * 4096); }
constexpr int KB = 32 - WIN;
template <int P, int K> DI void wr_steps(u32x4 (&d)[32], u32x4 (&wr)[16][2], const WA& a0, const DSrc& s, const WSrc& nx, u32 sig, f32x16& c0, f32x16& c1) {
  if constexpr (K < 32) {
    if constexpr (K == KB) {
      if constexpr (P == 0) bar();
      else { asm volatile("s_waitcnt lgkmcnt(0)" ::: "memory"); *(volatile LAS u32*)sig = 1; }
    }
    if constexpr (K % 8 == 0) __builtin_amdgcn_s_setprio(3 - K / 8);
    vmwait<(P == 0 || K < KB ? WIN - 1 : 31 - K)>(d[K]);
    c0 = mfma(wr[K & 15][0], d[K], c0); c1 = mfma(wr[K & 15][1], d[K], c1);
    asm volatile("" : "+v"(c0), "+v"(c1));
    if constexpr (K + WIN < 32) d_issue<K + WIN>(d, s);
    if constexpr (P == 0 && K == 1) bar();
    if constexpr (P == 0 && K >= KB) w_one<1>(nx, (K - KB) >> 2, (K - KB) & 3);
    if constexpr (K >= 1 && K <= 16) w_frag<P>(a0, wr[K - 1], 4 + ((K - 1) >> 2), (K - 1) & 3);
    wr_steps<P, K + 1>(d, wr, a0, s, nx, sig, c0, c1);
  }
}
template <int I> DI void d_first(u32x4 (&d)[32], const DSrc& s) { if constexpr (I < WIN) { d_issue<I>(d, s); d_first<I + 1>(d, s); } }
template <int P> DI void wr_gemm(u32x4 (&wr)[16][2], const WA& a0, const DSrc& s, const WSrc& nx, u32 sig, f32x16& c0, f32x16& c1) {
  u32x4 d[32];
  d_first<0>(d, s);
  c0 = splat(0.f); c1 = splat(0.f);
  wr_steps<P, 0>(d, wr, a0, s, nx, sig, c0, c1);
  if constexpr (P == 0) {
#pragma unroll
    for (int i = WIN; i < 12; ++i) w_one<1>(nx, i >> 2, i & 3);
  }
}
constexpr u32 RSL = 1088;
template <u32 RS> DI void red_write(u32 a, u32 bs, const f32x16& c0, const f32x16& c1) {
#pragma unroll
  for (int q = 0; q < 4; ++q) {
    *(LAS f32x4*)(a + q * RS) = f32x4{c0[4 * q], c0[4 * q + 1], c0[4 * q + 2], c0[4 * q + 3]};
    *(LAS f32x4*)(a + bs + q * RS) = f32x4{c1[4 * q], c1[4 * q + 1], c1[4 * q + 2], c1[4 * q + 3]};
  }
}

template <int O, int I> DI void put16(const f32x16& c) {
  if constexpr (I < 16) { asm volatile("ds_write_addtid_b32 %0 offset:%1" ::"v"(c[I]), "i"(O + (I >> 1) * 768 + (I & 1) * 272) : "memory"); put16<O, I + 1>(c); }
}

constexpr u32 AST = 49152;
struct ASrc { u32x4 rx, rw; u32 vx0, vx1, vw, vq, sx, s0, s1, sq, rot, L; int w; };
DI void a_one(const ASrc& s, int c, int j) {
  const u32 st = s.L + (c % 3) * AST, cc = (c + s.rot) & 7;
  if (j < 4) glds(st + s.w * 4096 + j * 1024, (j & 1) ? s.vx1 : s.vx0, s.rx, s.sx + j * 8192 + cc * 128);
  else glds(st + 32768 + (j - 4) * 8192 + s.w * 1024, s.vw, s.rw, (j == 4 ? s.s0 : s.s1) + cc * (64 * 3072));
}
template <bool KV, int C, int J> DI void a_pref(const ASrc& s, const WSrc& nx) {
  if constexpr (J < 0) {}
  else if constexpr (C < 8) a_one(s, C, J);
  else if constexpr (C == 8) { if constexpr (KV && J < 4) glds(s.L + 98304 + s.w * 4096 + J * 1024, s.vq, s.rw, s.sq + J * (16 * 3072)); }
  else w_one<1>(nx, (6 * (C - 9) + J) >> 2, (6 * (C - 9) + J) & 3);
}
constexpr int a_gap(int C, int g) {
  const int t0[6] = {5, 6, 9, 10, 13, 14}, t1[6] = {1, 3, 6, 9, 11, 14}, t2[6] = {1, 2, -1, -1, -1, -1};
  for (int j = 0; j < 6; ++j) if ((C == 2 ? t0[j] : C == 10 ? t2[j] : t1[j]) == g) return j;
  return -1;
}
struct AMisc { u32x4 rws; u32 cfoff, tag, rdy, clr; int lane; };
struct AAd { u32 x0[4], x1[4], wa; };
template <bool KV, int T> DI void a_steps(const ASrc& s, const WSrc& nx, AAd& ad, u32x4 (&x0)[33], u32x4 (&f)[2][3], f32x16 (&c)[4], u32& cf, const AMisc& ms) {
  if constexpr (T < 32) {
    constexpr int C = T >> 2, K = T & 3, P = T & 1, N = (T + 1) & 3;
    if constexpr (K == 3) {
      if constexpr (C < 6) vmbar<6 + ((KV && C == 1) || C == 3)>(); else if constexpr (C == 6) vmbar<KV ? 5 : 0>(); else vmbar<6>();
      if constexpr (C < 7) {
        constexpr u32 dl = (C + 1) % 3 ? AST : 0u - 2 * AST;
#pragma unroll
        for (int k = 0; k < 4; ++k) { ad.x0[k] += dl; ad.x1[k] += dl; }
        ad.wa += dl;
        asm volatile("" : "+v"(ad.x0[0]), "+v"(ad.x0[1]), "+v"(ad.x0[2]), "+v"(ad.x0[3]), "+v"(ad.x1[0]), "+v"(ad.x1[1]), "+v"(ad.x1[2]), "+v"(ad.x1[3]), "+v"(ad.wa));
      }
      if constexpr (C == 0 && KV) { if (s.w < 4) st128c(u32x4{~0u, ~0u, ~0u, ~0u}, ms.lane * 16, ms.rws, ms.clr); else st32c(~0u, (ms.lane & 31) * 4, ms.rws, ms.clr); }
      if constexpr (C == 2) { st32c(ms.tag, ms.rdy, ms.rws, 0); }
      if constexpr (C == 5 && KV) ld32c(cf, ms.cfoff, ms.rws, 0);
    }
    constexpr int PC = K == 3 ? C + 3 : C + 2, G0 = 4 * N;
    __builtin_amdgcn_s_setprio(3 - N);
    if constexpr (T < 31) { x0[T + 1] = lds128(ad.x0[N]); f[P ^ 1][1] = trd2(ad.wa + N * 2048, ad.wa + N * 2048 + 512); }
    SB();
    if constexpr (KV) c[0] = mfma(x0[T], f[P][1], c[0]); else c[0] = mfma(f[P][1], x0[T], c[0]);
    SB();
    if constexpr (T < 31) { f[P ^ 1][0] = lds128(ad.x1[N]); f[P ^ 1][2] = trd2(ad.wa + 8192 + N * 2048, ad.wa + 8192 + N * 2048 + 512); }
    a_pref<KV, PC, a_gap(PC, G0)>(s, nx);
    SB();
    if constexpr (KV) c[1] = mfma(f[P][0], f[P][1], c[1]); else c[1] = mfma(f[P][1], f[P][0], c[1]);
    SB();
    a_pref<KV, PC, a_gap(PC, G0 + 1)>(s, nx);
    SB();
    if constexpr (KV) c[2] = mfma(x0[T], f[P][2], c[2]); else c[2] = mfma(f[P][2], x0[T], c[2]);
    SB();
    a_pref<KV, PC, a_gap(PC, G0 + 2)>(s, nx);
    SB();
    if constexpr (KV) c[3] = mfma(f[P][0], f[P][2], c[3]); else c[3] = mfma(f[P][2], f[P][0], c[3]);
    SB();
    a_pref<KV, PC, a_gap(PC, G0 + 3)>(s, nx);
    SB();
    a_steps<KV, T + 1>(s, nx, ad, x0, f, c, cf, ms);
  }
}

DI ASrc a_src(const char* x, const char* wqkv, u32 L, int lane, int w, int xcd, int slot) {
  const bool kv = slot < 30;
  const int ai = slot - 22, rc = (xcd & 6) + (kv ? ai >> 2 : slot - 30), hg = (xcd & 1) * 4 + (ai & 3), qj = xcd & 1;
  ASrc sa;
  sa.rx = srd(x, 2048 * 1024); sa.rw = srd(wqkv, 512 * 3072); sa.L = L; sa.w = w;
  sa.rot = xcd & 6;
  sa.vx0 = (lane >> 3) * 1024 + ((lane & 7) ^ (lane >> 4)) * 16; sa.vx1 = sa.vx0 ^ 64;
  sa.vw = (lane >> 3) * 3072 + ((lane & 7) ^ ((lane >> 2) & 4)) * 16;
  sa.vq = (lane >> 2) * 3072 + (lane & 3) * 16;
  sa.sx = (rc * 256 + w * 32) * 1024;
  sa.s0 = w * 8 * 3072 + (kv ? 1024 + hg * 128 : 512 + qj * 256);
  sa.s1 = sa.s0 + (kv ? 1024 : 128);
  sa.sq = ((w + sa.rot) & 7) * (64 * 3072) + hg * 64;
  return sa;
}
DI WSrc w1_src(const char* w1, u32 lds, int lane, int w, int xcd, int slot, int& ft, int& dt1) {
  const int gj = slot < 16 ? (xcd >> 1) * 16 + slot : 48 + (xcd >> 1) * 6 + slot;
  const u32 wch = (lane & 7) ^ ((lane >> 2) & 4);
  ft = gj >> 2; dt1 = (xcd & 1) * 4 + (gj & 3);
  WSrc s1;
  s1.rs = srd(w1, 2048 * 2752); s1.stride = 2752; s1.lds = lds;
  s1.voff = (lane >> 3) * 2752 + (ft == 21 ? (wch & 3) : wch) * 16;
  s1.sbase = ((w >> 1) * 512 + (w & 1) * 32) * 2752 + ft * 128;
  s1.rot = (2 * (dt1 + ft)) & 7;
  return s1;
}
template <bool KV> DI u32 attn(u32 ev, const char* x, const char* wqkv, u32x4 rws, const WSrc& s2, u32 L, int lane, int w, int xcd, int slot, u32 wa0, u32x4 (&wr)[16][2]) {
  const int hh = lane >> 5, col = lane & 31, q4 = (lane & 15) >> 2, mi = w >> 1, ni = w & 1;
  const int ai = slot - 22, rc = (xcd & 6) + (KV ? ai >> 2 : slot - 30), hg = (xcd & 1) * 4 + (ai & 3), qj = xcd & 1;
  const ASrc sa = a_src(x, wqkv, L, lane, w, xcd, slot);
  AAd ad;
#pragma unroll
  for (int k = 0; k < 4; ++k) { ad.x0[k] = L + (mi * 64 + ni * 32 + col) * 128 + ((2 * k + hh) ^ ((col >> 1) & 7)) * 16; ad.x1[k] = ad.x0[k] ^ 4096; }
  ad.wa = L + 32768 + (8 * hh + q4) * 128 + 64 * ((q4 >> 1) ^ ni) + 32 * ((lane >> 4) & 1) + 8 * (lane & 3);
  const int pr = hg + (mi & 1) * 8;
  const bool ga = w < 4;
  const u32 cfoff = ga ? WS_F + xcd * FRS + (128 + (pr >> 1) * 8 + (lane & 7)) * 128 : OOB;
  asm volatile("" : "+v"(ad.x0[0]), "+v"(ad.x0[1]), "+v"(ad.x0[2]), "+v"(ad.x0[3]), "+v"(ad.x1[0]), "+v"(ad.x1[1]), "+v"(ad.x1[2]), "+v"(ad.x1[3]), "+v"(ad.wa)::"memory");
  const u32 tag = tag_wait<6>(ev);
  bar();
  const AMisc ms = {rws, cfoff, tag, lane == 0 ? WS_F + w * FRS + (KV ? 128 + hg * 8 + rc : 192 + qj * 8 + rc) * 128 : OOB, w < 4 ? WS_KV + ((hg * 2 + (w >> 1)) * 8 + rc) * 2048 + (w & 1) * 1024 : WS_Z + ((hg * 2 + (w & 1)) * 8 + rc) * 128, lane};
  u32x4 x0[33], f[2][3]; f32x16 c[4]; u32 cf;
  c[0] = c[1] = splat(KV ? 1.f : 1.000001f); c[2] = c[3] = splat(KV ? 0.f : 1.000001f);
  x0[0] = lds128(ad.x0[0]); f[0][0] = lds128(ad.x1[0]); f[0][1] = trd2(ad.wa, ad.wa + 512); f[0][2] = trd2(ad.wa + 8192, ad.wa + 8192 + 512);
  a_steps<KV, 0>(sa, s2, ad, x0, f, c, cf, ms);
  __builtin_amdgcn_s_setprio(0);
  asm volatile("" : "+v"(c[0]), "+v"(c[1]), "+v"(c[2]), "+v"(c[3]));
  const u32 pfl = WS_F + xcd * FRS + (512 + rc * 16 + 8 + qj) * 128;
  if constexpr (!KV) {
#pragma unroll
    for (int j = 0; j < 4; ++j)
#pragma unroll
      for (int h = 0; h < 2; ++h) { st128c(phi8<true>(c[j], 8 * h), lane * 16 + qj * 8192 + ni * 2048, rws, WS_P + (rc * 8 + (w ^ (j & 1))) * 16384 + ((j & 2) * 2 + h) * 1024); SB(); }
    asm volatile("s_barrier" ::: "memory");
    w_issue<1>(s2, 2);
    vmbar<4>();
    if (w == 0) st32c(tag, lane == 0 ? 0 : OOB, rws, pfl);
    if (blockIdx.x == 255 && w == 0) {
#pragma unroll 1
      for (int i = 0; i < 4; ++i) poll(WS_F + 7 * FRS + (i * 64 + lane) * 128, rws, tag);
      st32c(tag, lane == 0 ? 0 : OOB, rws, WS_E);
    }
    vmbar<0>();
    w_stage(w_addr(wa0), wr, 0, 12);
    bar();
    w_flag<3>(s2, w, tag, OOB, rws, 0);
    return tag;
  }
  const u32 S = L + 65536, KT = S + 14336, ZT = KT + 2048;
  if (w >= 4) w_issue<1>(s2, 2);
  const u32 on = ((lane >> 4) ^ (lane >> 2)) & 1 ? 0 : 0x3f803f80u;
  const u32x4 cs = {on, on, on, on};
  f32x16 d1 = splat(0.f); f32x4 d2 = {64e-6f, 64e-6f, 64e-6f, 64e-6f}, d3 = {0.f, 0.f, 0.f, 0.f};
  u32x4 p[4];
#pragma unroll
  for (int i = 0; i < 4; ++i) {
    const f32x16& k = c[i >> 1]; const int o = 8 * (i & 1);
    p[i][0] = phi2<false>(k[o], k[o + 1]); SB();
    if (i) d2 = mfma16(cs, p[i - 1], d2);
    const u32x4 v = pack8(c[2 + (i >> 1)], o);
    p[i][1] = phi2<false>(k[o + 2], k[o + 3]); SB();
    d3 = mfma16(cs, v, d3);
    p[i][2] = phi2<false>(k[o + 4], k[o + 5]); p[i][3] = phi2<false>(k[o + 6], k[o + 7]); SB();
    d1 = mfma(p[i], v, d1);
    SB(); if (w < 4) w_one<1>(s2, 2, i); SB();
  }
  d2 = mfma16(cs, p[3], d2);
  const bool lo = col < 16;
  f32x4 k0 = {lo ? d1[0] : d1[8], lo ? d1[1] : d1[9], lo ? d1[2] : d1[10], lo ? d1[3] : d1[11]};
  f32x4 k1 = {lo ? d1[4] : d1[12], lo ? d1[5] : d1[13], lo ? d1[6] : d1[14], lo ? d1[7] : d1[15]};
  const float ev6 = d3[0] * 1e-6f;
  k0 = add4(k0, f32x4{ev6, ev6, ev6, ev6}); k1 = add4(k1, f32x4{ev6, ev6, ev6, ev6});
  const float zp = d2[0];
  if (w >= 2) { *(LAS f32x4*)(S + (w - 2) * 2048 + lane * 16) = k0; *(LAS f32x4*)(S + (w - 2) * 2048 + 1024 + lane * 16) = k1; *(LAS float*)(S + 12288 + (w - 2) * 256 + lane * 4) = zp; }
  vmbar<0>();
  if (w < 2) {
    const u32 a = S + w * 2048 + lane * 16, z = S + 12288 + w * 256 + lane * 4;
    k0 = add4(add4(k0, ldsf4(a)), add4(ldsf4(a + 4096), ldsf4(a + 8192))); k1 = add4(add4(k1, ldsf4(a + 1024)), add4(ldsf4(a + 5120), ldsf4(a + 9216)));
    const float t = add1(add1(zp, ldsf(z)), add1(ldsf(z + 512), ldsf(z + 1024)));
    u32x4 p0 = __builtin_bit_cast(u32x4, k0), p1 = __builtin_bit_cast(u32x4, k1);
#pragma unroll
    for (int i = 0; i < 4; ++i) { p0[i] = umin(p0[i], ~1u); p1[i] = umin(p1[i], ~1u); }
    const u32 o = (hg * 2 + w) * 8 + rc;
    st128c(p0, lane * 16, rws, WS_KV + o * 2048); st128c(p1, lane * 16, rws, WS_KV + o * 2048 + 1024); st32c(umin(__float_as_uint(t), ~1u), col * 4, rws, WS_Z + o * 128);
  }
  u32x4 pq[2][2], t[8]; u32x2 z; u32 pf;
  u32 qa = L + 98304 + (8 * hh + q4) * 64 + 32 * ((lane >> 4) & 1) + 8 * (lane & 3);
  asm volatile("" : "+v"(qa), "+v"(cf));
  u32x4 qf[4];
#pragma unroll
  for (int i = 0; i < 3; ++i) qf[i] = trd2(qa + i * 1024, qa + i * 1024 + 256);
  f32x16 cq = splat(1.000001f);
#pragma unroll
  for (int i = 0; i < 32; ++i) {
    SB();
    if (i < 29) qf[(i + 3) & 3] = trd2(qa + (i + 3) * 1024, qa + (i + 3) * 1024 + 256);
    SB();
    cq = mfma(qf[i & 3], x0[i], cq);
  }
  SB();
  auto sweep = [&]() {
#pragma unroll
    for (int r = 0; r < 8; ++r) ld128c<0>(t[r], lane * 16, rws, WS_KV + (pr * 8 + r) * 2048 + ni * 1024);
    asm volatile("buffer_load_dwordx2 %0, %1, %2, %3 offen sc1" : "=v"(z) : "v"((lane >> 3) * 128 + (lane & 7) * 8), "s"(rws), "s"(WS_Z + pr * 1024 + ni * 64) : "memory");
  };
  asm volatile("" : "=v"(t[0]), "=v"(t[1]), "=v"(t[2]), "=v"(t[3]), "=v"(t[4]), "=v"(t[5]), "=v"(t[6]), "=v"(t[7]), "=v"(z));
  if (ga && __builtin_amdgcn_ballot_w64(cf != tag) != 0) poll(cfoff, rws, tag);
  ld32c(pf, pfl, rws, 0);
  if (mi == 0) sweep();
  pq[0][0] = phi8<true>(cq, 0); pq[0][1] = phi8<true>(cq, 8);
  asm volatile("" : "+v"(pq[0][0]), "+v"(pq[0][1]));
  w_stage(w_addr(wa0), wr, 0, 12);
  SB();
  if (mi == 1) sweep();
  if (!ga) vmwait<0>();
  asm volatile("s_waitcnt vmcnt(9)" : "+v"(pf)::"memory");
  if (__builtin_amdgcn_ballot_w64(pf != tag) != 0) poll(pfl, rws, tag);
  ld128c<0>(pq[1][0], lane * 16, rws, WS_P + (rc * 8 + w) * 16384 + hg * 2048); ld128c<1024>(pq[1][1], lane * 16, rws, WS_P + (rc * 8 + w) * 16384 + hg * 2048);
#define GW(N) asm volatile("s_waitcnt vmcnt(%9)" : "+v"(t[0]), "+v"(t[1]), "+v"(t[2]), "+v"(t[3]), "+v"(t[4]), "+v"(t[5]), "+v"(t[6]), "+v"(t[7]), "+v"(z) : "i"(N) : "memory")
  GW(2);
  if (ga) {
    for (int it = 0; it < SPIN_MAX; ++it) {
      u32 m = umax(z[0], z[1]);
#pragma unroll
      for (int r = 0; r < 8; ++r) m = umax(umax(umax(m, t[r][0]), t[r][1]), umax(t[r][2], t[r][3]));
      if (__builtin_amdgcn_ballot_w64(m == ~0u) == 0) break;
      sweep();
      GW(0);
    }
    f32x4 a = __builtin_bit_cast(f32x4, t[0]);
#pragma unroll
    for (int r = 1; r < 8; ++r) a += __builtin_bit_cast(f32x4, t[r]);
    float zs = fold32(__uint_as_float(z[0]), __uint_as_float(z[1]));
    zs = dppadd<0x128>(fold16(zs, zs));
    const int pl = mi, zf = 2 * (lane & 7) + hh;
    *(LAS u32x2*)(KT + pl * 1024 + lane * 16 + ni * 8) = u32x2{pk(a[0], a[1]), pk(a[2], a[3])};
    *(LAS unsigned short*)(ZT + pl * 64 + ni * 32 + ((zf >> 2) & 1) * 16 + ((zf & 3) + 4 * (zf >> 3)) * 2) = (unsigned short)pk(zs, zs);
  }
  bar();
  f32x16 dd[2][2];
#pragma unroll
  for (int j = 0; j < 2; ++j) {
    if (j) asm volatile("s_waitcnt vmcnt(0)" : "+v"(pq[1][0]), "+v"(pq[1][1])::"memory");
#pragma unroll
    for (int h = 0; h < 2; ++h) dd[j][h] = mfma(lds128(((col < 16) == (h == 0)) ? KT + j * 1024 + lane * 16 : ZT + j * 64 + h * 32 + hh * 16), pq[j][h], splat(0.f));
  }
  SB();
  w_issue<1>(s2, 3);
  SB();
#pragma unroll
  for (int j = 0; j < 2; ++j)
#pragma unroll
    for (int h = 0; h < 2; ++h) {
      const int o = 8 * h;
      const f32x16& e = dd[j][h];
      const float rd = __builtin_amdgcn_rcpf(e[8 - o] + 1e-6f);
      const u32x4 ov = {pk(e[o] * rd, e[o + 1] * rd), pk(e[o + 2] * rd, e[o + 3] * rd), pk(e[o + 4] * rd, e[o + 5] * rd), pk(e[o + 6] * rd, e[o + 7] * rd)};
      st128c(ov, lane * 16, rws, WS_O + (rc * 8 + w) * 32768 + ((hg + 8 * j) * 2 + h) * 1024);
    }
  bar();
  w_flag<4>(s2, w, tag, lane < 2 ? ((rc & 6) + lane) * FRS : OOB, rws, WS_F + (512 + rc * 16 + hg) * 128);
  return tag;
}

__global__ __launch_bounds__(512, 2) void fused_kernel(const char* __restrict__ x, const char* __restrict__ wqkv, const char* __restrict__ wo,
                                                        const char* __restrict__ w1, const char* __restrict__ b1, const char* __restrict__ w2,
                                                        const char* __restrict__ b2, const char* __restrict__ gw, char* __restrict__ out, char* ws) {
  __shared__ __attribute__((aligned(1024))) char sm[163840];
  u32 ev;
  {
    unsigned long long pc; u32 cn;
    asm volatile("s_getpc_b64 %0\n.Lfk_pc:\n\ts_mov_b32 %1, .Lfk_end-.Lfk_pc" : "=s"(pc), "=s"(cn));
    const u32 vo = (blockIdx.x >> 3 & 15) == 0 ? threadIdx.x * 128 : OOB;
    asm volatile("buffer_load_dword %0, %1, %2, 0 offen" : "=v"(ev) : "v"(vo), "s"(srd((const void*)pc, cn)) : "memory");
    SB();
  }
  asm volatile("" : "+s"(x), "+s"(wqkv), "+s"(wo), "+s"(w1), "+s"(b1), "+s"(w2), "+s"(b2), "+s"(gw), "+s"(out), "+s"(ws));
  const u32 L = (u32)(size_t)(LAS char*)sm;
  const int lane = threadIdx.x & 63, w = __builtin_amdgcn_readfirstlane(threadIdx.x >> 6);
  int xcd = blockIdx.x & 7, slot = blockIdx.x >> 3;
  const int hh = lane >> 5;
  const u32x4 rws = srd(ws, WS_END);
  u32x4 xr[2], gr;
  asm volatile("s_nop 4" : "=v"(xr[0]), "=v"(xr[1]), "=v"(gr)::"memory");
  ev_load(ev, rws);
  if (slot < 22) {
    int ft, dt1;
    const WSrc s1 = w1_src(w1, L + w * 4096, lane, w, xcd, slot, ft, dt1);
    if (slot < 16) {
      const int r = (xcd * 16 + slot) * 16 + w;
      const u32x4 rx = srd(x, 2048 * 1024);
      ld128(xr[0], lane * 16, rx, r * 1024); ld128(xr[1], lane * 16, rx, (r + 8) * 1024);
      ld128(gr, lane * 16, srd(gw, 1024), 0);
      asm volatile("s_barrier" ::: "memory");
    }
    w_issue<0>(s1, 0);
  } else {
    const ASrc sa = a_src(x, wqkv, L, lane, w, xcd, slot);
#pragma unroll
    for (int j = 0; j < 6; ++j) a_one(sa, 0, j);
    asm volatile("s_barrier" ::: "memory");
#pragma unroll
    for (int j = 0; j < 6; ++j) a_one(sa, 1, j);
  }
  asm volatile("" : "+s"(slot), "+s"(xcd));
  const int g = w >> 1, hf = w & 1, q4 = (lane & 15) >> 2;
  const u32 wch = (lane & 7) ^ ((lane >> 2) & 4);
  const u32 wa0 = L + g * 8192 + (4 * hh + q4) * 128 + 64 * (q4 >> 1) + 32 * ((lane >> 4) & 1) + 8 * (lane & 3);

  const int dt = (xcd & 1) * 4 + (slot & 3), stl = (xcd >> 1) * 8 + (slot >> 2);
  WSrc s2;
  s2.rs = g < 3 ? srd(w2, 1376 * 4096) : srd(wo, 512 * 1024); s2.stride = g < 3 ? 4096 : 1024; s2.lds = L + g * 8192 + hf * 4096;
  s2.voff = (lane >> 3) * s2.stride + wch * 16;
  s2.sbase = g < 3 ? (g * 512 + hf * 32) * 4096 + stl * 128 : hf * 32 * 1024 + dt * 128;
  s2.rot = 2 * ((slot + (slot >> 2)) & 3);
  u32 tag;
  u32x4 wr[16][2];

  if (slot >= 30) tag = attn<false>(ev, x, wqkv, rws, s2, L, lane, w, xcd, slot, wa0, wr);
  else if (slot >= 22) tag = attn<true>(ev, x, wqkv, rws, s2, L, lane, w, xcd, slot, wa0, wr);
  else {
    const int gi = slot < 16 ? xcd * 16 + slot : 112 + xcd * 6 + slot;
    int ft, dt1;
    const WSrc s1 = w1_src(w1, s2.lds, lane, w, xcd, slot, ft, dt1);
    if (slot < 16) {
      const int sb = xcd * 16 + slot;
      tag = tag_wait<4>(ev);
      asm volatile("" : "+v"(xr[0]), "+v"(xr[1]), "+v"(gr)::"memory");
      f32x2 v[2][4], t; float s[2], gd[2], q[2];
#pragma unroll
      for (int j = 0; j < 2; ++j) {
#pragma unroll
        for (int i = 0; i < 4; ++i) v[j][i] = f32x2{blo(xr[j][i]), bhi(xr[j][i])};
        t = (v[j][0] + v[j][1]) + (v[j][2] + v[j][3]); s[j] = t[0] + t[1];
      }
      const float t0 = fold32(s[0], s[1]), t1 = rowsum(fold16(t0, t0)) * (1.f / 512.f);
#pragma unroll
      for (int j = 0; j < 2; ++j) {
        t = f32x2{0.f, 0.f};
#pragma unroll
        for (int i = 0; i < 4; ++i) t = __builtin_elementwise_fma(v[j][i], f32x2{blo(gr[i]), bhi(gr[i])}, t);
        gd[j] = t[0] + t[1];
        const float m = rdl(t1, 32 * j);
        t = f32x2{0.f, 0.f};
#pragma unroll
        for (int i = 0; i < 4; ++i) { v[j][i] -= m; t = __builtin_elementwise_fma(v[j][i], v[j][i], t); }
        q[j] = t[0] + t[1];
      }
      const float t2 = rowsum(fold16(fold32(q[0], q[1]), fold32(gd[0], gd[1])));
      const float rs = __builtin_amdgcn_rsqf(t2 * (1.f / 512.f) + 1e-5f);
#pragma unroll
      for (int j = 0; j < 2; ++j) {
        const float r = rdl(rs, 32 * j);
#pragma unroll
        for (int i = 0; i < 4; ++i) v[j][i] *= r;
        *(LAS u32x4*)(L + (w + 8 * j) * 1088 + lane * 16) = u32x4{pk(v[j][0][0], v[j][0][1]), pk(v[j][1][0], v[j][1][1]), pk(v[j][2][0], v[j][2][1]), pk(v[j][3][0], v[j][3][1])};
      }
      const float gt = __builtin_amdgcn_rcpf(1.f + __builtin_amdgcn_exp2f(t2 * -1.4426950408889634f));
      bar();
      const u32 ta = L + (4 * hh + q4) * 1088 + w * 128 + 32 * ((lane >> 4) & 1) + 8 * (lane & 3);
      const u32x4 h0 = trd2(ta, ta + 8 * 1088), h1 = trd2(ta + 64, ta + 64 + 8 * 1088);
      st32c(__float_as_uint(gt), (lane & 31) == 16 ? lane & 32 : OOB, rws, WS_G + (sb * 16 + w) * 4);
      st128c(h0, lane * 16, rws, WS_H + w * 262144 + sb * 1024);
      st128c(h1, lane * 16, rws, WS_H + w * 262144 + 131072 + sb * 1024);
      asm volatile("s_barrier" ::: "memory");
      w_issue<0>(s1, 1);
      vmbar<4>();
    } else {
      u32 tl;
      asm volatile("buffer_load_dword %0, %1, %2, 0 offen" : "=v"(tl) : "v"((lane << 18) + 2048),
                   "s"(w == 0 ? srd(w2, 1376 * 4096) : w == 1 ? srd(wo, 512 * 1024) : w == 2 ? srd(out, 2048 * 1024) : w == 3 ? srd(b1, 2752) : w == 4 ? srd(b2, 4096) : srd(ws, w == 5 ? WS_END : 0)) : "memory");
      w_issue<0>(s1, 1);
      tag = tag_wait<4>(ev);
      asm volatile("" ::"v"(tl));
      bar();
    }
    const WA a0 = w_addr(wa0);
    if (w == 0) st32c(tag, lane < 8 ? lane * FRS : OOB, rws, WS_F + (slot < 16 ? gi : gi + 80) * 128);
    w_round<0, 0, 2>(s1, a0, wr); w_issue<0>(s1, 3);
    vmbar0<8>(w);
    w_round<0, 1, 4>(s1, a0, wr);
    vmbar<8>();
    w_round<0, 2, 5>(s1, a0, wr);
    vmbar<8>();
    const u32 hfl = hf ? WS_F + xcd * FRS + (g * 32 + (lane & 31)) * 128 : OOB;
    u32 pf; u32x2 bb0, bb1;
    ld32c(pf, hfl, rws, 0);
    asm volatile("buffer_load_dwordx2 %0, %2, %3, %4 offen\n\tbuffer_load_dwordx2 %1, %2, %3, %4 offen offset:16" : "=&v"(bb0), "=&v"(bb1) : "v"(hh * 8), "s"(srd(b1, 2752)), "s"(ft * 128 + g * 32) : "memory");
    w_round<0, 3, 6>(s1, a0, wr); w_issue<0>(s1, 7);
    asm volatile("s_waitcnt vmcnt(8)" : "+v"(pf), "+v"(bb0), "+v"(bb1)::"memory");
    if (hf && __builtin_amdgcn_ballot_w64(pf != tag) != 0) poll(hfl, rws, tag);
    bar();
    f32x16 c0, c1;
    wr_gemm<0>(wr, a0, DSrc{rws, (u32)lane * 16, WS_H + (dt1 * 2 + hf) * 131072 + g * 32768, s1.rot}, s2, 0, c0, c1);
    const u32 R = L + 65536 + hf * 32768 + lane * 16;
    f32x4 lo = {blo(bb0[0]), bhi(bb0[0]), blo(bb0[1]), bhi(bb0[1])}, hi = {blo(bb1[0]), bhi(bb1[0]), blo(bb1[1]), bhi(bb1[1])};
#pragma unroll
    for (int q = 0; q < 4; ++q) {
      const f32x16& c = q < 2 ? c0 : c1; const int o = (q & 1) * 8;
      const f32x4 a = {c[o], c[o + 1], c[o + 2], c[o + 3]}, b = {c[o + 4], c[o + 5], c[o + 6], c[o + 7]};
      if (q == g) { lo += a; hi += b; }
      else { const u32 t = R + q * 8192 + ((g - q) & 3) * 2048; *(LAS f32x4*)t = a; *(LAS f32x4*)(t + 1024) = b; }
    }
    vmbar<0>();
#pragma unroll
    for (int k = 1; k < 4; ++k) { lo += ldsf4(R + g * 8192 + k * 2048); hi += ldsf4(R + g * 8192 + k * 2048 + 1024); }
    const u32x4 uv = {pk(gelu(lo[0]), gelu(lo[1])), pk(gelu(lo[2]), gelu(lo[3])), pk(gelu(hi[0]), gelu(hi[1])), pk(gelu(hi[2]), gelu(hi[3]))};
    st128c(uv, lane * 16, rws, WS_U + (dt1 * 2 + hf) * UDB + (ft * 4 + g) * 1024);
    w_stage(a0, wr, 0, 12);
    bar();
    w_flag<3>(s2, w, tag, lane < 4 ? (lane * 2 + (dt1 >> 2)) * FRS : OOB, rws, WS_F + (256 + dt1 * 32 + ft) * 128);
  }

  {
    asm volatile("" : "+s"(s2.lds), "+s"(s2.rot), "+s"(s2.sbase));
    const int sr = w * 8 + (lane >> 3), cq = lane & 7;
    u32 b2v;
    asm volatile("buffer_load_ushort %0, %1, %2, %3 offen" : "=v"(b2v) : "v"(sr * 2), "s"(srd(b2, 4096)), "s"(stl * 128) : "memory");
    const u32 GT = L + 131072, SG = GT + 256;
    const WA a0 = w_addr(wa0);
    f32x16 c0, c1;
    const int nf = g == 2 ? 6 : 8;
    const u32 fo = WS_F + xcd * FRS + ((g < 3 ? 256 + dt * 32 + g * 8 : 512 + (stl >> 2) * 16) + (lane < nf ? lane : 0)) * 128;
    w_issue<1>(s2, 6);
    w_issue<1>(s2, 7);
    vmbar0<17>(w);
    w_round<1, 3, 8>(s2, a0, wr);
    bar();
    *(volatile LAS u32*)(SG + w * 4) = 0;
    poll_bar(fo, rws, tag);
    asm volatile("" : "+v"(b2v));
    u32 gv;
    ld32c(gv, w == 0 ? lane * 4 : OOB, rws, WS_G + stl * 256);
    const u32 ub = WS_U + (dt * 2 + hf) * UDB;
    wr_gemm<1>(wr, a0, DSrc{srd(ws, g < 3 ? ub + 86 * 1024 : WS_END), (u32)lane * 16, g < 3 ? ub + g * 32768 : WS_O + (stl * 2 + hf) * 32768, s2.rot}, s2, SG + w * 4, c0, c1);
    asm volatile("" : "+v"(gv));
    if (w == 0) *(LAS u32*)(GT + lane * 4) = gv;
    for (int it = 0; it < SPIN_MAX * 16 && *(volatile LAS u32*)(SG + (w ^ 1) * 4) == 0; ++it) {}
    const u32 pb = L + g * 8192 + hf * 32768;
    if (g < 3) {
      asm volatile("s_mov_b32 m0, %0\n\ts_nop 15\n\ts_nop 7" ::"s"(pb + hf * 128) : "memory");
      put16<0, 0>(c0);
      asm volatile("s_mov_b32 m0, %0\n\ts_nop 0" ::"s"(pb + hf * 128 + 6144) : "memory");
      put16<65536 - 6144, 0>(c1);
    } else red_write<RSL>(pb + lane * 16, 65536, c0, c1);
    bar();
    const int sl = lane >> 3, i = (sl & 3) + 4 * (w & 3);
    const u32 ya = L + (cq >> 2) * (32768 + 128) + (w >> 2) * 65536 + (i >> 1) * 768 + (i & 1) * 272 + (sl >> 2) * 128 + (cq & 3) * 32;
    const f32x4 y0 = (ldsf4(ya) + ldsf4(ya + 8192)) + ldsf4(ya + 16384), y1 = (ldsf4(ya + 16) + ldsf4(ya + 8208)) + ldsf4(ya + 16400);
    const u32 aa = L + 24576 + (w >> 2) * 32768 + (cq >> 2) * 65536 + (cq & 3) * RSL + (sr & 31) * 16;
    const f32x4 a0v = ldsf4(aa), a1v = ldsf4(aa + 512);
    const float gt = ldsf(GT + sr * 4), bb = blo(b2v), og = 1.f - gt;
    u32x4 ov;
    ov[0] = pk(gt * a0v[0] + og * (y0[0] + bb), gt * a0v[1] + og * (y0[1] + bb));
    ov[1] = pk(gt * a0v[2] + og * (y0[2] + bb), gt * a0v[3] + og * (y0[3] + bb));
    ov[2] = pk(gt * a1v[0] + og * (y1[0] + bb), gt * a1v[1] + og * (y1[1] + bb));
    ov[3] = pk(gt * a1v[2] + og * (y1[2] + bb), gt * a1v[3] + og * (y1[3] + bb));
    asm volatile("buffer_store_dwordx4 %0, %1, %2, %3 offen nt" ::"v"(ov), "v"(sr * 1024 + cq * 16), "s"(srd(out, 2048 * 1024)), "s"(stl * 65536 + dt * 128) : "memory");
    asm volatile(".Lfk_end:");
  }
}

extern "C" void kernel_launch(void* const* d_in, const int* in_sizes, int n_in, void* d_out, int out_size, void* d_ws, size_t ws_size, hipStream_t stream) {
  fused_kernel<<<256, 512, 0, stream>>>((const char*)d_in[0], (const char*)d_in[1], (const char*)d_in[2], (const char*)d_in[4], (const char*)d_in[5],
                                        (const char*)d_in[6], (const char*)d_in[7], (const char*)d_in[8], (char*)d_out, (char*)d_ws);
}
```

```cpp
#include <hip/hip_runtime.h>
#include <hip/hip_bf16.h>


#pragma clang diagnostic ignored "-Wint-to-pointer-cast"
#define DI __device__ __forceinline__
#define LAS __attribute__((address_space(3)))
#define SB() __builtin_amdgcn_sched_barrier(0)
typedef unsigned u32;
typedef u32 u32x4 __attribute__((ext_vector_type(4)));
typedef u32 u32x2 __attribute__((ext_vector_type(2)));
typedef float f32x2 __attribute__((ext_vector_type(2)));
typedef float f32x4 __attribute__((ext_vector_type(4)));
typedef float f32x16 __attribute__((ext_vector_type(16)));
typedef __bf16 bf16x8 __attribute__((ext_vector_type(8)));
typedef __bf16 bf16x2 __attribute__((ext_vector_type(2)));
typedef short s16x4 __attribute__((ext_vector_type(4)));

constexpr u32 WS_E = 0, WS_G = 32768;
constexpr u32 WS_F = 352u << 10, FRS = 641 * 128;
constexpr u32 WS_KV = 65536, WS_Z = WS_KV + 262144, WS_H = 1u << 20, WS_U = 3u << 20, WS_O = 5u << 20, WS_P = 7u << 20, WS_END = 8u << 20, OOB = 1u << 30;
constexpr u32 UDB = 88 * 1024;
constexpr int SPIN_MAX = 20000;
constexpr int WIN = 10;

DI u32x4 srd(const void* p, u32 bytes) { const unsigned long long a = (unsigned long long)p; return u32x4{(u32)a, (u32)(a >> 32), bytes, 0x00020000u}; }
DI f32x16 mfma(u32x4 a, u32x4 b, f32x16 c) { return __builtin_amdgcn_mfma_f32_32x32x16_bf16(__builtin_bit_cast(bf16x8, a), __builtin_bit_cast(bf16x8, b), c, 0, 0, 0); }
DI f32x4 mfma16(u32x4 a, u32x4 b, f32x4 c) { return __builtin_amdgcn_mfma_f32_16x16x32_bf16(__builtin_bit_cast(bf16x8, a), __builtin_bit_cast(bf16x8, b), c, 0, 0, 0); }
DI u32x2 trd(u32 a) { return __builtin_bit_cast(u32x2, __builtin_amdgcn_ds_read_tr16_b64_v4i16((LAS s16x4*)a)); }
DI u32x4 trd2(u32 a, u32 b) { const u32x2 x = trd(a), y = trd(b); return u32x4{x[0], x[1], y[0], y[1]}; }
DI u32x4 lds128(u32 a) { return *(LAS u32x4*)a; }
DI f32x4 ldsf4(u32 a) { return *(LAS f32x4*)a; }
DI float ldsf(u32 a) { return *(LAS float*)a; }
DI u32 pk(float a, float b) { return __builtin_bit_cast(u32, __builtin_convertvector(f32x2{a, b}, bf16x2)); }
DI float blo(u32 v) { return __uint_as_float(v << 16); }
DI float bhi(u32 v) { return __uint_as_float(v & 0xffff0000u); }
DI u32x4 pack8(const f32x16& x, int o) { return u32x4{pk(x[o], x[o + 1]), pk(x[o + 2], x[o + 3]), pk(x[o + 4], x[o + 5]), pk(x[o + 6], x[o + 7])}; }
DI f32x16 splat(float v) { return f32x16{v, v, v, v, v, v, v, v, v, v, v, v, v, v, v, v}; }
DI float add1(float a, float b) { float r; asm("v_add_f32 %0, %1, %2" : "=v"(r) : "v"(a), "v"(b)); return r; }
DI f32x4 add4(f32x4 a, f32x4 b) { return f32x4{add1(a[0], b[0]), add1(a[1], b[1]), add1(a[2], b[2]), add1(a[3], b[3])}; }
DI u32 umin(u32 a, u32 b) { return a < b ? a : b; }
DI u32 umax(u32 a, u32 b) { return a > b ? a : b; }

DI void glds(u32 lds, u32 voff, u32x4 rs, u32 soff) {
  asm volatile("s_mov_b32 m0, %0\n\ts_nop 0\n\tbuffer_load_dwordx4 %1, %2, %3 offen lds" ::"s"(lds), "v"(voff), "s"(rs), "s"(soff) : "memory");
}
template <int OFF> DI void ld128c(u32x4& d, u32 voff, u32x4 rs, u32 soff) {
  asm volatile("buffer_load_dwordx4 %0, %1, %2, %3 offen offset:%4 sc1" : "=v"(d) : "v"(voff), "s"(rs), "s"(soff), "i"(OFF) : "memory");
}
DI void ld128(u32x4& d, u32 voff, u32x4 rs, u32 soff) { asm volatile("buffer_load_dwordx4 %0, %1, %2, %3 offen" : "=v"(d) : "v"(voff), "s"(rs), "s"(soff) : "memory"); }
template <int OFF = 0> DI void ld32c(u32& d, u32 voff, u32x4 rs, u32 soff) { asm volatile("buffer_load_dword %0, %1, %2, %3 offen offset:%4 sc1" : "=v"(d) : "v"(voff), "s"(rs), "s"(soff), "i"(OFF) : "memory"); }
DI void st128c(u32x4 v, u32 voff, u32x4 rs, u32 soff) { asm volatile("buffer_store_dwordx4 %0, %1, %2, %3 offen sc1\n\ts_nop 1" ::"v"(v), "v"(voff), "s"(rs), "s"(soff) : "memory"); }
DI void st32c(u32 v, u32 voff, u32x4 rs, u32 soff) { asm volatile("buffer_store_dword %0, %1, %2, %3 offen sc1" ::"v"(v), "v"(voff), "s"(rs), "s"(soff) : "memory"); }
template <int N> DI void vmwait() { asm volatile("s_waitcnt vmcnt(%0)" ::"i"(N) : "memory"); }
template <int N> DI void vmwait(u32x4& a) { asm volatile("s_waitcnt vmcnt(%1)" : "+v"(a) : "i"(N) : "memory"); }
DI void bar() { asm volatile("s_waitcnt lgkmcnt(0)\n\ts_barrier" ::: "memory"); }
template <int N> DI void vmbar() { asm volatile("s_waitcnt vmcnt(%0) lgkmcnt(0)\n\ts_barrier" ::"i"(N) : "memory"); }

DI void ev_load(u32& e, u32x4 ws) { asm volatile("buffer_load_dword %0, off, %1, 0" : "+v"(e) : "s"(ws) : "memory"); }
template <int N> DI u32 tag_wait(u32 e) {
  u32 t;
  asm volatile("s_waitcnt vmcnt(%2)\n\tv_readfirstlane_b32 %0, %1\n\ts_nop 1" : "=s"(t) : "v"(e), "i"(N) : "memory");
  return t * 1664525u + 1013904223u;
}
DI void poll(u32 voff, u32x4 ws, u32 tag) {
  for (int it = 0; it < SPIN_MAX; ++it) {
    u32 f;
    asm volatile("buffer_load_dword %0, %1, %2, 0 offen sc1\n\ts_waitcnt vmcnt(0)" : "=&v"(f) : "v"(voff), "s"(ws) : "memory");
    if (__builtin_amdgcn_ballot_w64(f != tag) == 0) break;
    __builtin_amdgcn_s_sleep(4);
  }
}
DI void poll_bar(u32 voff, u32x4 ws, u32 tag) {
  u32 f;
  asm volatile("buffer_load_dword %0, %1, %2, 0 offen sc1\n\ts_waitcnt vmcnt(0) lgkmcnt(0)\n\ts_barrier" : "=&v"(f) : "v"(voff), "s"(ws) : "memory");
  if (__builtin_amdgcn_ballot_w64(f != tag) != 0) poll(voff, ws, tag);
}
template <int C> DI float dppadd(float v) { return v + __int_as_float(__builtin_amdgcn_update_dpp(0, __float_as_int(v), C, 0xf, 0xf, true)); }
DI float rowsum(float v) { v = dppadd<0xB1>(v); v = dppadd<0x4E>(v); v = dppadd<0x141>(v); return dppadd<0x140>(v); }
DI float rdl(float v, int l) { return __int_as_float(__builtin_amdgcn_readlane(__float_as_int(v), l)); }
DI float fold32(float a, float b) { const auto r = __builtin_amdgcn_permlane32_swap(__float_as_uint(a), __float_as_uint(b), false, false); return __uint_as_float(r[0]) + __uint_as_float(r[1]); }
DI float fold16(float a, float b) { const auto r = __builtin_amdgcn_permlane16_swap(__float_as_uint(a), __float_as_uint(b), false, false); return __uint_as_float(r[0]) + __uint_as_float(r[1]); }
template <bool E> DI u32 phi2(float a, float b) {
  constexpr float l2e = 1.4426950408889634f, c = -l2e * (E ? 1.000001f : 1.f);
  float e0 = __builtin_amdgcn_fmed3f(__builtin_amdgcn_exp2f(__builtin_fmaf(a, l2e, c)), 0.f, 1.f), e1 = __builtin_amdgcn_fmed3f(__builtin_amdgcn_exp2f(__builtin_fmaf(b, l2e, c)), 0.f, 1.f);
  if (E) { e0 += 1e-6f; asm("" : "+v"(e0)); e1 += 1e-6f; }
  return pk(__builtin_amdgcn_fmed3f(a, e0, 3.4028234664e38f), __builtin_amdgcn_fmed3f(b, e1, 3.4028234664e38f));
}
template <bool E> DI u32x4 phi8(const f32x16& x, int o) { return u32x4{phi2<E>(x[o], x[o + 1]), phi2<E>(x[o + 2], x[o + 3]), phi2<E>(x[o + 4], x[o + 5]), phi2<E>(x[o + 6], x[o + 7])}; }
DI float gelu(float v) {
  const float t = v * (-2.3022081981f - 0.1029432397f * v * v);
  return v * __builtin_amdgcn_rcpf(1.f + __builtin_amdgcn_exp2f(t));
}

constexpr int wslot(int P, int c) { return (c + (P && c < 3 ? 4 : 1)) % 5; }
struct WSrc { u32x4 rs; u32 voff, sbase, stride, lds, rot; };
template <int P> DI void w_one(const WSrc& s, int c, int i) { glds(s.lds + wslot(P, c) * 32768 + i * 1024, s.voff, s.rs, s.sbase + (64 * ((c + s.rot) & 7) + 8 * i) * s.stride); }
template <int P> DI void w_issue(const WSrc& s, int c) {
#pragma unroll
  for (int i = 0; i < 4; ++i) w_one<P>(s, c, i);
}
struct WA { u32 a[3][2]; };
DI WA w_addr(u32 a0) { WA r;
#pragma unroll
  for (int i = 0; i < 3; ++i) { r.a[i][0] = a0 + i * 65536; r.a[i][1] = (a0 ^ 64) + i * 65536; asm volatile("" : "+v"(r.a[i][0]), "+v"(r.a[i][1])); }
  return r; }
template <int P> DI void w_frag(const WA& r, u32x4 (&f)[2], int c, int ks) {
#pragma unroll
  for (int b = 0; b < 2; ++b) { const u32 a = r.a[wslot(P, c) >> 1][b] + (wslot(P, c) & 1) * 32768 + ks * 2048; f[b] = trd2(a, a + 1024); }
}
template <int P, int C, int F> DI void w_round(const WSrc& s, const WA& a0, u32x4 (&wr)[16][2]) {
#pragma unroll
  for (int k = 0; k < 4; ++k) { w_frag<P>(a0, wr[4 * C + k], C, k); SB(); if constexpr (F < 8) w_one<P>(s, F, k); SB(); }
}
DI void w_stage(const WA& a0, u32x4 (&wr)[16][2], int k0, int k1) {
#pragma unroll
  for (int k = k0; k < k1; ++k) w_frag<1>(a0, wr[k], k >> 2, k & 3);
}
template <int C0> DI void w_flag(const WSrc& s, int w, u32 tag, u32 fv, u32x4 rws, u32 flag) {
  if constexpr (C0 < 6) w_issue<1>(s, C0);
  vmbar<(C0 < 6 ? 4 : 0)>();
  if (w == 0) st32c(tag, fv, rws, flag);
#pragma unroll
  for (int c = C0 + 1; c < 6; ++c) w_issue<1>(s, c);
}
template <int N> DI void vmbar0(int w) { if (w == 0) vmwait<N + 1>(); else vmwait<N>(); bar(); }
struct DSrc { u32x4 rs; u32 voff, sb, rot; };
template <int I> DI void d_issue(u32x4 (&d)[32], const DSrc& s) { ld128c<(I & 3) * 1024>(d[I], s.voff, s.rs, s.sb + (((I >> 2) + s.rot) & 7) * 4096); }
constexpr int KB = 32 - WIN;
template <int P, int K> DI void wr_steps(u32x4 (&d)[32], u32x4 (&wr)[16][2], const WA& a0, const DSrc& s, const WSrc& nx, u32 sig, f32x16& c0, f32x16& c1) {
  if constexpr (K < 32) {
    if constexpr (K == KB) {
      if constexpr (P == 0) bar();
      else { asm volatile("s_waitcnt lgkmcnt(0)" ::: "memory"); *(volatile LAS u32*)sig = 1; }
    }
    if constexpr (K % 8 == 0) __builtin_amdgcn_s_setprio(3 - K / 8);
    vmwait<(P == 0 || K < KB ? WIN - 1 : 31 - K)>(d[K]);
    c0 = mfma(wr[K & 15][0], d[K], c0); c1 = mfma(wr[K & 15][1], d[K], c1);
    asm volatile("" : "+v"(c0), "+v"(c1));
    if constexpr (K + WIN < 32) d_issue<K + WIN>(d, s);
    if constexpr (P == 0 && K == 1) bar();
    if constexpr (P == 0 && K >= KB) w_one<1>(nx, (K - KB) >> 2, (K - KB) & 3);
    if constexpr (K >= 1 && K <= 16) w_frag<P>(a0, wr[K - 1], 4 + ((K - 1) >> 2), (K - 1) & 3);
    wr_steps<P, K + 1>(d, wr, a0, s, nx, sig, c0, c1);
  }
}
template <int I> DI void d_first(u32x4 (&d)[32], const DSrc& s) { if constexpr (I < WIN) { d_issue<I>(d, s); d_first<I + 1>(d, s); } }
template <int P> DI void wr_gemm(u32x4 (&wr)[16][2], const WA& a0, const DSrc& s, const WSrc& nx, u32 sig, f32x16& c0, f32x16& c1) {
  u32x4 d[32];
  d_first<0>(d, s);
  c0 = splat(0.f); c1 = splat(0.f);
  wr_steps<P, 0>(d, wr, a0, s, nx, sig, c0, c1);
  if constexpr (P == 0) {
#pragma unroll
    for (int i = WIN; i < 12; ++i) w_one<1>(nx, i >> 2, i & 3);
  }
}
constexpr u32 RSL = 1088;
template <u32 RS> DI void red_write(u32 a, u32 bs, const f32x16& c0, const f32x16& c1) {
#pragma unroll
  for (int q = 0; q < 4; ++q) {
    *(LAS f32x4*)(a + q * RS) = f32x4{c0[4 * q], c0[4 * q + 1], c0[4 * q + 2], c0[4 * q + 3]};
    *(LAS f32x4*)(a + bs + q * RS) = f32x4{c1[4 * q], c1[4 * q + 1], c1[4 * q + 2], c1[4 * q + 3]};
  }
}

template <int O, int I> DI void put16(const f32x16& c) {
  if constexpr (I < 16) { asm volatile("ds_write_addtid_b32 %0 offset:%1" ::"v"(c[I]), "i"(O + (I >> 1) * 768 + (I & 1) * 272) : "memory"); put16<O, I + 1>(c); }
}

constexpr u32 AST = 49152;
struct ASrc { u32x4 rx, rw; u32 vx0, vx1, vw, vq, sx, s0, s1, sq, rot, L; int w; };
DI void a_one(const ASrc& s, int c, int j) {
  const u32 st = s.L + (c % 3) * AST, cc = (c + s.rot) & 7;
  if (j < 4) glds(st + s.w * 4096 + j * 1024, (j & 1) ? s.vx1 : s.vx0, s.rx, s.sx + j * 8192 + cc * 128);
  else glds(st + 32768 + (j - 4) * 8192 + s.w * 1024, s.vw, s.rw, (j == 4 ? s.s0 : s.s1) + cc * (64 * 3072));
}
template <bool KV, int C, int J> DI void a_pref(const ASrc& s, const WSrc& nx) {
  if constexpr (J < 0) {}
  else if constexpr (C < 8) a_one(s, C, J);
  else if constexpr (C == 8) { if constexpr (KV && J < 4) glds(s.L + 98304 + s.w * 4096 + J * 1024, s.vq, s.rw, s.sq + J * (16 * 3072)); }
  else w_one<1>(nx, (6 * (C - 9) + J) >> 2, (6 * (C - 9) + J) & 3);
}
constexpr int a_gap(int C, int g) {
  const int t0[6] = {5, 6, 9, 10, 13, 14}, t1[6] = {1, 3, 6, 9, 11, 14}, t2[6] = {1, 2, -1, -1, -1, -1};
  for (int j = 0; j < 6; ++j) if ((C == 2 ? t0[j] : C == 10 ? t2[j] : t1[j]) == g) return j;
  return -1;
}
struct AMisc { u32x4 rws; u32 cfoff, tag, rdy, clr; int lane; };
struct AAd { u32 x0[4], x1[4], wa; };
template <bool KV, int T> DI void a_steps(const ASrc& s, const WSrc& nx, AAd& ad, u32x4 (&x0)[33], u32x4 (&f)[2][3], f32x16 (&c)[4], u32& cf, const AMisc& ms) {
  if constexpr (T < 32) {
    constexpr int C = T >> 2, K = T & 3, P = T & 1, N = (T + 1) & 3;
    if constexpr (K == 3) {
      if constexpr (C < 6) vmbar<6 + ((KV && C == 1) || C == 3)>(); else if constexpr (C == 6) vmbar<KV ? 5 : 0>(); else vmbar<6>();
      if constexpr (C < 7) {
        constexpr u32 dl = (C + 1) % 3 ? AST : 0u - 2 * AST;
#pragma unroll
        for (int k = 0; k < 4; ++k) { ad.x0[k] += dl; ad.x1[k] += dl; }
        ad.wa += dl;
        asm volatile("" : "+v"(ad.x0[0]), "+v"(ad.x0[1]), "+v"(ad.x0[2]), "+v"(ad.x0[3]), "+v"(ad.x1[0]), "+v"(ad.x1[1]), "+v"(ad.x1[2]), "+v"(ad.x1[3]), "+v"(ad.wa));
      }
      if constexpr (C == 0 && KV) { if (s.w < 4) st128c(u32x4{~0u, ~0u, ~0u, ~0u}, ms.lane * 16, ms.rws, ms.clr); else st32c(~0u, (ms.lane & 31) * 4, ms.rws, ms.clr); }
      if constexpr (C == 2) { st32c(ms.tag, ms.rdy, ms.rws, 0); }
      if constexpr (C == 5 && KV) ld32c(cf, ms.cfoff, ms.rws, 0);
    }
    constexpr int PC = K == 3 ? C + 3 : C + 2, G0 = 4 * N;
    __builtin_amdgcn_s_setprio(3 - N);
    if constexpr (T < 31) { x0[T + 1] = lds128(ad.x0[N]); f[P ^ 1][1] = trd2(ad.wa + N * 2048, ad.wa + N * 2048 + 512); }
    SB();
    if constexpr (KV) c[0] = mfma(x0[T], f[P][1], c[0]); else c[0] = mfma(f[P][1], x0[T], c[0]);
    SB();
    if constexpr (T < 31) { f[P ^ 1][0] = lds128(ad.x1[N]); f[P ^ 1][2] = trd2(ad.wa + 8192 + N * 2048, ad.wa + 8192 + N * 2048 + 512); }
    a_pref<KV, PC, a_gap(PC, G0)>(s, nx);
    SB();
    if constexpr (KV) c[1] = mfma(f[P][0], f[P][1], c[1]); else c[1] = mfma(f[P][1], f[P][0], c[1]);
    SB();
    a_pref<KV, PC, a_gap(PC, G0 + 1)>(s, nx);
    SB();
    if constexpr (KV) c[2] = mfma(x0[T], f[P][2], c[2]); else c[2] = mfma(f[P][2], x0[T], c[2]);
    SB();
    a_pref<KV, PC, a_gap(PC, G0 + 2)>(s, nx);
    SB();
    if constexpr (KV) c[3] = mfma(f[P][0], f[P][2], c[3]); else c[3] = mfma(f[P][2], f[P][0], c[3]);
    SB();
    a_pref<KV, PC, a_gap(PC, G0 + 3)>(s, nx);
    SB();
    a_steps<KV, T + 1>(s, nx, ad, x0, f, c, cf, ms);
  }
}

DI ASrc a_src(const char* x, const char* wqkv, u32 L, int lane, int w, int xcd, int slot) {
  const bool kv = slot < 30;
  const int ai = slot - 22, rc = (xcd & 6) + (kv ? ai >> 2 : slot - 30), hg = (xcd & 1) * 4 + (ai & 3), qj = xcd & 1;
  ASrc sa;
  sa.rx = srd(x, 2048 * 1024); sa.rw = srd(wqkv, 512 * 3072); sa.L = L; sa.w = w;
  sa.rot = xcd & 6;
  sa.vx0 = (lane >> 3) * 1024 + ((lane & 7) ^ (lane >> 4)) * 16; sa.vx1 = sa.vx0 ^ 64;
  sa.vw = (lane >> 3) * 3072 + ((lane & 7) ^ ((lane >> 2) & 4)) * 16;
  sa.vq = (lane >> 2) * 3072 + (lane & 3) * 16;
  sa.sx = (rc * 256 + w * 32) * 1024;
  sa.s0 = w * 8 * 3072 + (kv ? 1024 + hg * 128 : 512 + qj * 256);
  sa.s1 = sa.s0 + (kv ? 1024 : 128);
  sa.sq = ((w + sa.rot) & 7) * (64 * 3072) + hg * 64;
  return sa;
}
DI WSrc w1_src(const char* w1, u32 lds, int lane, int w, int xcd, int slot, int& ft, int& dt1) {
  const int gj = slot < 16 ? (xcd >> 1) * 16 + slot : 48 + (xcd >> 1) * 6 + slot;
  const u32 wch = (lane & 7) ^ ((lane >> 2) & 4);
  ft = gj >> 2; dt1 = (xcd & 1) * 4 + (gj & 3);
  WSrc s1;
  s1.rs = srd(w1, 2048 * 2752); s1.stride = 2752; s1.lds = lds;
  s1.voff = (lane >> 3) * 2752 + (ft == 21 ? (wch & 3) : wch) * 16;
  s1.sbase = ((w >> 1) * 512 + (w & 1) * 32) * 2752 + ft * 128;
  s1.rot = (2 * (dt1 + ft)) & 7;
  return s1;
}
template <bool KV> DI u32 attn(u32 ev, const char* x, const char* wqkv, u32x4 rws, const WSrc& s2, u32 L, int lane, int w, int xcd, int slot, u32 wa0, u32x4 (&wr)[16][2]) {
  const int hh = lane >> 5, col = lane & 31, q4 = (lane & 15) >> 2, mi = w >> 1, ni = w & 1;
  const int ai = slot - 22, rc = (xcd & 6) + (KV ? ai >> 2 : slot - 30), hg = (xcd & 1) * 4 + (ai & 3), qj = xcd & 1;
  const ASrc sa = a_src(x, wqkv, L, lane, w, xcd, slot);
  AAd ad;
#pragma unroll
  for (int k = 0; k < 4; ++k) { ad.x0[k] = L + (mi * 64 + ni * 32 + col) * 128 + ((2 * k + hh) ^ ((col >> 1) & 7)) * 16; ad.x1[k] = ad.x0[k] ^ 4096; }
  ad.wa = L + 32768 + (8 * hh + q4) * 128 + 64 * ((q4 >> 1) ^ ni) + 32 * ((lane >> 4) & 1) + 8 * (lane & 3);
  const int pr = hg + (mi & 1) * 8;
  const bool ga = w < 4;
  const u32 cfoff = ga ? WS_F + xcd * FRS + (128 + (pr >> 1) * 8 + (lane & 7)) * 128 : OOB;
  asm volatile("" : "+v"(ad.x0[0]), "+v"(ad.x0[1]), "+v"(ad.x0[2]), "+v"(ad.x0[3]), "+v"(ad.x1[0]), "+v"(ad.x1[1]), "+v"(ad.x1[2]), "+v"(ad.x1[3]), "+v"(ad.wa)::"memory");
  const u32 tag = tag_wait<6>(ev);
  bar();
  const AMisc ms = {rws, cfoff, tag, lane == 0 ? WS_F + w * FRS + (KV ? 128 + hg * 8 + rc : 192 + qj * 8 + rc) * 128 : OOB, w < 4 ? WS_KV + ((hg * 2 + (w >> 1)) * 8 + rc) * 2048 + (w & 1) * 1024 : WS_Z + ((hg * 2 + (w & 1)) * 8 + rc) * 128, lane};
  u32x4 x0[33], f[2][3]; f32x16 c[4]; u32 cf;
  c[0] = c[1] = splat(KV ? 1.f : 1.000001f); c[2] = c[3] = splat(KV ? 0.f : 1.000001f);
  x0[0] = lds128(ad.x0[0]); f[0][0] = lds128(ad.x1[0]); f[0][1] = trd2(ad.wa, ad.wa + 512); f[0][2] = trd2(ad.wa + 8192, ad.wa + 8192 + 512);
  a_steps<KV, 0>(sa, s2, ad, x0, f, c, cf, ms);
  __builtin_amdgcn_s_setprio(0);
  asm volatile("" : "+v"(c[0]), "+v"(c[1]), "+v"(c[2]), "+v"(c[3]));
  const u32 pfl = WS_F + xcd * FRS + (512 + rc * 16 + 8 + qj) * 128;
  if constexpr (!KV) {
#pragma unroll
    for (int j = 0; j < 4; ++j)
#pragma unroll
      for (int h = 0; h < 2; ++h) { st128c(phi8<true>(c[j], 8 * h), lane * 16 + qj * 8192 + ni * 2048, rws, WS_P + (rc * 8 + (w ^ (j & 1))) * 16384 + ((j & 2) * 2 + h) * 1024); SB(); }
    asm volatile("s_barrier" ::: "memory");
    w_issue<1>(s2, 2);
    vmbar<4>();
    if (w == 0) st32c(tag, lane == 0 ? 0 : OOB, rws, pfl);
    if (blockIdx.x == 255 && w == 0) {
#pragma unroll 1
      for (int i = 0; i < 4; ++i) poll(WS_F + 7 * FRS + (i * 64 + lane) * 128, rws, tag);
      st32c(tag, lane == 0 ? 0 : OOB, rws, WS_E);
    }
    vmbar<0>();
    w_stage(w_addr(wa0), wr, 0, 12);
    bar();
    w_flag<3>(s2, w, tag, OOB, rws, 0);
    return tag;
  }
  const u32 S = L + 65536, KT = S + 14336, ZT = KT + 2048;
  if (w >= 4) w_issue<1>(s2, 2);
  const u32 on = ((lane >> 4) ^ (lane >> 2)) & 1 ? 0 : 0x3f803f80u;
  const u32x4 cs = {on, on, on, on};
  f32x16 d1 = splat(0.f); f32x4 d2 = {64e-6f, 64e-6f, 64e-6f, 64e-6f}, d3 = {0.f, 0.f, 0.f, 0.f};
  u32x4 p[4];
#pragma unroll
  for (int i = 0; i < 4; ++i) {
    const f32x16& k = c[i >> 1]; const int o = 8 * (i & 1);
    p[i][0] = phi2<false>(k[o], k[o + 1]); SB();
    if (i) d2 = mfma16(cs, p[i - 1], d2);
    const u32x4 v = pack8(c[2 + (i >> 1)], o);
    p[i][1] = phi2<false>(k[o + 2], k[o + 3]); SB();
    d3 = mfma16(cs, v, d3);
    p[i][2] = phi2<false>(k[o + 4], k[o + 5]); p[i][3] = phi2<false>(k[o + 6], k[o + 7]); SB();
    d1 = mfma(p[i], v, d1);
    SB(); if (w < 4) w_one<1>(s2, 2, i); SB();
  }
  d2 = mfma16(cs, p[3], d2);
  const bool lo = col < 16;
  f32x4 k0 = {lo ? d1[0] : d1[8], lo ? d1[1] : d1[9], lo ? d1[2] : d1[10], lo ? d1[3] : d1[11]};
  f32x4 k1 = {lo ? d1[4] : d1[12], lo ? d1[5] : d1[13], lo ? d1[6] : d1[14], lo ? d1[7] : d1[15]};
  const float ev6 = d3[0] * 1e-6f;
  k0 = add4(k0, f32x4{ev6, ev6, ev6, ev6}); k1 = add4(k1, f32x4{ev6, ev6, ev6, ev6});
  const float zp = d2[0];
  if (w >= 2) { *(LAS f32x4*)(S + (w - 2) * 2048 + lane * 16) = k0; *(LAS f32x4*)(S + (w - 2) * 2048 + 1024 + lane * 16) = k1; *(LAS float*)(S + 12288 + (w - 2) * 256 + lane * 4) = zp; }
  vmbar<0>();
  if (w < 2) {
    const u32 a = S + w * 2048 + lane * 16, z = S + 12288 + w * 256 + lane * 4;
    k0 = add4(add4(k0, ldsf4(a)), add4(ldsf4(a + 4096), ldsf4(a + 8192))); k1 = add4(add4(k1, ldsf4(a + 1024)), add4(ldsf4(a + 5120), ldsf4(a + 9216)));
    const float t = add1(add1(zp, ldsf(z)), add1(ldsf(z + 512), ldsf(z + 1024)));
    u32x4 p0 = __builtin_bit_cast(u32x4, k0), p1 = __builtin_bit_cast(u32x4, k1);
#pragma unroll
    for (int i = 0; i < 4; ++i) { p0[i] = umin(p0[i], ~1u); p1[i] = umin(p1[i], ~1u); }
    const u32 o = (hg * 2 + w) * 8 + rc;
    st128c(p0, lane * 16, rws, WS_KV + o * 2048); st128c(p1, lane * 16, rws, WS_KV + o * 2048 + 1024); st32c(umin(__float_as_uint(t), ~1u), col * 4, rws, WS_Z + o * 128);
  }
  u32x4 pq[2][2], t[8]; u32x2 z; u32 pf;
  u32 qa = L + 98304 + (8 * hh + q4) * 64 + 32 * ((lane >> 4) & 1) + 8 * (lane & 3);
  asm volatile("" : "+v"(qa), "+v"(cf));
  u32x4 qf[4];
#pragma unroll
  for (int i = 0; i < 3; ++i) qf[i] = trd2(qa + i * 1024, qa + i * 1024 + 256);
  f32x16 cq = splat(1.000001f);
#pragma unroll
  for (int i = 0; i < 32; ++i) {
    SB();
    if (i < 29) qf[(i + 3) & 3] = trd2(qa + (i + 3) * 1024, qa + (i + 3) * 1024 + 256);
    SB();
    cq = mfma(qf[i & 3], x0[i], cq);
  }
  SB();
  auto sweep = [&]() {
#pragma unroll
    for (int r = 0; r < 8; ++r) ld128c<0>(t[r], lane * 16, rws, WS_KV + (pr * 8 + r) * 2048 + ni * 1024);
    asm volatile("buffer_load_dwordx2 %0, %1, %2, %3 offen sc1" : "=v"(z) : "v"((lane >> 3) * 128 + (lane & 7) * 8), "s"(rws), "s"(WS_Z + pr * 1024 + ni * 64) : "memory");
  };
  asm volatile("" : "=v"(t[0]), "=v"(t[1]), "=v"(t[2]), "=v"(t[3]), "=v"(t[4]), "=v"(t[5]), "=v"(t[6]), "=v"(t[7]), "=v"(z));
  if (ga && __builtin_amdgcn_ballot_w64(cf != tag) != 0) poll(cfoff, rws, tag);
  ld32c(pf, pfl, rws, 0);
  if (mi == 0) sweep();
  pq[0][0] = phi8<true>(cq, 0); pq[0][1] = phi8<true>(cq, 8);
  asm volatile("" : "+v"(pq[0][0]), "+v"(pq[0][1]));
  w_stage(w_addr(wa0), wr, 0, 12);
  SB();
  if (mi == 1) sweep();
  if (!ga) vmwait<0>();
  asm volatile("s_waitcnt vmcnt(9)" : "+v"(pf)::"memory");
  if (__builtin_amdgcn_ballot_w64(pf != tag) != 0) poll(pfl, rws, tag);
  ld128c<0>(pq[1][0], lane * 16, rws, WS_P + (rc * 8 + w) * 16384 + hg * 2048); ld128c<1024>(pq[1][1], lane * 16, rws, WS_P + (rc * 8 + w) * 16384 + hg * 2048);
#define GW(N) asm volatile("s_waitcnt vmcnt(%9)" : "+v"(t[0]), "+v"(t[1]), "+v"(t[2]), "+v"(t[3]), "+v"(t[4]), "+v"(t[5]), "+v"(t[6]), "+v"(t[7]), "+v"(z) : "i"(N) : "memory")
  GW(2);
  if (ga) {
    for (int it = 0; it < SPIN_MAX; ++it) {
      u32 m = umax(z[0], z[1]);
#pragma unroll
      for (int r = 0; r < 8; ++r) m = umax(umax(umax(m, t[r][0]), t[r][1]), umax(t[r][2], t[r][3]));
      if (__builtin_amdgcn_ballot_w64(m == ~0u) == 0) break;
      sweep();
      GW(0);
    }
    f32x4 a = __builtin_bit_cast(f32x4, t[0]);
#pragma unroll
    for (int r = 1; r < 8; ++r) a += __builtin_bit_cast(f32x4, t[r]);
    float zs = fold32(__uint_as_float(z[0]), __uint_as_float(z[1]));
    zs = dppadd<0x128>(fold16(zs, zs));
    const int pl = mi, zf = 2 * (lane & 7) + hh;
    *(LAS u32x2*)(KT + pl * 1024 + lane * 16 + ni * 8) = u32x2{pk(a[0], a[1]), pk(a[2], a[3])};
    *(LAS unsigned short*)(ZT + pl * 64 + ni * 32 + ((zf >> 2) & 1) * 16 + ((zf & 3) + 4 * (zf >> 3)) * 2) = (unsigned short)pk(zs, zs);
  }
  bar();
  f32x16 dd[2][2];
#pragma unroll
  for (int j = 0; j < 2; ++j) {
    if (j) asm volatile("s_waitcnt vmcnt(0)" : "+v"(pq[1][0]), "+v"(pq[1][1])::"memory");
#pragma unroll
    for (int h = 0; h < 2; ++h) dd[j][h] = mfma(lds128(((col < 16) == (h == 0)) ? KT + j * 1024 + lane * 16 : ZT + j * 64 + h * 32 + hh * 16), pq[j][h], splat(0.f));
  }
  SB();
  w_issue<1>(s2, 3);
  SB();
#pragma unroll
  for (int j = 0; j < 2; ++j)
#pragma unroll
    for (int h = 0; h < 2; ++h) {
      const int o = 8 * h;
      const f32x16& e = dd[j][h];
      const float rd = __builtin_amdgcn_rcpf(e[8 - o] + 1e-6f);
      const u32x4 ov = {pk(e[o] * rd, e[o + 1] * rd), pk(e[o + 2] * rd, e[o + 3] * rd), pk(e[o + 4] * rd, e[o + 5] * rd), pk(e[o + 6] * rd, e[o + 7] * rd)};
      st128c(ov, lane * 16, rws, WS_O + (rc * 8 + w) * 32768 + ((hg + 8 * j) * 2 + h) * 1024);
    }
  bar();
  w_flag<4>(s2, w, tag, lane < 2 ? ((rc & 6) + lane) * FRS : OOB, rws, WS_F + (512 + rc * 16 + hg) * 128);
  return tag;
}

__global__ __launch_bounds__(512, 2) void fused_kernel(const char* __restrict__ x, const char* __restrict__ wqkv, const char* __restrict__ wo,
                                                        const char* __restrict__ w1, const char* __restrict__ b1, const char* __restrict__ w2,
                                                        const char* __restrict__ b2, const char* __restrict__ gw, char* __restrict__ out, char* ws) {
  __shared__ __attribute__((aligned(1024))) char sm[163840];
  u32 ev;
  {
    unsigned long long pc; u32 cn;
    asm volatile("s_getpc_b64 %0\n.Lfk_pc:\n\ts_mov_b32 %1, .Lfk_end-.Lfk_pc" : "=s"(pc), "=s"(cn));
    const u32 vo = (blockIdx.x >> 3 & 15) == 0 ? threadIdx.x * 128 : OOB;
    asm volatile("buffer_load_dword %0, %1, %2, 0 offen" : "=v"(ev) : "v"(vo), "s"(srd((const void*)pc, cn)) : "memory");
    SB();
  }
  asm volatile("" : "+s"(x), "+s"(wqkv), "+s"(wo), "+s"(w1), "+s"(b1), "+s"(w2), "+s"(b2), "+s"(gw), "+s"(out), "+s"(ws));
  const u32 L = (u32)(size_t)(LAS char*)sm;
  const int lane = threadIdx.x & 63, w = __builtin_amdgcn_readfirstlane(threadIdx.x >> 6);
  int xcd = blockIdx.x & 7, slot = blockIdx.x >> 3;
  const int hh = lane >> 5;
  const u32x4 rws = srd(ws, WS_END);
  u32x4 xr[2], gr;
  asm volatile("s_nop 4" : "=v"(xr[0]), "=v"(xr[1]), "=v"(gr)::"memory");
  ev_load(ev, rws);
  if (slot < 22) {
    int ft, dt1;
    const WSrc s1 = w1_src(w1, L + w * 4096, lane, w, xcd, slot, ft, dt1);
    if (slot < 16) {
      const int r = (xcd * 16 + slot) * 16 + w;
      const u32x4 rx = srd(x, 2048 * 1024);
      ld128(xr[0], lane * 16, rx, r * 1024); ld128(xr[1], lane * 16, rx, (r + 8) * 1024);
      ld128(gr, lane * 16, srd(gw, 1024), 0);
      asm volatile("s_barrier" ::: "memory");
    }
    w_issue<0>(s1, 0);
  } else {
    const ASrc sa = a_src(x, wqkv, L, lane, w, xcd, slot);
#pragma unroll
    for (int j = 0; j < 6; ++j) a_one(sa, 0, j);
    asm volatile("s_barrier" ::: "memory");
#pragma unroll
    for (int j = 0; j < 6; ++j) a_one(sa, 1, j);
  }
  asm volatile("" : "+s"(slot), "+s"(xcd));
  const int g = w >> 1, hf = w & 1, q4 = (lane & 15) >> 2;
  const u32 wch = (lane & 7) ^ ((lane >> 2) & 4);
  const u32 wa0 = L + g * 8192 + (4 * hh + q4) * 128 + 64 * (q4 >> 1) + 32 * ((lane >> 4) & 1) + 8 * (lane & 3);

  const int dt = (xcd & 1) * 4 + (slot & 3), stl = (xcd >> 1) * 8 + (slot >> 2);
  WSrc s2;
  s2.rs = g < 3 ? srd(w2, 1376 * 4096) : srd(wo, 512 * 1024); s2.stride = g < 3 ? 4096 : 1024; s2.lds = L + g * 8192 + hf * 4096;
  s2.voff = (lane >> 3) * s2.stride + wch * 16;
  s2.sbase = g < 3 ? (g * 512 + hf * 32) * 4096 + stl * 128 : hf * 32 * 1024 + dt * 128;
  s2.rot = 2 * ((slot + (slot >> 2)) & 3);
  u32 tag;
  u32x4 wr[16][2];

  if (slot >= 30) tag = attn<false>(ev, x, wqkv, rws, s2, L, lane, w, xcd, slot, wa0, wr);
  else if (slot >= 22) tag = attn<true>(ev, x, wqkv, rws, s2, L, lane, w, xcd, slot, wa0, wr);
  else {
    const int gi = slot < 16 ? xcd * 16 + slot : 112 + xcd * 6 + slot;
    int ft, dt1;
    const WSrc s1 = w1_src(w1, s2.lds, lane, w, xcd, slot, ft, dt1);
    if (slot < 16) {
      const int sb = xcd * 16 + slot;
      tag = tag_wait<4>(ev);
      asm volatile("" : "+v"(xr[0]), "+v"(xr[1]), "+v"(gr)::"memory");
      f32x2 v[2][4], t; float s[2], gd[2], q[2];
#pragma unroll
      for (int j = 0; j < 2; ++j) {
#pragma unroll
        for (int i = 0; i < 4; ++i) v[j][i] = f32x2{blo(xr[j][i]), bhi(xr[j][i])};
        t = (v[j][0] + v[j][1]) + (v[j][2] + v[j][3]); s[j] = t[0] + t[1];
      }
      const float t0 = fold32(s[0], s[1]), t1 = rowsum(fold16(t0, t0)) * (1.f / 512.f);
#pragma unroll
      for (int j = 0; j < 2; ++j) {
        t = f32x2{0.f, 0.f};
#pragma unroll
        for (int i = 0; i < 4; ++i) t = __builtin_elementwise_fma(v[j][i], f32x2{blo(gr[i]), bhi(gr[i])}, t);
        gd[j] = t[0] + t[1];
        const float m = rdl(t1, 32 * j);
        t = f32x2{0.f, 0.f};
#pragma unroll
        for (int i = 0; i < 4; ++i) { v[j][i] -= m; t = __builtin_elementwise_fma(v[j][i], v[j][i], t); }
        q[j] = t[0] + t[1];
      }
      const float t2 = rowsum(fold16(fold32(q[0], q[1]), fold32(gd[0], gd[1])));
      const float rs = __builtin_amdgcn_rsqf(t2 * (1.f / 512.f) + 1e-5f);
#pragma unroll
      for (int j = 0; j < 2; ++j) {
        const float r = rdl(rs, 32 * j);
#pragma unroll
        for (int i = 0; i < 4; ++i) v[j][i] *= r;
        *(LAS u32x4*)(L + (w + 8 * j) * 1088 + lane * 16) = u32x4{pk(v[j][0][0], v[j][0][1]), pk(v[j][1][0], v[j][1][1]), pk(v[j][2][0], v[j][2][1]), pk(v[j][3][0], v[j][3][1])};
      }
      const float gt = __builtin_amdgcn_rcpf(1.f + __builtin_amdgcn_exp2f(t2 * -1.4426950408889634f));
      bar();
      const u32 ta = L + (4 * hh + q4) * 1088 + w * 128 + 32 * ((lane >> 4) & 1) + 8 * (lane & 3);
      const u32x4 h0 = trd2(ta, ta + 8 * 1088), h1 = trd2(ta + 64, ta + 64 + 8 * 1088);
      st32c(__float_as_uint(gt), (lane & 31) == 16 ? lane & 32 : OOB, rws, WS_G + (sb * 16 + w) * 4);
      st128c(h0, lane * 16, rws, WS_H + w * 262144 + sb * 1024);
      st128c(h1, lane * 16, rws, WS_H + w * 262144 + 131072 + sb * 1024);
      asm volatile("s_barrier" ::: "memory");
      w_issue<0>(s1, 1);
      vmbar<4>();
    } else {
      u32 tl;
      asm volatile("buffer_load_dword %0, %1, %2, 0 offen" : "=v"(tl) : "v"((lane << 18) + 2048),
                   "s"(w == 0 ? srd(w2, 1376 * 4096) : w == 1 ? srd(wo, 512 * 1024) : w == 2 ? srd(out, 2048 * 1024) : w == 3 ? srd(b1, 2752) : w == 4 ? srd(b2, 4096) : srd(ws, w == 5 ? WS_END : 0)) : "memory");
      w_issue<0>(s1, 1);
      tag = tag_wait<4>(ev);
      asm volatile("" ::"v"(tl));
      bar();
    }
    const WA a0 = w_addr(wa0);
    if (w == 0) st32c(tag, lane < 8 ? lane * FRS : OOB, rws, WS_F + (slot < 16 ? gi : gi + 80) * 128);
    w_round<0, 0, 2>(s1, a0, wr); w_issue<0>(s1, 3);
    vmbar0<8>(w);
    w_round<0, 1, 4>(s1, a0, wr);
    vmbar<8>();
    w_round<0, 2, 5>(s1, a0, wr);
    vmbar<8>();
    const u32 hfl = hf ? WS_F + xcd * FRS + (g * 32 + (lane & 31)) * 128 : OOB;
    u32 pf; u32x2 bb0, bb1;
    ld32c(pf, hfl, rws, 0);
    asm volatile("buffer_load_dwordx2 %0, %2, %3, %4 offen\n\tbuffer_load_dwordx2 %1, %2, %3, %4 offen offset:16" : "=&v"(bb0), "=&v"(bb1) : "v"(hh * 8), "s"(srd(b1, 2752)), "s"(ft * 128 + g * 32) : "memory");
    w_round<0, 3, 6>(s1, a0, wr); w_issue<0>(s1, 7);
    asm volatile("s_waitcnt vmcnt(8)" : "+v"(pf), "+v"(bb0), "+v"(bb1)::"memory");
    if (hf && __builtin_amdgcn_ballot_w64(pf != tag) != 0) poll(hfl, rws, tag);
    bar();
    f32x16 c0, c1;
    wr_gemm<0>(wr, a0, DSrc{rws, (u32)lane * 16, WS_H + (dt1 * 2 + hf) * 131072 + g * 32768, s1.rot}, s2, 0, c0, c1);
    const u32 R = L + 65536 + hf * 32768 + lane * 16;
    f32x4 lo = {blo(bb0[0]), bhi(bb0[0]), blo(bb0[1]), bhi(bb0[1])}, hi = {blo(bb1[0]), bhi(bb1[0]), blo(bb1[1]), bhi(bb1[1])};
#pragma unroll
    for (int q = 0; q < 4; ++q) {
      const f32x16& c = q < 2 ? c0 : c1; const int o = (q & 1) * 8;
      const f32x4 a = {c[o], c[o + 1], c[o + 2], c[o + 3]}, b = {c[o + 4], c[o + 5], c[o + 6], c[o + 7]};
      if (q == g) { lo += a; hi += b; }
      else { const u32 t = R + q * 8192 + ((g - q) & 3) * 2048; *(LAS f32x4*)t = a; *(LAS f32x4*)(t + 1024) = b; }
    }
    vmbar<0>();
#pragma unroll
    for (int k = 1; k < 4; ++k) { lo += ldsf4(R + g * 8192 + k * 2048); hi += ldsf4(R + g * 8192 + k * 2048 + 1024); }
    const u32x4 uv = {pk(gelu(lo[0]), gelu(lo[1])), pk(gelu(lo[2]), gelu(lo[3])), pk(gelu(hi[0]), gelu(hi[1])), pk(gelu(hi[2]), gelu(hi[3]))};
    st128c(uv, lane * 16, rws, WS_U + (dt1 * 2 + hf) * UDB + (ft * 4 + g) * 1024);
    w_stage(a0, wr, 0, 12);
    bar();
    w_flag<3>(s2, w, tag, lane < 4 ? (lane * 2 + (dt1 >> 2)) * FRS : OOB, rws, WS_F + (256 + dt1 * 32 + ft) * 128);
  }

  {
    asm volatile("" : "+s"(s2.lds), "+s"(s2.rot), "+s"(s2.sbase));
    const int sr = w * 8 + (lane >> 3), cq = lane & 7;
    u32 b2v;
    asm volatile("buffer_load_ushort %0, %1, %2, %3 offen" : "=v"(b2v) : "v"(sr * 2), "s"(srd(b2, 4096)), "s"(stl * 128) : "memory");
    const u32 GT = L + 131072, SG = GT + 256;
    const WA a0 = w_addr(wa0);
    f32x16 c0, c1;
    const int nf = g == 2 ? 6 : 8;
    const u32 fo = WS_F + xcd * FRS + ((g < 3 ? 256 + dt * 32 + g * 8 : 512 + (stl >> 2) * 16) + (lane < nf ? lane : 0)) * 128;
    u32 tl;
    asm volatile("buffer_load_dword %0, off, %1, %2" : "=v"(tl) : "s"(srd(out, w == 0 ? 2048 * 1024 : 0)), "s"((stl >> 2 << 18) + 2048) : "memory");
    w_issue<1>(s2, 6);
    w_issue<1>(s2, 7);
    vmbar0<18>(w);
    w_round<1, 3, 8>(s2, a0, wr);
    bar();
    *(volatile LAS u32*)(SG + w * 4) = 0;
    poll_bar(fo, rws, tag);
    asm volatile("" : "+v"(b2v) : "v"(tl));
    u32 gv;
    ld32c(gv, w == 0 ? lane * 4 : OOB, rws, WS_G + stl * 256);
    const u32 ub = WS_U + (dt * 2 + hf) * UDB;
    wr_gemm<1>(wr, a0, DSrc{srd(ws, g < 3 ? ub + 86 * 1024 : WS_END), (u32)lane * 16, g < 3 ? ub + g * 32768 : WS_O + (stl * 2 + hf) * 32768, s2.rot}, s2, SG + w * 4, c0, c1);
    asm volatile("" : "+v"(gv));
    if (w == 0) *(LAS u32*)(GT + lane * 4) = gv;
    for (int it = 0; it < SPIN_MAX * 16 && *(volatile LAS u32*)(SG + (w ^ 1) * 4) == 0; ++it) {}
    const u32 pb = L + g * 8192 + hf * 32768;
    if (g < 3) {
      asm volatile("s_mov_b32 m0, %0\n\ts_nop 15\n\ts_nop 7" ::"s"(pb + hf * 128) : "memory");
      put16<0, 0>(c0);
      asm volatile("s_mov_b32 m0, %0\n\ts_nop 0" ::"s"(pb + hf * 128 + 6144) : "memory");
      put16<65536 - 6144, 0>(c1);
    } else red_write<RSL>(pb + lane * 16, 65536, c0, c1);
    bar();
    const int sl = lane >> 3, i = (sl & 3) + 4 * (w & 3);
    const u32 ya = L + (cq >> 2) * (32768 + 128) + (w >> 2) * 65536 + (i >> 1) * 768 + (i & 1) * 272 + (sl >> 2) * 128 + (cq & 3) * 32;
    const f32x4 y0 = (ldsf4(ya) + ldsf4(ya + 8192)) + ldsf4(ya + 16384), y1 = (ldsf4(ya + 16) + ldsf4(ya + 8208)) + ldsf4(ya + 16400);
    const u32 aa = L + 24576 + (w >> 2) * 32768 + (cq >> 2) * 65536 + (cq & 3) * RSL + (sr & 31) * 16;
    const f32x4 a0v = ldsf4(aa), a1v = ldsf4(aa + 512);
    const float gt = ldsf(GT + sr * 4), bb = blo(b2v), og = 1.f - gt;
    u32x4 ov;
    ov[0] = pk(gt * a0v[0] + og * (y0[0] + bb), gt * a0v[1] + og * (y0[1] + bb));
    ov[1] = pk(gt * a0v[2] + og * (y0[2] + bb), gt * a0v[3] + og * (y0[3] + bb));
    ov[2] = pk(gt * a1v[0] + og * (y1[0] + bb), gt * a1v[1] + og * (y1[1] + bb));
    ov[3] = pk(gt * a1v[2] + og * (y1[2] + bb), gt * a1v[3] + og * (y1[3] + bb));
    asm volatile("buffer_store_dwordx4 %0, %1, %2, %3 offen nt" ::"v"(ov), "v"(sr * 1024 + cq * 16), "s"(srd(out, 2048 * 1024)), "s"(stl * 65536 + dt * 128) : "memory");
    asm volatile(".Lfk_end:");
  }
}

extern "C" void kernel_launch(void* const* d_in, const int* in_sizes, int n_in, void* d_out, int out_size, void* d_ws, size_t ws_size, hipStream_t stream) {
  fused_kernel<<<256, 512, 0, stream>>>((const char*)d_in[0], (const char*)d_in[1], (const char*)d_in[2], (const char*)d_in[4], (const char*)d_in[5],
                                        (const char*)d_in[6], (const char*)d_in[7], (const char*)d_in[8], (char*)d_out, (char*)d_ws);
}
```

```cpp
#include <hip/hip_runtime.h>
#include <hip/hip_bf16.h>


#pragma clang diagnostic ignored "-Wint-to-pointer-cast"
#define DI __device__ __forceinline__
#define LAS __attribute__((address_space(3)))
#define SB() __builtin_amdgcn_sched_barrier(0)
typedef unsigned u32;
typedef u32 u32x4 __attribute__((ext_vector_type(4)));
typedef u32 u32x2 __attribute__((ext_vector_type(2)));
typedef float f32x2 __attribute__((ext_vector_type(2)));
typedef float f32x4 __attribute__((ext_vector_type(4)));
typedef float f32x16 __attribute__((ext_vector_type(16)));
typedef __bf16 bf16x8 __attribute__((ext_vector_type(8)));
typedef __bf16 bf16x2 __attribute__((ext_vector_type(2)));
typedef short s16x4 __attribute__((ext_vector_type(4)));

constexpr u32 WS_E = 0, WS_G = 32768;
constexpr u32 WS_F = 7u << 20, FRS = 641 * 128;
constexpr u32 WS_KV = 65536, WS_Z = WS_KV + 262144, WS_H = 1u << 20, WS_U = 3u << 20, WS_O = 5u << 20, WS_P = 8u << 20, WS_END = 9u << 20, OOB = 1u << 30;
constexpr u32 UDB = 88 * 1024;
constexpr int SPIN_MAX = 20000;
constexpr int WIN = 10;

DI u32x4 srd(const void* p, u32 bytes) { const unsigned long long a = (unsigned long long)p; return u32x4{(u32)a, (u32)(a >> 32), bytes, 0x00020000u}; }
DI f32x16 mfma(u32x4 a, u32x4 b, f32x16 c) { return __builtin_amdgcn_mfma_f32_32x32x16_bf16(__builtin_bit_cast(bf16x8, a), __builtin_bit_cast(bf16x8, b), c, 0, 0, 0); }
DI f32x4 mfma16(u32x4 a, u32x4 b, f32x4 c) { return __builtin_amdgcn_mfma_f32_16x16x32_bf16(__builtin_bit_cast(bf16x8, a), __builtin_bit_cast(bf16x8, b), c, 0, 0, 0); }
DI u32x2 trd(u32 a) { return __builtin_bit_cast(u32x2, __builtin_amdgcn_ds_read_tr16_b64_v4i16((LAS s16x4*)a)); }
DI u32x4 trd2(u32 a, u32 b) { const u32x2 x = trd(a), y = trd(b); return u32x4{x[0], x[1], y[0], y[1]}; }
DI u32x4 lds128(u32 a) { return *(LAS u32x4*)a; }
DI f32x4 ldsf4(u32 a) { return *(LAS f32x4*)a; }
DI float ldsf(u32 a) { return *(LAS float*)a; }
DI u32 pk(float a, float b) { return __builtin_bit_cast(u32, __builtin_convertvector(f32x2{a, b}, bf16x2)); }
DI float blo(u32 v) { return __uint_as_float(v << 16); }
DI float bhi(u32 v) { return __uint_as_float(v & 0xffff0000u); }
DI u32x4 pack8(const f32x16& x, int o) { return u32x4{pk(x[o], x[o + 1]), pk(x[o + 2], x[o + 3]), pk(x[o + 4], x[o + 5]), pk(x[o + 6], x[o + 7])}; }
DI f32x16 splat(float v) { return f32x16{v, v, v, v, v, v, v, v, v, v, v, v, v, v, v, v}; }
DI float add1(float a, float b) { float r; asm("v_add_f32 %0, %1, %2" : "=v"(r) : "v"(a), "v"(b)); return r; }
DI f32x4 add4(f32x4 a, f32x4 b) { return f32x4{add1(a[0], b[0]), add1(a[1], b[1]), add1(a[2], b[2]), add1(a[3], b[3])}; }
DI u32 umin(u32 a, u32 b) { return a < b ? a : b; }
DI u32 umax(u32 a, u32 b) { return a > b ? a : b; }

DI void glds(u32 lds, u32 voff, u32x4 rs, u32 soff) {
  asm volatile("s_mov_b32 m0, %0\n\ts_nop 0\n\tbuffer_load_dwordx4 %1, %2, %3 offen lds" ::"s"(lds), "v"(voff), "s"(rs), "s"(soff) : "memory");
}
template <int OFF> DI void ld128c(u32x4& d, u32 voff, u32x4 rs, u32 soff) {
  asm volatile("buffer_load_dwordx4 %0, %1, %2, %3 offen offset:%4 sc1" : "=v"(d) : "v"(voff), "s"(rs), "s"(soff), "i"(OFF) : "memory");
}
DI void ld128(u32x4& d, u32 voff, u32x4 rs, u32 soff) { asm volatile("buffer_load_dwordx4 %0, %1, %2, %3 offen" : "=v"(d) : "v"(voff), "s"(rs), "s"(soff) : "memory"); }
template <int OFF = 0> DI void ld32c(u32& d, u32 voff, u32x4 rs, u32 soff) { asm volatile("buffer_load_dword %0, %1, %2, %3 offen offset:%4 sc1" : "=v"(d) : "v"(voff), "s"(rs), "s"(soff), "i"(OFF) : "memory"); }
DI void st128c(u32x4 v, u32 voff, u32x4 rs, u32 soff) { asm volatile("buffer_store_dwordx4 %0, %1, %2, %3 offen sc1\n\ts_nop 1" ::"v"(v), "v"(voff), "s"(rs), "s"(soff) : "memory"); }
DI void st32c(u32 v, u32 voff, u32x4 rs, u32 soff) { asm volatile("buffer_store_dword %0, %1, %2, %3 offen sc1" ::"v"(v), "v"(voff), "s"(rs), "s"(soff) : "memory"); }
template <int N> DI void vmwait() { asm volatile("s_waitcnt vmcnt(%0)" ::"i"(N) : "memory"); }
template <int N> DI void vmwait(u32x4& a) { asm volatile("s_waitcnt vmcnt(%1)" : "+v"(a) : "i"(N) : "memory"); }
DI void bar() { asm volatile("s_waitcnt lgkmcnt(0)\n\ts_barrier" ::: "memory"); }
template <int N> DI void vmbar() { asm volatile("s_waitcnt vmcnt(%0) lgkmcnt(0)\n\ts_barrier" ::"i"(N) : "memory"); }

DI void ev_load(u32& e, u32x4 ws) { asm volatile("buffer_load_dword %0, off, %1, 0" : "+v"(e) : "s"(ws) : "memory"); }
template <int N> DI u32 tag_wait(u32 e) {
  u32 t;
  asm volatile("s_waitcnt vmcnt(%2)\n\tv_readfirstlane_b32 %0, %1\n\ts_nop 1" : "=s"(t) : "v"(e), "i"(N) : "memory");
  return t * 1664525u + 1013904223u;
}
DI void poll(u32 voff, u32x4 ws, u32 tag) {
  for (int it = 0; it < SPIN_MAX; ++it) {
    u32 f;
    asm volatile("buffer_load_dword %0, %1, %2, 0 offen sc1\n\ts_waitcnt vmcnt(0)" : "=&v"(f) : "v"(voff), "s"(ws) : "memory");
    if (__builtin_amdgcn_ballot_w64(f != tag) == 0) break;
    __builtin_amdgcn_s_sleep(4);
  }
}
DI void poll_bar(u32 voff, u32x4 ws, u32 tag) {
  u32 f;
  asm volatile("buffer_load_dword %0, %1, %2, 0 offen sc1\n\ts_waitcnt vmcnt(0) lgkmcnt(0)\n\ts_barrier" : "=&v"(f) : "v"(voff), "s"(ws) : "memory");
  if (__builtin_amdgcn_ballot_w64(f != tag) != 0) poll(voff, ws, tag);
}
template <int C> DI float dppadd(float v) { return v + __int_as_float(__builtin_amdgcn_update_dpp(0, __float_as_int(v), C, 0xf, 0xf, true)); }
DI float rowsum(float v) { v = dppadd<0xB1>(v); v = dppadd<0x4E>(v); v = dppadd<0x141>(v); return dppadd<0x140>(v); }
DI float rdl(float v, int l) { return __int_as_float(__builtin_amdgcn_readlane(__float_as_int(v), l)); }
DI float fold32(float a, float b) { const auto r = __builtin_amdgcn_permlane32_swap(__float_as_uint(a), __float_as_uint(b), false, false); return __uint_as_float(r[0]) + __uint_as_float(r[1]); }
DI float fold16(float a, float b) { const auto r = __builtin_amdgcn_permlane16_swap(__float_as_uint(a), __float_as_uint(b), false, false); return __uint_as_float(r[0]) + __uint_as_float(r[1]); }
template <bool E> DI u32 phi2(float a, float b) {
  constexpr float l2e = 1.4426950408889634f, c = -l2e * (E ? 1.000001f : 1.f);
  float e0 = __builtin_amdgcn_fmed3f(__builtin_amdgcn_exp2f(__builtin_fmaf(a, l2e, c)), 0.f, 1.f), e1 = __builtin_amdgcn_fmed3f(__builtin_amdgcn_exp2f(__builtin_fmaf(b, l2e, c)), 0.f, 1.f);
  if (E) { e0 += 1e-6f; asm("" : "+v"(e0)); e1 += 1e-6f; }
  return pk(__builtin_amdgcn_fmed3f(a, e0, 3.4028234664e38f), __builtin_amdgcn_fmed3f(b, e1, 3.4028234664e38f));
}
template <bool E> DI u32x4 phi8(const f32x16& x, int o) { return u32x4{phi2<E>(x[o], x[o + 1]), phi2<E>(x[o + 2], x[o + 3]), phi2<E>(x[o + 4], x[o + 5]), phi2<E>(x[o + 6], x[o + 7])}; }
DI float gelu(float v) {
  const float t = v * (-2.3022081981f - 0.1029432397f * v * v);
  return v * __builtin_amdgcn_rcpf(1.f + __builtin_amdgcn_exp2f(t));
}

constexpr int wslot(int P, int c) { return (c + (P && c < 3 ? 4 : 1)) % 5; }
struct WSrc { u32x4 rs; u32 voff, sbase, stride, lds, rot; };
template <int P> DI void w_one(const WSrc& s, int c, int i) { glds(s.lds + wslot(P, c) * 32768 + i * 1024, s.voff, s.rs, s.sbase + (64 * ((c + s.rot) & 7) + 8 * i) * s.stride); }
template <int P> DI void w_issue(const WSrc& s, int c) {
#pragma unroll
  for (int i = 0; i < 4; ++i) w_one<P>(s, c, i);
}
struct WA { u32 a[3][2]; };
DI WA w_addr(u32 a0) { WA r;
#pragma unroll
  for (int i = 0; i < 3; ++i) { r.a[i][0] = a0 + i * 65536; r.a[i][1] = (a0 ^ 64) + i * 65536; asm volatile("" : "+v"(r.a[i][0]), "+v"(r.a[i][1])); }
  return r; }
template <int P> DI void w_frag(const WA& r, u32x4 (&f)[2], int c, int ks) {
#pragma unroll
  for (int b = 0; b < 2; ++b) { const u32 a = r.a[wslot(P, c) >> 1][b] + (wslot(P, c) & 1) * 32768 + ks * 2048; f[b] = trd2(a, a + 1024); }
}
template <int P, int C, int F> DI void w_round(const WSrc& s, const WA& a0, u32x4 (&wr)[16][2]) {
#pragma unroll
  for (int k = 0; k < 4; ++k) { w_frag<P>(a0, wr[4 * C + k], C, k); SB(); if constexpr (F < 8) w_one<P>(s, F, k); SB(); }
}
DI void w_stage(const WA& a0, u32x4 (&wr)[16][2], int k0, int k1) {
#pragma unroll
  for (int k = k0; k < k1; ++k) w_frag<1>(a0, wr[k], k >> 2, k & 3);
}
template <int C0> DI void w_flag(const WSrc& s, int w, u32 tag, u32 fv, u32x4 rws, u32 flag) {
  if constexpr (C0 < 6) w_issue<1>(s, C0);
  vmbar<(C0 < 6 ? 4 : 0)>();
  if (w == 0) st32c(tag, fv, rws, flag);
#pragma unroll
  for (int c = C0 + 1; c < 6; ++c) w_issue<1>(s, c);
}
template <int N> DI void vmbar0(int w) { if (w == 0) vmwait<N + 1>(); else vmwait<N>(); bar(); }
struct DSrc { u32x4 rs; u32 voff, sb, rot; };
template <int I> DI void d_issue(u32x4 (&d)[32], const DSrc& s) { ld128c<(I & 3) * 1024>(d[I], s.voff, s.rs, s.sb + (((I >> 2) + s.rot) & 7) * 4096); }
constexpr int KB = 32 - WIN;
template <int P, int K> DI void wr_steps(u32x4 (&d)[32], u32x4 (&wr)[16][2], const WA& a0, const DSrc& s, const WSrc& nx, u32 sig, f32x16& c0, f32x16& c1) {
  if constexpr (K < 32) {
    if constexpr (K == KB) {
      if constexpr (P == 0) bar();
      else { asm volatile("s_waitcnt lgkmcnt(0)" ::: "memory"); *(volatile LAS u32*)sig = 1; }
    }
    if constexpr (K % 8 == 0) __builtin_amdgcn_s_setprio(3 - K / 8);
    vmwait<(P == 0 || K < KB ? WIN - 1 : 31 - K)>(d[K]);
    c0 = mfma(wr[K & 15][0], d[K], c0); c1 = mfma(wr[K & 15][1], d[K], c1);
    asm volatile("" : "+v"(c0), "+v"(c1));
    if constexpr (K + WIN < 32) d_issue<K + WIN>(d, s);
    if constexpr (P == 0 && K == 1) bar();
    if constexpr (P == 0 && K >= KB) w_one<1>(nx, (K - KB) >> 2, (K - KB) & 3);
    if constexpr (K >= 1 && K <= 16) w_frag<P>(a0, wr[K - 1], 4 + ((K - 1) >> 2), (K - 1) & 3);
    wr_steps<P, K + 1>(d, wr, a0, s, nx, sig, c0, c1);
  }
}
template <int I> DI void d_first(u32x4 (&d)[32], const DSrc& s) { if constexpr (I < WIN) { d_issue<I>(d, s); d_first<I + 1>(d, s); } }
template <int P> DI void wr_gemm(u32x4 (&wr)[16][2], const WA& a0, const DSrc& s, const WSrc& nx, u32 sig, f32x16& c0, f32x16& c1) {
  u32x4 d[32];
  d_first<0>(d, s);
  c0 = splat(0.f); c1 = splat(0.f);
  wr_steps<P, 0>(d, wr, a0, s, nx, sig, c0, c1);
  if constexpr (P == 0) {
#pragma unroll
    for (int i = WIN; i < 12; ++i) w_one<1>(nx, i >> 2, i & 3);
  }
}
constexpr u32 RSL = 1088;
template <u32 RS> DI void red_write(u32 a, u32 bs, const f32x16& c0, const f32x16& c1) {
#pragma unroll
  for (int q = 0; q < 4; ++q) {
    *(LAS f32x4*)(a + q * RS) = f32x4{c0[4 * q], c0[4 * q + 1], c0[4 * q + 2], c0[4 * q + 3]};
    *(LAS f32x4*)(a + bs + q * RS) = f32x4{c1[4 * q], c1[4 * q + 1], c1[4 * q + 2], c1[4 * q + 3]};
  }
}

template <int O, int I> DI void put16(const f32x16& c) {
  if constexpr (I < 16) { asm volatile("ds_write_addtid_b32 %0 offset:%1" ::"v"(c[I]), "i"(O + (I >> 1) * 768 + (I & 1) * 272) : "memory"); put16<O, I + 1>(c); }
}

constexpr u32 AST = 49152;
struct ASrc { u32x4 rx, rw; u32 vx0, vx1, vw, vq, sx, s0, s1, sq, rot, L; int w; };
DI void a_one(const ASrc& s, int c, int j) {
  const u32 st = s.L + (c % 3) * AST, cc = (c + s.rot) & 7;
  if (j < 4) glds(st + s.w * 4096 + j * 1024, (j & 1) ? s.vx1 : s.vx0, s.rx, s.sx + j * 8192 + cc * 128);
  else glds(st + 32768 + (j - 4) * 8192 + s.w * 1024, s.vw, s.rw, (j == 4 ? s.s0 : s.s1) + cc * (64 * 3072));
}
template <bool KV, int C, int J> DI void a_pref(const ASrc& s, const WSrc& nx) {
  if constexpr (J < 0) {}
  else if constexpr (C < 8) a_one(s, C, J);
  else if constexpr (C == 8) { if constexpr (KV && J < 4) glds(s.L + 98304 + s.w * 4096 + J * 1024, s.vq, s.rw, s.sq + J * (16 * 3072)); }
  else w_one<1>(nx, (6 * (C - 9) + J) >> 2, (6 * (C - 9) + J) & 3);
}
constexpr int a_gap(int C, int g) {
  const int t0[6] = {5, 6, 9, 10, 13, 14}, t1[6] = {1, 3, 6, 9, 11, 14}, t2[6] = {1, 2, -1, -1, -1, -1};
  for (int j = 0; j < 6; ++j) if ((C == 2 ? t0[j] : C == 10 ? t2[j] : t1[j]) == g) return j;
  return -1;
}
struct AMisc { u32x4 rws; u32 cfoff, tag, rdy, clr; int lane; };
struct AAd { u32 x0[4], x1[4], wa; };
template <bool KV, int T> DI void a_steps(const ASrc& s, const WSrc& nx, AAd& ad, u32x4 (&x0)[33], u32x4 (&f)[2][3], f32x16 (&c)[4], u32& cf, const AMisc& ms) {
  if constexpr (T < 32) {
    constexpr int C = T >> 2, K = T & 3, P = T & 1, N = (T + 1) & 3;
    if constexpr (K == 3) {
      if constexpr (C < 6) vmbar<6 + ((KV && C == 1) || C == 3)>(); else if constexpr (C == 6) vmbar<KV ? 5 : 0>(); else vmbar<6>();
      if constexpr (C < 7) {
        constexpr u32 dl = (C + 1) % 3 ? AST : 0u - 2 * AST;
#pragma unroll
        for (int k = 0; k < 4; ++k) { ad.x0[k] += dl; ad.x1[k] += dl; }
        ad.wa += dl;
        asm volatile("" : "+v"(ad.x0[0]), "+v"(ad.x0[1]), "+v"(ad.x0[2]), "+v"(ad.x0[3]), "+v"(ad.x1[0]), "+v"(ad.x1[1]), "+v"(ad.x1[2]), "+v"(ad.x1[3]), "+v"(ad.wa));
      }
      if constexpr (C == 0 && KV) { if (s.w < 4) st128c(u32x4{~0u, ~0u, ~0u, ~0u}, ms.lane * 16, ms.rws, ms.clr); else st32c(~0u, (ms.lane & 31) * 4, ms.rws, ms.clr); }
      if constexpr (C == 2) { st32c(ms.tag, ms.rdy, ms.rws, 0); }
      if constexpr (C == 5 && KV) ld32c(cf, ms.cfoff, ms.rws, 0);
    }
    constexpr int PC = K == 3 ? C + 3 : C + 2, G0 = 4 * N;
    __builtin_amdgcn_s_setprio(3 - N);
    if constexpr (T < 31) { x0[T + 1] = lds128(ad.x0[N]); f[P ^ 1][1] = trd2(ad.wa + N * 2048, ad.wa + N * 2048 + 512); }
    SB();
    if constexpr (KV) c[0] = mfma(x0[T], f[P][1], c[0]); else c[0] = mfma(f[P][1], x0[T], c[0]);
    SB();
    if constexpr (T < 31) { f[P ^ 1][0] = lds128(ad.x1[N]); f[P ^ 1][2] = trd2(ad.wa + 8192 + N * 2048, ad.wa + 8192 + N * 2048 + 512); }
    a_pref<KV, PC, a_gap(PC, G0)>(s, nx);
    SB();
    if constexpr (KV) c[1] = mfma(f[P][0], f[P][1], c[1]); else c[1] = mfma(f[P][1], f[P][0], c[1]);
    SB();
    a_pref<KV, PC, a_gap(PC, G0 + 1)>(s, nx);
    SB();
    if constexpr (KV) c[2] = mfma(x0[T], f[P][2], c[2]); else c[2] = mfma(f[P][2], x0[T], c[2]);
    SB();
    a_pref<KV, PC, a_gap(PC, G0 + 2)>(s, nx);
    SB();
    if constexpr (KV) c[3] = mfma(f[P][0], f[P][2], c[3]); else c[3] = mfma(f[P][2], f[P][0], c[3]);
    SB();
    a_pref<KV, PC, a_gap(PC, G0 + 3)>(s, nx);
    SB();
    a_steps<KV, T + 1>(s, nx, ad, x0, f, c, cf, ms);
  }
}

DI ASrc a_src(const char* x, const char* wqkv, u32 L, int lane, int w, int xcd, int slot) {
  const bool kv = slot < 30;
  const int ai = slot - 22, rc = (xcd & 6) + (kv ? ai >> 2 : slot - 30), hg = (xcd & 1) * 4 + (ai & 3), qj = xcd & 1;
  ASrc sa;
  sa.rx = srd(x, 2048 * 1024); sa.rw = srd(wqkv, 512 * 3072); sa.L = L; sa.w = w;
  sa.rot = xcd & 6;
  sa.vx0 = (lane >> 3) * 1024 + ((lane & 7) ^ (lane >> 4)) * 16; sa.vx1 = sa.vx0 ^ 64;
  sa.vw = (lane >> 3) * 3072 + ((lane & 7) ^ ((lane >> 2) & 4)) * 16;
  sa.vq = (lane >> 2) * 3072 + (lane & 3) * 16;
  sa.sx = (rc * 256 + w * 32) * 1024;
  sa.s0 = w * 8 * 3072 + (kv ? 1024 + hg * 128 : 512 + qj * 256);
  sa.s1 = sa.s0 + (kv ? 1024 : 128);
  sa.sq = ((w + sa.rot) & 7) * (64 * 3072) + hg * 64;
  return sa;
}
DI WSrc w1_src(const char* w1, u32 lds, int lane, int w, int xcd, int slot, int& ft, int& dt1) {
  const int gj = slot < 16 ? (xcd >> 1) * 16 + slot : 48 + (xcd >> 1) * 6 + slot;
  const u32 wch = (lane & 7) ^ ((lane >> 2) & 4);
  ft = gj >> 2; dt1 = (xcd & 1) * 4 + (gj & 3);
  WSrc s1;
  s1.rs = srd(w1, 2048 * 2752); s1.stride = 2752; s1.lds = lds;
  s1.voff = (lane >> 3) * 2752 + (ft == 21 ? (wch & 3) : wch) * 16;
  s1.sbase = ((w >> 1) * 512 + (w & 1) * 32) * 2752 + ft * 128;
  s1.rot = (2 * (dt1 + ft) + (xcd & 1)) & 7;
  return s1;
}
template <bool KV> DI u32 attn(u32 ev, const char* x, const char* wqkv, u32x4 rws, const WSrc& s2, u32 L, int lane, int w, int xcd, int slot, u32 wa0, u32x4 (&wr)[16][2]) {
  const int hh = lane >> 5, col = lane & 31, q4 = (lane & 15) >> 2, mi = w >> 1, ni = w & 1;
  const int ai = slot - 22, rc = (xcd & 6) + (KV ? ai >> 2 : slot - 30), hg = (xcd & 1) * 4 + (ai & 3), qj = xcd & 1;
  const ASrc sa = a_src(x, wqkv, L, lane, w, xcd, slot);
  AAd ad;
#pragma unroll
  for (int k = 0; k < 4; ++k) { ad.x0[k] = L + (mi * 64 + ni * 32 + col) * 128 + ((2 * k + hh) ^ ((col >> 1) & 7)) * 16; ad.x1[k] = ad.x0[k] ^ 4096; }
  ad.wa = L + 32768 + (8 * hh + q4) * 128 + 64 * ((q4 >> 1) ^ ni) + 32 * ((lane >> 4) & 1) + 8 * (lane & 3);
  const int pr = hg + (mi & 1) * 8;
  const bool ga = w < 4;
  const u32 cfoff = ga ? WS_F + xcd * FRS + (128 + (pr >> 1) * 8 + (lane & 7)) * 128 : OOB;
  asm volatile("" : "+v"(ad.x0[0]), "+v"(ad.x0[1]), "+v"(ad.x0[2]), "+v"(ad.x0[3]), "+v"(ad.x1[0]), "+v"(ad.x1[1]), "+v"(ad.x1[2]), "+v"(ad.x1[3]), "+v"(ad.wa)::"memory");
  const u32 tag = tag_wait<6>(ev);
  bar();
  const AMisc ms = {rws, cfoff, tag, lane == 0 ? WS_F + w * FRS + (KV ? 128 + hg * 8 + rc : 192 + qj * 8 + rc) * 128 : OOB, w < 4 ? WS_KV + ((hg * 2 + (w >> 1)) * 8 + rc) * 2048 + (w & 1) * 1024 : WS_Z + ((hg * 2 + (w & 1)) * 8 + rc) * 128, lane};
  u32x4 x0[33], f[2][3]; f32x16 c[4]; u32 cf;
  c[0] = c[1] = splat(KV ? 1.f : 1.000001f); c[2] = c[3] = splat(KV ? 0.f : 1.000001f);
  x0[0] = lds128(ad.x0[0]); f[0][0] = lds128(ad.x1[0]); f[0][1] = trd2(ad.wa, ad.wa + 512); f[0][2] = trd2(ad.wa + 8192, ad.wa + 8192 + 512);
  a_steps<KV, 0>(sa, s2, ad, x0, f, c, cf, ms);
  __builtin_amdgcn_s_setprio(0);
  asm volatile("" : "+v"(c[0]), "+v"(c[1]), "+v"(c[2]), "+v"(c[3]));
  const u32 pfl = WS_F + xcd * FRS + (512 + rc * 16 + 8 + qj) * 128;
  if constexpr (!KV) {
#pragma unroll
    for (int j = 0; j < 4; ++j)
#pragma unroll
      for (int h = 0; h < 2; ++h) { st128c(phi8<true>(c[j], 8 * h), lane * 16 + qj * 8192 + ni * 2048, rws, WS_P + (rc * 8 + (w ^ (j & 1))) * 16384 + ((j & 2) * 2 + h) * 1024); SB(); }
    asm volatile("s_barrier" ::: "memory");
    w_issue<1>(s2, 2);
    vmbar<4>();
    if (w == 0) st32c(tag, lane == 0 ? 0 : OOB, rws, pfl);
    if (blockIdx.x == 255 && w == 0) {
#pragma unroll 1
      for (int i = 0; i < 4; ++i) poll(WS_F + 7 * FRS + (i * 64 + lane) * 128, rws, tag);
      st32c(tag, lane == 0 ? 0 : OOB, rws, WS_E);
    }
    vmbar<0>();
    w_stage(w_addr(wa0), wr, 0, 12);
    bar();
    w_flag<3>(s2, w, tag, OOB, rws, 0);
    return tag;
  }
  const u32 S = L + 65536, KT = S + 14336, ZT = KT + 2048;
  if (w >= 4) w_issue<1>(s2, 2);
  const u32 on = ((lane >> 4) ^ (lane >> 2)) & 1 ? 0 : 0x3f803f80u;
  const u32x4 cs = {on, on, on, on};
  f32x16 d1 = splat(0.f); f32x4 d2 = {64e-6f, 64e-6f, 64e-6f, 64e-6f}, d3 = {0.f, 0.f, 0.f, 0.f};
  u32x4 p[4];
#pragma unroll
  for (int i = 0; i < 4; ++i) {
    const f32x16& k = c[i >> 1]; const int o = 8 * (i & 1);
    p[i][0] = phi2<false>(k[o], k[o + 1]); SB();
    if (i) d2 = mfma16(cs, p[i - 1], d2);
    const u32x4 v = pack8(c[2 + (i >> 1)], o);
    p[i][1] = phi2<false>(k[o + 2], k[o + 3]); SB();
    d3 = mfma16(cs, v, d3);
    p[i][2] = phi2<false>(k[o + 4], k[o + 5]); p[i][3] = phi2<false>(k[o + 6], k[o + 7]); SB();
    d1 = mfma(p[i], v, d1);
    SB(); if (w < 4) w_one<1>(s2, 2, i); SB();
  }
  d2 = mfma16(cs, p[3], d2);
  const bool lo = col < 16;
  f32x4 k0 = {lo ? d1[0] : d1[8], lo ? d1[1] : d1[9], lo ? d1[2] : d1[10], lo ? d1[3] : d1[11]};
  f32x4 k1 = {lo ? d1[4] : d1[12], lo ? d1[5] : d1[13], lo ? d1[6] : d1[14], lo ? d1[7] : d1[15]};
  const float ev6 = d3[0] * 1e-6f;
  k0 = add4(k0, f32x4{ev6, ev6, ev6, ev6}); k1 = add4(k1, f32x4{ev6, ev6, ev6, ev6});
  const float zp = d2[0];
  if (w >= 2) { *(LAS f32x4*)(S + (w - 2) * 2048 + lane * 16) = k0; *(LAS f32x4*)(S + (w - 2) * 2048 + 1024 + lane * 16) = k1; *(LAS float*)(S + 12288 + (w - 2) * 256 + lane * 4) = zp; }
  vmbar<0>();
  if (w < 2) {
    const u32 a = S + w * 2048 + lane * 16, z = S + 12288 + w * 256 + lane * 4;
    k0 = add4(add4(k0, ldsf4(a)), add4(ldsf4(a + 4096), ldsf4(a + 8192))); k1 = add4(add4(k1, ldsf4(a + 1024)), add4(ldsf4(a + 5120), ldsf4(a + 9216)));
    const float t = add1(add1(zp, ldsf(z)), add1(ldsf(z + 512), ldsf(z + 1024)));
    u32x4 p0 = __builtin_bit_cast(u32x4, k0), p1 = __builtin_bit_cast(u32x4, k1);
#pragma unroll
    for (int i = 0; i < 4; ++i) { p0[i] = umin(p0[i], ~1u); p1[i] = umin(p1[i], ~1u); }
    const u32 o = (hg * 2 + w) * 8 + rc;
    st128c(p0, lane * 16, rws, WS_KV + o * 2048); st128c(p1, lane * 16, rws, WS_KV + o * 2048 + 1024); st32c(umin(__float_as_uint(t), ~1u), col * 4, rws, WS_Z + o * 128);
  }
  u32x4 pq[2][2], t[8]; u32x2 z; u32 pf;
  u32 qa = L + 98304 + (8 * hh + q4) * 64 + 32 * ((lane >> 4) & 1) + 8 * (lane & 3);
  asm volatile("" : "+v"(qa), "+v"(cf));
  u32x4 qf[4];
#pragma unroll
  for (int i = 0; i < 3; ++i) qf[i] = trd2(qa + i * 1024, qa + i * 1024 + 256);
  f32x16 cq = splat(1.000001f);
#pragma unroll
  for (int i = 0; i < 32; ++i) {
    SB();
    if (i < 29) qf[(i + 3) & 3] = trd2(qa + (i + 3) * 1024, qa + (i + 3) * 1024 + 256);
    SB();
    cq = mfma(qf[i & 3], x0[i], cq);
  }
  SB();
  auto sweep = [&]() {
#pragma unroll
    for (int r = 0; r < 8; ++r) ld128c<0>(t[r], lane * 16, rws, WS_KV + (pr * 8 + r) * 2048 + ni * 1024);
    asm volatile("buffer_load_dwordx2 %0, %1, %2, %3 offen sc1" : "=v"(z) : "v"((lane >> 3) * 128 + (lane & 7) * 8), "s"(rws), "s"(WS_Z + pr * 1024 + ni * 64) : "memory");
  };
  asm volatile("" : "=v"(t[0]), "=v"(t[1]), "=v"(t[2]), "=v"(t[3]), "=v"(t[4]), "=v"(t[5]), "=v"(t[6]), "=v"(t[7]), "=v"(z));
  if (ga && __builtin_amdgcn_ballot_w64(cf != tag) != 0) poll(cfoff, rws, tag);
  ld32c(pf, pfl, rws, 0);
  if (mi == 0) sweep();
  pq[0][0] = phi8<true>(cq, 0); pq[0][1] = phi8<true>(cq, 8);
  asm volatile("" : "+v"(pq[0][0]), "+v"(pq[0][1]));
  w_stage(w_addr(wa0), wr, 0, 12);
  SB();
  if (mi == 1) sweep();
  if (!ga) vmwait<0>();
  asm volatile("s_waitcnt vmcnt(9)" : "+v"(pf)::"memory");
  if (__builtin_amdgcn_ballot_w64(pf != tag) != 0) poll(pfl, rws, tag);
  ld128c<0>(pq[1][0], lane * 16, rws, WS_P + (rc * 8 + w) * 16384 + hg * 2048); ld128c<1024>(pq[1][1], lane * 16, rws, WS_P + (rc * 8 + w) * 16384 + hg * 2048);
#define GW(N) asm volatile("s_waitcnt vmcnt(%9)" : "+v"(t[0]), "+v"(t[1]), "+v"(t[2]), "+v"(t[3]), "+v"(t[4]), "+v"(t[5]), "+v"(t[6]), "+v"(t[7]), "+v"(z) : "i"(N) : "memory")
  GW(2);
  if (ga) {
    for (int it = 0; it < SPIN_MAX; ++it) {
      u32 m = umax(z[0], z[1]);
#pragma unroll
      for (int r = 0; r < 8; ++r) m = umax(umax(umax(m, t[r][0]), t[r][1]), umax(t[r][2], t[r][3]));
      if (__builtin_amdgcn_ballot_w64(m == ~0u) == 0) break;
      sweep();
      GW(0);
    }
    f32x4 a = __builtin_bit_cast(f32x4, t[0]);
#pragma unroll
    for (int r = 1; r < 8; ++r) a += __builtin_bit_cast(f32x4, t[r]);
    float zs = fold32(__uint_as_float(z[0]), __uint_as_float(z[1]));
    zs = dppadd<0x128>(fold16(zs, zs));
    const int pl = mi, zf = 2 * (lane & 7) + hh;
    *(LAS u32x2*)(KT + pl * 1024 + lane * 16 + ni * 8) = u32x2{pk(a[0], a[1]), pk(a[2], a[3])};
    *(LAS unsigned short*)(ZT + pl * 64 + ni * 32 + ((zf >> 2) & 1) * 16 + ((zf & 3) + 4 * (zf >> 3)) * 2) = (unsigned short)pk(zs, zs);
  }
  bar();
  f32x16 dd[2][2];
#pragma unroll
  for (int j = 0; j < 2; ++j) {
    if (j) asm volatile("s_waitcnt vmcnt(0)" : "+v"(pq[1][0]), "+v"(pq[1][1])::"memory");
#pragma unroll
    for (int h = 0; h < 2; ++h) dd[j][h] = mfma(lds128(((col < 16) == (h == 0)) ? KT + j * 1024 + lane * 16 : ZT + j * 64 + h * 32 + hh * 16), pq[j][h], splat(0.f));
  }
  SB();
  w_issue<1>(s2, 3);
  SB();
#pragma unroll
  for (int j = 0; j < 2; ++j)
#pragma unroll
    for (int h = 0; h < 2; ++h) {
      const int o = 8 * h;
      const f32x16& e = dd[j][h];
      const float rd = __builtin_amdgcn_rcpf(e[8 - o] + 1e-6f);
      const u32x4 ov = {pk(e[o] * rd, e[o + 1] * rd), pk(e[o + 2] * rd, e[o + 3] * rd), pk(e[o + 4] * rd, e[o + 5] * rd), pk(e[o + 6] * rd, e[o + 7] * rd)};
      st128c(ov, lane * 16, rws, WS_O + (rc * 8 + w) * 32768 + ((hg + 8 * j) * 2 + h) * 1024);
    }
  bar();
  w_flag<4>(s2, w, tag, lane < 2 ? ((rc & 6) + lane) * FRS : OOB, rws, WS_F + (512 + rc * 16 + hg) * 128);
  return tag;
}

__global__ __launch_bounds__(512, 2) void fused_kernel(const char* __restrict__ x, const char* __restrict__ wqkv, const char* __restrict__ wo,
                                                        const char* __restrict__ w1, const char* __restrict__ b1, const char* __restrict__ w2,
                                                        const char* __restrict__ b2, const char* __restrict__ gw, char* __restrict__ out, char* ws) {
  __shared__ __attribute__((aligned(1024))) char sm[163840];
  u32 ev;
  {
    unsigned long long pc; u32 cn;
    asm volatile("s_getpc_b64 %0\n.Lfk_pc:\n\ts_mov_b32 %1, .Lfk_end-.Lfk_pc" : "=s"(pc), "=s"(cn));
    const u32 vo = (blockIdx.x >> 3 & 15) == 0 ? threadIdx.x * 128 : OOB;
    asm volatile("buffer_load_dword %0, %1, %2, 0 offen" : "=v"(ev) : "v"(vo), "s"(srd((const void*)pc, cn)) : "memory");
    SB();
  }
  asm volatile("" : "+s"(x), "+s"(wqkv), "+s"(wo), "+s"(w1), "+s"(b1), "+s"(w2), "+s"(b2), "+s"(gw), "+s"(out), "+s"(ws));
  const u32 L = (u32)(size_t)(LAS char*)sm;
  const int lane = threadIdx.x & 63, w = __builtin_amdgcn_readfirstlane(threadIdx.x >> 6);
  int xcd = blockIdx.x & 7, slot = blockIdx.x >> 3;
  const int hh = lane >> 5;
  const u32x4 rws = srd(ws, WS_END);
  u32x4 xr[2], gr;
  asm volatile("s_nop 4" : "=v"(xr[0]), "=v"(xr[1]), "=v"(gr)::"memory");
  ev_load(ev, rws);
  if (slot < 22) {
    int ft, dt1;
    const WSrc s1 = w1_src(w1, L + w * 4096, lane, w, xcd, slot, ft, dt1);
    if (slot < 16) {
      const int r = (xcd * 16 + slot) * 16 + w;
      const u32x4 rx = srd(x, 2048 * 1024);
      ld128(xr[0], lane * 16, rx, r * 1024); ld128(xr[1], lane * 16, rx, (r + 8) * 1024);
      ld128(gr, lane * 16, srd(gw, 1024), 0);
      asm volatile("s_barrier" ::: "memory");
    }
    w_issue<0>(s1, 0);
  } else {
    const ASrc sa = a_src(x, wqkv, L, lane, w, xcd, slot);
#pragma unroll
    for (int j = 0; j < 6; ++j) a_one(sa, 0, j);
    asm volatile("s_barrier" ::: "memory");
#pragma unroll
    for (int j = 0; j < 6; ++j) a_one(sa, 1, j);
  }
  asm volatile("" : "+s"(slot), "+s"(xcd));
  const int g = w >> 1, hf = w & 1, q4 = (lane & 15) >> 2;
  const u32 wch = (lane & 7) ^ ((lane >> 2) & 4);
  const u32 wa0 = L + g * 8192 + (4 * hh + q4) * 128 + 64 * (q4 >> 1) + 32 * ((lane >> 4) & 1) + 8 * (lane & 3);

  const int dt = (xcd & 1) * 4 + (slot & 3), stl = (xcd >> 1) * 8 + (slot >> 2);
  WSrc s2;
  s2.rs = g < 3 ? srd(w2, 1376 * 4096) : srd(wo, 512 * 1024); s2.stride = g < 3 ? 4096 : 1024; s2.lds = L + g * 8192 + hf * 4096;
  s2.voff = (lane >> 3) * s2.stride + wch * 16;
  s2.sbase = g < 3 ? (g * 512 + hf * 32) * 4096 + stl * 128 : hf * 32 * 1024 + dt * 128;
  s2.rot = 2 * ((slot + (slot >> 2)) & 3) + (xcd & 1);
  u32 tag;
  u32x4 wr[16][2];

  if (slot >= 30) tag = attn<false>(ev, x, wqkv, rws, s2, L, lane, w, xcd, slot, wa0, wr);
  else if (slot >= 22) tag = attn<true>(ev, x, wqkv, rws, s2, L, lane, w, xcd, slot, wa0, wr);
  else {
    const int gi = slot < 16 ? xcd * 16 + slot : 112 + xcd * 6 + slot;
    int ft, dt1;
    const WSrc s1 = w1_src(w1, s2.lds, lane, w, xcd, slot, ft, dt1);
    if (slot < 16) {
      const int sb = xcd * 16 + slot;
      tag = tag_wait<4>(ev);
      asm volatile("" : "+v"(xr[0]), "+v"(xr[1]), "+v"(gr)::"memory");
      f32x2 v[2][4], t; float s[2], gd[2], q[2];
#pragma unroll
      for (int j = 0; j < 2; ++j) {
#pragma unroll
        for (int i = 0; i < 4; ++i) v[j][i] = f32x2{blo(xr[j][i]), bhi(xr[j][i])};
        t = (v[j][0] + v[j][1]) + (v[j][2] + v[j][3]); s[j] = t[0] + t[1];
      }
      const float t0 = fold32(s[0], s[1]), t1 = rowsum(fold16(t0, t0)) * (1.f / 512.f);
#pragma unroll
      for (int j = 0; j < 2; ++j) {
        t = f32x2{0.f, 0.f};
#pragma unroll
        for (int i = 0; i < 4; ++i) t = __builtin_elementwise_fma(v[j][i], f32x2{blo(gr[i]), bhi(gr[i])}, t);
        gd[j] = t[0] + t[1];
        const float m = rdl(t1, 32 * j);
        t = f32x2{0.f, 0.f};
#pragma unroll
        for (int i = 0; i < 4; ++i) { v[j][i] -= m; t = __builtin_elementwise_fma(v[j][i], v[j][i], t); }
        q[j] = t[0] + t[1];
      }
      const float t2 = rowsum(fold16(fold32(q[0], q[1]), fold32(gd[0], gd[1])));
      const float rs = __builtin_amdgcn_rsqf(t2 * (1.f / 512.f) + 1e-5f);
#pragma unroll
      for (int j = 0; j < 2; ++j) {
        const float r = rdl(rs, 32 * j);
#pragma unroll
        for (int i = 0; i < 4; ++i) v[j][i] *= r;
        *(LAS u32x4*)(L + (w + 8 * j) * 1088 + lane * 16) = u32x4{pk(v[j][0][0], v[j][0][1]), pk(v[j][1][0], v[j][1][1]), pk(v[j][2][0], v[j][2][1]), pk(v[j][3][0], v[j][3][1])};
      }
      const float gt = __builtin_amdgcn_rcpf(1.f + __builtin_amdgcn_exp2f(t2 * -1.4426950408889634f));
      bar();
      const u32 ta = L + (4 * hh + q4) * 1088 + w * 128 + 32 * ((lane >> 4) & 1) + 8 * (lane & 3);
      const u32x4 h0 = trd2(ta, ta + 8 * 1088), h1 = trd2(ta + 64, ta + 64 + 8 * 1088);
      st32c(__float_as_uint(gt), (lane & 31) == 16 ? lane & 32 : OOB, rws, WS_G + (sb * 16 + w) * 4);
      st128c(h0, lane * 16, rws, WS_H + w * 262144 + sb * 1024);
      st128c(h1, lane * 16, rws, WS_H + w * 262144 + 131072 + sb * 1024);
      asm volatile("s_barrier" ::: "memory");
      w_issue<0>(s1, 1);
      vmbar<4>();
    } else {
      u32 tl;
      asm volatile("buffer_load_dword %0, %1, %2, %3 offen sc1" : "=v"(tl) : "v"(w < 6 ? (lane << 18) + 2048 : w == 7 ? (lane < 44 ? (lane >> 1) * 3072 + (lane & 1) * 128 : OOB) : lane < (slot < 20 ? 22 : 10) ? lane * 128 : OOB),
                   "s"(w == 0 ? srd(w2, 1376 * 4096) : w == 1 ? srd(wo, 512 * 1024) : w == 2 ? srd(out, 2048 * 1024) : w == 3 ? srd(b1, 2752) : w == 4 ? srd(b2, 4096) : w == 7 ? srd(wqkv, 512 * 3072) : rws),
                   "s"(w < 6 ? 0 : w == 7 ? ((xcd >> 1) * 6 + slot - 16) * (22 * 3072) + (xcd & 1) * 256 : WS_F + xcd * FRS + (slot < 20 ? 256 + ((xcd & 1) * 4 + slot - 16) * 32 : 512 + ((xcd & 6) + slot - 20) * 16) * 128) : "memory");
      w_issue<0>(s1, 1);
      tag = tag_wait<4>(ev);
      asm volatile("" ::"v"(tl));
      bar();
    }
    const WA a0 = w_addr(wa0);
    if (w == 0) st32c(tag, lane < 8 ? lane * FRS : OOB, rws, WS_F + (slot < 16 ? gi : gi + 80) * 128);
    w_round<0, 0, 2>(s1, a0, wr); w_issue<0>(s1, 3);
    vmbar0<8>(w);
    w_round<0, 1, 4>(s1, a0, wr);
    vmbar<8>();
    w_round<0, 2, 5>(s1, a0, wr);
    vmbar<8>();
    const u32 hfl = hf ? WS_F + xcd * FRS + (g * 32 + (lane & 31)) * 128 : OOB;
    u32 pf; u32x2 bb0, bb1;
    ld32c(pf, hfl, rws, 0);
    asm volatile("buffer_load_dwordx2 %0, %2, %3, %4 offen\n\tbuffer_load_dwordx2 %1, %2, %3, %4 offen offset:16" : "=&v"(bb0), "=&v"(bb1) : "v"(hh * 8), "s"(srd(b1, 2752)), "s"(ft * 128 + g * 32) : "memory");
    w_round<0, 3, 6>(s1, a0, wr); w_issue<0>(s1, 7);
    asm volatile("s_waitcnt vmcnt(8)" : "+v"(pf), "+v"(bb0), "+v"(bb1)::"memory");
    if (hf && __builtin_amdgcn_ballot_w64(pf != tag) != 0) poll(hfl, rws, tag);
    bar();
    f32x16 c0, c1;
    wr_gemm<0>(wr, a0, DSrc{rws, (u32)lane * 16, WS_H + (dt1 * 2 + hf) * 131072 + g * 32768, s1.rot}, s2, 0, c0, c1);
    const u32 R = L + 65536 + hf * 32768 + lane * 16;
    f32x4 lo = {blo(bb0[0]), bhi(bb0[0]), blo(bb0[1]), bhi(bb0[1])}, hi = {blo(bb1[0]), bhi(bb1[0]), blo(bb1[1]), bhi(bb1[1])};
#pragma unroll
    for (int q = 0; q < 4; ++q) {
      const f32x16& c = q < 2 ? c0 : c1; const int o = (q & 1) * 8;
      const f32x4 a = {c[o], c[o + 1], c[o + 2], c[o + 3]}, b = {c[o + 4], c[o + 5], c[o + 6], c[o + 7]};
      if (q == g) { lo += a; hi += b; }
      else { const u32 t = R + q * 8192 + ((g - q) & 3) * 2048; *(LAS f32x4*)t = a; *(LAS f32x4*)(t + 1024) = b; }
    }
    vmbar<0>();
#pragma unroll
    for (int k = 1; k < 4; ++k) { lo += ldsf4(R + g * 8192 + k * 2048); hi += ldsf4(R + g * 8192 + k * 2048 + 1024); }
    const u32x4 uv = {pk(gelu(lo[0]), gelu(lo[1])), pk(gelu(lo[2]), gelu(lo[3])), pk(gelu(hi[0]), gelu(hi[1])), pk(gelu(hi[2]), gelu(hi[3]))};
    st128c(uv, lane * 16, rws, WS_U + (dt1 * 2 + hf) * UDB + (ft * 4 + g) * 1024);
    w_stage(a0, wr, 0, 12);
    bar();
    w_flag<3>(s2, w, tag, lane < 4 ? (lane * 2 + (dt1 >> 2)) * FRS : OOB, rws, WS_F + (256 + dt1 * 32 + ft) * 128);
  }

  {
    asm volatile("" : "+s"(s2.lds), "+s"(s2.rot), "+s"(s2.sbase));
    const int sr = w * 8 + (lane >> 3), cq = lane & 7;
    u32 b2v;
    asm volatile("buffer_load_ushort %0, %1, %2, %3 offen" : "=v"(b2v) : "v"(sr * 2), "s"(srd(b2, 4096)), "s"(stl * 128) : "memory");
    const u32 GT = L + 131072, SG = GT + 256;
    const WA a0 = w_addr(wa0);
    f32x16 c0, c1;
    const int nf = g == 2 ? 6 : 8;
    const u32 fo = WS_F + xcd * FRS + ((g < 3 ? 256 + dt * 32 + g * 8 : 512 + (stl >> 2) * 16) + (lane < nf ? lane : 0)) * 128;
    w_issue<1>(s2, 6);
    w_issue<1>(s2, 7);
    vmbar0<17>(w);
    w_round<1, 3, 8>(s2, a0, wr);
    bar();
    *(volatile LAS u32*)(SG + w * 4) = 0;
    poll_bar(fo, rws, tag);
    asm volatile("" : "+v"(b2v));
    u32 gv;
    ld32c(gv, w == 0 ? lane * 4 : OOB, rws, WS_G + stl * 256);
    const u32 ub = WS_U + (dt * 2 + hf) * UDB;
    wr_gemm<1>(wr, a0, DSrc{srd(ws, g < 3 ? ub + 86 * 1024 : WS_END), (u32)lane * 16, g < 3 ? ub + g * 32768 : WS_O + (stl * 2 + hf) * 32768, s2.rot}, s2, SG + w * 4, c0, c1);
    asm volatile("" : "+v"(gv));
    if (w == 0) *(LAS u32*)(GT + lane * 4) = gv;
    for (int it = 0; it < SPIN_MAX * 16 && *(volatile LAS u32*)(SG + (w ^ 1) * 4) == 0; ++it) {}
    const u32 pb = L + g * 8192 + hf * 32768;
    if (g < 3) {
      asm volatile("s_mov_b32 m0, %0\n\ts_nop 15\n\ts_nop 7" ::"s"(pb + hf * 128) : "memory");
      put16<0, 0>(c0);
      asm volatile("s_mov_b32 m0, %0\n\ts_nop 0" ::"s"(pb + hf * 128 + 6144) : "memory");
      put16<65536 - 6144, 0>(c1);
    } else red_write<RSL>(pb + lane * 16, 65536, c0, c1);
    bar();
    const int sl = lane >> 3, i = (sl & 3) + 4 * (w & 3);
    const u32 ya = L + (cq >> 2) * (32768 + 128) + (w >> 2) * 65536 + (i >> 1) * 768 + (i & 1) * 272 + (sl >> 2) * 128 + (cq & 3) * 32;
    const f32x4 y0 = (ldsf4(ya) + ldsf4(ya + 8192)) + ldsf4(ya + 16384), y1 = (ldsf4(ya + 16) + ldsf4(ya + 8208)) + ldsf4(ya + 16400);
    const u32 aa = L + 24576 + (w >> 2) * 32768 + (cq >> 2) * 65536 + (cq & 3) * RSL + (sr & 31) * 16;
    const f32x4 a0v = ldsf4(aa), a1v = ldsf4(aa + 512);
    const float gt = ldsf(GT + sr * 4), bb = blo(b2v), og = 1.f - gt;
    u32x4 ov;
    ov[0] = pk(gt * a0v[0] + og * (y0[0] + bb), gt * a0v[1] + og * (y0[1] + bb));
    ov[1] = pk(gt * a0v[2] + og * (y0[2] + bb), gt * a0v[3] + og * (y0[3] + bb));
    ov[2] = pk(gt * a1v[0] + og * (y1[0] + bb), gt * a1v[1] + og * (y1[1] + bb));
    ov[3] = pk(gt * a1v[2] + og * (y1[2] + bb), gt * a1v[3] + og * (y1[3] + bb));
    asm volatile("buffer_store_dwordx4 %0, %1, %2, %3 offen nt" ::"v"(ov), "v"(sr * 1024 + cq * 16), "s"(srd(out, 2048 * 1024)), "s"(stl * 65536 + dt * 128) : "memory");
    asm volatile(".Lfk_end:");
  }
}

extern "C" void kernel_launch(void* const* d_in, const int* in_sizes, int n_in, void* d_out, int out_size, void* d_ws, size_t ws_size, hipStream_t stream) {
  fused_kernel<<<256, 512, 0, stream>>>((const char*)d_in[0], (const char*)d_in[1], (const char*)d_in[2], (const char*)d_in[4], (const char*)d_in[5],
                                        (const char*)d_in[6], (const char*)d_in[7], (const char*)d_in[8], (char*)d_out, (char*)d_ws);
}
```

```cpp
#include <hip/hip_runtime.h>
#include <hip/hip_bf16.h>


#pragma clang diagnostic ignored "-Wint-to-pointer-cast"
#define DI __device__ __forceinline__
#define LAS __attribute__((address_space(3)))
#define SB() __builtin_amdgcn_sched_barrier(0)
typedef unsigned u32;
typedef u32 u32x4 __attribute__((ext_vector_type(4)));
typedef u32 u32x2 __attribute__((ext_vector_type(2)));
typedef float f32x2 __attribute__((ext_vector_type(2)));
typedef float f32x4 __attribute__((ext_vector_type(4)));
typedef float f32x16 __attribute__((ext_vector_type(16)));
typedef __bf16 bf16x8 __attribute__((ext_vector_type(8)));
typedef __bf16 bf16x2 __attribute__((ext_vector_type(2)));
typedef short s16x4 __attribute__((ext_vector_type(4)));

constexpr u32 WS_E = 0, WS_G = 32768;
constexpr u32 WS_F = 7u << 20, FRS = 641 * 128;
constexpr u32 WS_KV = 65536, WS_Z = WS_KV + 262144, WS_H = 1u << 20, WS_U = 3u << 20, WS_O = 5u << 20, WS_P = 8u << 20, WS_END = 9u << 20, OOB = 1u << 30;
constexpr u32 UDB = 88 * 1024;
constexpr int SPIN_MAX = 20000;
constexpr int WIN = 10;

DI u32x4 srd(const void* p, u32 bytes) { const unsigned long long a = (unsigned long long)p; return u32x4{(u32)a, (u32)(a >> 32), bytes, 0x00020000u}; }
DI f32x16 mfma(u32x4 a, u32x4 b, f32x16 c) { return __builtin_amdgcn_mfma_f32_32x32x16_bf16(__builtin_bit_cast(bf16x8, a), __builtin_bit_cast(bf16x8, b), c, 0, 0, 0); }
DI f32x4 mfma16(u32x4 a, u32x4 b, f32x4 c) { return __builtin_amdgcn_mfma_f32_16x16x32_bf16(__builtin_bit_cast(bf16x8, a), __builtin_bit_cast(bf16x8, b), c, 0, 0, 0); }
DI u32x2 trd(u32 a) { return __builtin_bit_cast(u32x2, __builtin_amdgcn_ds_read_tr16_b64_v4i16((LAS s16x4*)a)); }
DI u32x4 trd2(u32 a, u32 b) { const u32x2 x = trd(a), y = trd(b); return u32x4{x[0], x[1], y[0], y[1]}; }
DI u32x4 lds128(u32 a) { return *(LAS u32x4*)a; }
DI f32x4 ldsf4(u32 a) { return *(LAS f32x4*)a; }
DI float ldsf(u32 a) { return *(LAS float*)a; }
DI u32 pk(float a, float b) { return __builtin_bit_cast(u32, __builtin_convertvector(f32x2{a, b}, bf16x2)); }
DI float blo(u32 v) { return __uint_as_float(v << 16); }
DI float bhi(u32 v) { return __uint_as_float(v & 0xffff0000u); }
DI u32x4 pack8(const f32x16& x, int o) { return u32x4{pk(x[o], x[o + 1]), pk(x[o + 2], x[o + 3]), pk(x[o + 4], x[o + 5]), pk(x[o + 6], x[o + 7])}; }
DI f32x16 splat(float v) { return f32x16{v, v, v, v, v, v, v, v, v, v, v, v, v, v, v, v}; }
DI float add1(float a, float b) { float r; asm("v_add_f32 %0, %1, %2" : "=v"(r) : "v"(a), "v"(b)); return r; }
DI f32x4 add4(f32x4 a, f32x4 b) { return f32x4{add1(a[0], b[0]), add1(a[1], b[1]), add1(a[2], b[2]), add1(a[3], b[3])}; }
DI u32 umin(u32 a, u32 b) { return a < b ? a : b; }
DI u32 umax(u32 a, u32 b) { return a > b ? a : b; }

DI void glds(u32 lds, u32 voff, u32x4 rs, u32 soff) {
  asm volatile("s_mov_b32 m0, %0\n\ts_nop 0\n\tbuffer_load_dwordx4 %1, %2, %3 offen lds" ::"s"(lds), "v"(voff), "s"(rs), "s"(soff) : "memory");
}
template <int OFF> DI void ld128c(u32x4& d, u32 voff, u32x4 rs, u32 soff) {
  asm volatile("buffer_load_dwordx4 %0, %1, %2, %3 offen offset:%4 sc1" : "=v"(d) : "v"(voff), "s"(rs), "s"(soff), "i"(OFF) : "memory");
}
DI void ld128(u32x4& d, u32 voff, u32x4 rs, u32 soff) { asm volatile("buffer_load_dwordx4 %0, %1, %2, %3 offen" : "=v"(d) : "v"(voff), "s"(rs), "s"(soff) : "memory"); }
template <int OFF = 0> DI void ld32c(u32& d, u32 voff, u32x4 rs, u32 soff) { asm volatile("buffer_load_dword %0, %1, %2, %3 offen offset:%4 sc1" : "=v"(d) : "v"(voff), "s"(rs), "s"(soff), "i"(OFF) : "memory"); }
DI void st128c(u32x4 v, u32 voff, u32x4 rs, u32 soff) { asm volatile("buffer_store_dwordx4 %0, %1, %2, %3 offen sc1\n\ts_nop 1" ::"v"(v), "v"(voff), "s"(rs), "s"(soff) : "memory"); }
DI void st32c(u32 v, u32 voff, u32x4 rs, u32 soff) { asm volatile("buffer_store_dword %0, %1, %2, %3 offen sc1" ::"v"(v), "v"(voff), "s"(rs), "s"(soff) : "memory"); }
template <int N> DI void vmwait() { asm volatile("s_waitcnt vmcnt(%0)" ::"i"(N) : "memory"); }
template <int N> DI void vmwait(u32x4& a) { asm volatile("s_waitcnt vmcnt(%1)" : "+v"(a) : "i"(N) : "memory"); }
DI void bar() { asm volatile("s_waitcnt lgkmcnt(0)\n\ts_barrier" ::: "memory"); }
template <int N> DI void vmbar() { asm volatile("s_waitcnt vmcnt(%0) lgkmcnt(0)\n\ts_barrier" ::"i"(N) : "memory"); }

DI void ev_load(u32& e, u32x4 ws) { asm volatile("buffer_load_dword %0, off, %1, 0" : "+v"(e) : "s"(ws) : "memory"); }
template <int N> DI u32 tag_wait(u32 e) {
  u32 t;
  asm volatile("s_waitcnt vmcnt(%2)\n\tv_readfirstlane_b32 %0, %1\n\ts_nop 1" : "=s"(t) : "v"(e), "i"(N) : "memory");
  return t * 1664525u + 1013904223u;
}
DI void poll(u32 voff, u32x4 ws, u32 tag) {
  for (int it = 0; it < SPIN_MAX; ++it) {
    u32 f;
    asm volatile("buffer_load_dword %0, %1, %2, 0 offen sc1\n\ts_waitcnt vmcnt(0)" : "=&v"(f) : "v"(voff), "s"(ws) : "memory");
    if (__builtin_amdgcn_ballot_w64(f != tag) == 0) break;
    __builtin_amdgcn_s_sleep(4);
  }
}
DI void poll_bar(u32 voff, u32x4 ws, u32 tag) {
  u32 f;
  asm volatile("buffer_load_dword %0, %1, %2, 0 offen sc1\n\ts_waitcnt vmcnt(0) lgkmcnt(0)\n\ts_barrier" : "=&v"(f) : "v"(voff), "s"(ws) : "memory");
  if (__builtin_amdgcn_ballot_w64(f != tag) != 0) poll(voff, ws, tag);
}
template <int C> DI float dppadd(float v) { return v + __int_as_float(__builtin_amdgcn_update_dpp(0, __float_as_int(v), C, 0xf, 0xf, true)); }
DI float rowsum(float v) { v = dppadd<0xB1>(v); v = dppadd<0x4E>(v); v = dppadd<0x141>(v); return dppadd<0x140>(v); }
DI float rdl(float v, int l) { return __int_as_float(__builtin_amdgcn_readlane(__float_as_int(v), l)); }
DI float fold32(float a, float b) { const auto r = __builtin_amdgcn_permlane32_swap(__float_as_uint(a), __float_as_uint(b), false, false); return __uint_as_float(r[0]) + __uint_as_float(r[1]); }
DI float fold16(float a, float b) { const auto r = __builtin_amdgcn_permlane16_swap(__float_as_uint(a), __float_as_uint(b), false, false); return __uint_as_float(r[0]) + __uint_as_float(r[1]); }
template <bool E> DI u32 phi2(float a, float b) {
  constexpr float l2e = 1.4426950408889634f, c = -l2e * (E ? 1.000001f : 1.f);
  float e0 = __builtin_amdgcn_fmed3f(__builtin_amdgcn_exp2f(__builtin_fmaf(a, l2e, c)), 0.f, 1.f), e1 = __builtin_amdgcn_fmed3f(__builtin_amdgcn_exp2f(__builtin_fmaf(b, l2e, c)), 0.f, 1.f);
  if (E) { e0 += 1e-6f; asm("" : "+v"(e0)); e1 += 1e-6f; }
  return pk(__builtin_amdgcn_fmed3f(a, e0, 3.4028234664e38f), __builtin_amdgcn_fmed3f(b, e1, 3.4028234664e38f));
}
template <bool E> DI u32x4 phi8(const f32x16& x, int o) { return u32x4{phi2<E>(x[o], x[o + 1]), phi2<E>(x[o + 2], x[o + 3]), phi2<E>(x[o + 4], x[o + 5]), phi2<E>(x[o + 6], x[o + 7])}; }
DI float gelu(float v) {
  const float t = v * (-2.3022081981f - 0.1029432397f * v * v);
  return v * __builtin_amdgcn_rcpf(1.f + __builtin_amdgcn_exp2f(t));
}

constexpr int wslot(int P, int c) { return (c + (P && c < 3 ? 4 : 1)) % 5; }
struct WSrc { u32x4 rs; u32 voff, sbase, stride, lds, rot; };
template <int P> DI void w_one(const WSrc& s, int c, int i) { glds(s.lds + wslot(P, c) * 32768 + i * 1024, s.voff, s.rs, s.sbase + (64 * ((c + s.rot) & 7) + 8 * i) * s.stride); }
template <int P> DI void w_issue(const WSrc& s, int c) {
#pragma unroll
  for (int i = 0; i < 4; ++i) w_one<P>(s, c, i);
}
struct WA { u32 a[3][2]; };
DI WA w_addr(u32 a0) { WA r;
#pragma unroll
  for (int i = 0; i < 3; ++i) { r.a[i][0] = a0 + i * 65536; r.a[i][1] = (a0 ^ 64) + i * 65536; asm volatile("" : "+v"(r.a[i][0]), "+v"(r.a[i][1])); }
  return r; }
template <int P> DI void w_frag(const WA& r, u32x4 (&f)[2], int c, int ks) {
#pragma unroll
  for (int b = 0; b < 2; ++b) { const u32 a = r.a[wslot(P, c) >> 1][b] + (wslot(P, c) & 1) * 32768 + ks * 2048; f[b] = trd2(a, a + 1024); }
}
template <int P, int C, int F> DI void w_round(const WSrc& s, const WA& a0, u32x4 (&wr)[16][2]) {
#pragma unroll
  for (int k = 0; k < 4; ++k) { w_frag<P>(a0, wr[4 * C + k], C, k); SB(); if constexpr (F < 8) w_one<P>(s, F, k); SB(); }
}
DI void w_stage(const WA& a0, u32x4 (&wr)[16][2], int k0, int k1) {
#pragma unroll
  for (int k = k0; k < k1; ++k) w_frag<1>(a0, wr[k], k >> 2, k & 3);
}
template <int C0> DI void w_flag(const WSrc& s, int w, u32 tag, u32 fv, u32x4 rws, u32 flag) {
  if constexpr (C0 < 6) w_issue<1>(s, C0);
  vmbar<(C0 < 6 ? 4 : 0)>();
  if (w == 0) st32c(tag, fv, rws, flag);
#pragma unroll
  for (int c = C0 + 1; c < 6; ++c) w_issue<1>(s, c);
}
template <int N> DI void vmbar0(int w) { if (w == 0) vmwait<N + 1>(); else vmwait<N>(); bar(); }
struct DSrc { u32x4 rs; u32 voff, sb, rot; };
template <int I> DI void d_issue(u32x4 (&d)[32], const DSrc& s) { ld128c<(I & 3) * 1024>(d[I], s.voff, s.rs, s.sb + (((I >> 2) + s.rot) & 7) * 4096); }
constexpr int KB = 32 - WIN;
template <int P, int K> DI void wr_steps(u32x4 (&d)[32], u32x4 (&wr)[16][2], const WA& a0, const DSrc& s, const WSrc& nx, u32 sig, f32x16& c0, f32x16& c1) {
  if constexpr (K < 32) {
    if constexpr (K == KB) {
      if constexpr (P == 0) bar();
      else { asm volatile("s_waitcnt lgkmcnt(0)" ::: "memory"); *(volatile LAS u32*)sig = 1; }
    }
    if constexpr (K % 8 == 0) __builtin_amdgcn_s_setprio(3 - K / 8);
    vmwait<(P == 0 || K < KB ? WIN - 1 : 31 - K)>(d[K]);
    c0 = mfma(wr[K & 15][0], d[K], c0); c1 = mfma(wr[K & 15][1], d[K], c1);
    asm volatile("" : "+v"(c0), "+v"(c1));
    if constexpr (K + WIN < 32) d_issue<K + WIN>(d, s);
    if constexpr (P == 0 && K == 1) bar();
    if constexpr (P == 0 && K >= KB) w_one<1>(nx, (K - KB) >> 2, (K - KB) & 3);
    if constexpr (K >= 1 && K <= 16) w_frag<P>(a0, wr[K - 1], 4 + ((K - 1) >> 2), (K - 1) & 3);
    wr_steps<P, K + 1>(d, wr, a0, s, nx, sig, c0, c1);
  }
}
template <int I> DI void d_first(u32x4 (&d)[32], const DSrc& s) { if constexpr (I < WIN) { d_issue<I>(d, s); d_first<I + 1>(d, s); } }
template <int P> DI void wr_gemm(u32x4 (&wr)[16][2], const WA& a0, const DSrc& s, const WSrc& nx, u32 sig, f32x16& c0, f32x16& c1) {
  u32x4 d[32];
  d_first<0>(d, s);
  c0 = splat(0.f); c1 = splat(0.f);
  wr_steps<P, 0>(d, wr, a0, s, nx, sig, c0, c1);
  if constexpr (P == 0) {
#pragma unroll
    for (int i = WIN; i < 12; ++i) w_one<1>(nx, i >> 2, i & 3);
  }
}
constexpr u32 RSL = 1088;
template <u32 RS> DI void red_write(u32 a, u32 bs, const f32x16& c0, const f32x16& c1) {
#pragma unroll
  for (int q = 0; q < 4; ++q) {
    *(LAS f32x4*)(a + q * RS) = f32x4{c0[4 * q], c0[4 * q + 1], c0[4 * q + 2], c0[4 * q + 3]};
    *(LAS f32x4*)(a + bs + q * RS) = f32x4{c1[4 * q], c1[4 * q + 1], c1[4 * q + 2], c1[4 * q + 3]};
  }
}

template <int O, int I> DI void put16(const f32x16& c) {
  if constexpr (I < 16) { asm volatile("ds_write_addtid_b32 %0 offset:%1" ::"v"(c[I]), "i"(O + (I >> 1) * 768 + (I & 1) * 272) : "memory"); put16<O, I + 1>(c); }
}

constexpr u32 AST = 49152;
struct ASrc { u32x4 rx, rw; u32 vx0, vx1, vw, vq, sx, s0, s1, sq, rot, L; int w; };
DI void a_one(const ASrc& s, int c, int j) {
  const u32 st = s.L + (c % 3) * AST, cc = (c + s.rot) & 7;
  if (j < 4) glds(st + s.w * 4096 + j * 1024, (j & 1) ? s.vx1 : s.vx0, s.rx, s.sx + j * 8192 + cc * 128);
  else glds(st + 32768 + (j - 4) * 8192 + s.w * 1024, s.vw, s.rw, (j == 4 ? s.s0 : s.s1) + cc * (64 * 3072));
}
template <bool KV, int C, int J> DI void a_pref(const ASrc& s, const WSrc& nx) {
  if constexpr (J < 0) {}
  else if constexpr (C < 8) a_one(s, C, J);
  else if constexpr (C == 8) { if constexpr (KV && J < 4) glds(s.L + 98304 + s.w * 4096 + J * 1024, s.vq, s.rw, s.sq + J * (16 * 3072)); }
  else w_one<1>(nx, (6 * (C - 9) + J) >> 2, (6 * (C - 9) + J) & 3);
}
constexpr int a_gap(int C, int g) {
  const int t0[6] = {5, 6, 9, 10, 13, 14}, t1[6] = {1, 3, 6, 9, 11, 14}, t2[6] = {1, 2, -1, -1, -1, -1};
  for (int j = 0; j < 6; ++j) if ((C == 2 ? t0[j] : C == 10 ? t2[j] : t1[j]) == g) return j;
  return -1;
}
struct AMisc { u32x4 rws; u32 cfoff, tag, rdy, clr; int lane; };
struct AAd { u32 x0[4], x1[4], wa; };
template <bool KV, int T> DI void a_steps(const ASrc& s, const WSrc& nx, AAd& ad, u32x4 (&x0)[33], u32x4 (&f)[2][3], f32x16 (&c)[4], u32& cf, const AMisc& ms) {
  if constexpr (T < 32) {
    constexpr int C = T >> 2, K = T & 3, P = T & 1, N = (T + 1) & 3;
    if constexpr (K == 3) {
      if constexpr (C < 6) vmbar<6 + ((KV && C == 1) || C == 3)>(); else if constexpr (C == 6) vmbar<KV ? 5 : 0>(); else vmbar<6>();
      if constexpr (C < 7) {
        constexpr u32 dl = (C + 1) % 3 ? AST : 0u - 2 * AST;
#pragma unroll
        for (int k = 0; k < 4; ++k) { ad.x0[k] += dl; ad.x1[k] += dl; }
        ad.wa += dl;
        asm volatile("" : "+v"(ad.x0[0]), "+v"(ad.x0[1]), "+v"(ad.x0[2]), "+v"(ad.x0[3]), "+v"(ad.x1[0]), "+v"(ad.x1[1]), "+v"(ad.x1[2]), "+v"(ad.x1[3]), "+v"(ad.wa));
      }
      if constexpr (C == 0 && KV) { if (s.w < 4) st128c(u32x4{~0u, ~0u, ~0u, ~0u}, ms.lane * 16, ms.rws, ms.clr); else st32c(~0u, (ms.lane & 31) * 4, ms.rws, ms.clr); }
      if constexpr (C == 2) { st32c(ms.tag, ms.rdy, ms.rws, 0); }
      if constexpr (C == 5 && KV) ld32c(cf, ms.cfoff, ms.rws, 0);
    }
    constexpr int PC = K == 3 ? C + 3 : C + 2, G0 = 4 * N;
    __builtin_amdgcn_s_setprio(3 - N);
    if constexpr (T < 31) { x0[T + 1] = lds128(ad.x0[N]); f[P ^ 1][1] = trd2(ad.wa + N * 2048, ad.wa + N * 2048 + 512); }
    SB();
    if constexpr (KV) c[0] = mfma(x0[T], f[P][1], c[0]); else c[0] = mfma(f[P][1], x0[T], c[0]);
    SB();
    if constexpr (T < 31) { f[P ^ 1][0] = lds128(ad.x1[N]); f[P ^ 1][2] = trd2(ad.wa + 8192 + N * 2048, ad.wa + 8192 + N * 2048 + 512); }
    a_pref<KV, PC, a_gap(PC, G0)>(s, nx);
    SB();
    if constexpr (KV) c[1] = mfma(f[P][0], f[P][1], c[1]); else c[1] = mfma(f[P][1], f[P][0], c[1]);
    SB();
    a_pref<KV, PC, a_gap(PC, G0 + 1)>(s, nx);
    SB();
    if constexpr (KV) c[2] = mfma(x0[T], f[P][2], c[2]); else c[2] = mfma(f[P][2], x0[T], c[2]);
    SB();
    a_pref<KV, PC, a_gap(PC, G0 + 2)>(s, nx);
    SB();
    if constexpr (KV) c[3] = mfma(f[P][0], f[P][2], c[3]); else c[3] = mfma(f[P][2], f[P][0], c[3]);
    SB();
    a_pref<KV, PC, a_gap(PC, G0 + 3)>(s, nx);
    SB();
    a_steps<KV, T + 1>(s, nx, ad, x0, f, c, cf, ms);
  }
}

DI ASrc a_src(const char* x, const char* wqkv, u32 L, int lane, int w, int xcd, int slot) {
  const bool kv = slot < 30;
  const int ai = slot - 22, rc = (xcd & 6) + (kv ? ai >> 2 : slot - 30), hg = (xcd & 1) * 4 + (ai & 3), qj = xcd & 1;
  ASrc sa;
  sa.rx = srd(x, 2048 * 1024); sa.rw = srd(wqkv, 512 * 3072); sa.L = L; sa.w = w;
  sa.rot = xcd & 6;
  sa.vx0 = (lane >> 3) * 1024 + ((lane & 7) ^ (lane >> 4)) * 16; sa.vx1 = sa.vx0 ^ 64;
  sa.vw = (lane >> 3) * 3072 + ((lane & 7) ^ ((lane >> 2) & 4)) * 16;
  sa.vq = (lane >> 2) * 3072 + (lane & 3) * 16;
  sa.sx = (rc * 256 + w * 32) * 1024;
  sa.s0 = w * 8 * 3072 + (kv ? 1024 + hg * 128 : 512 + qj * 256);
  sa.s1 = sa.s0 + (kv ? 1024 : 128);
  sa.sq = ((w + sa.rot) & 7) * (64 * 3072) + hg * 64;
  return sa;
}
DI WSrc w1_src(const char* w1, u32 lds, int lane, int w, int xcd, int slot, int& ft, int& dt1) {
  const int gj = slot < 16 ? (xcd >> 1) * 16 + slot : 48 + (xcd >> 1) * 6 + slot;
  const u32 wch = (lane & 7) ^ ((lane >> 2) & 4);
  ft = gj >> 2; dt1 = (xcd & 1) * 4 + (gj & 3);
  WSrc s1;
  s1.rs = srd(w1, 2048 * 2752); s1.stride = 2752; s1.lds = lds;
  s1.voff = (lane >> 3) * 2752 + (ft == 21 ? (wch & 3) : wch) * 16;
  s1.sbase = ((w >> 1) * 512 + (w & 1) * 32) * 2752 + ft * 128;
  s1.rot = (2 * (dt1 + ft)) & 7;
  return s1;
}
template <bool KV> DI u32 attn(u32 ev, const char* x, const char* wqkv, u32x4 rws, const WSrc& s2, u32 L, int lane, int w, int xcd, int slot, u32 wa0, u32x4 (&wr)[16][2]) {
  const int hh = lane >> 5, col = lane & 31, q4 = (lane & 15) >> 2, mi = w >> 1, ni = w & 1;
  const int ai = slot - 22, rc = (xcd & 6) + (KV ? ai >> 2 : slot - 30), hg = (xcd & 1) * 4 + (ai & 3), qj = xcd & 1;
  const ASrc sa = a_src(x, wqkv, L, lane, w, xcd, slot);
  AAd ad;
#pragma unroll
  for (int k = 0; k < 4; ++k) { ad.x0[k] = L + (mi * 64 + ni * 32 + col) * 128 + ((2 * k + hh) ^ ((col >> 1) & 7)) * 16; ad.x1[k] = ad.x0[k] ^ 4096; }
  ad.wa = L + 32768 + (8 * hh + q4) * 128 + 64 * ((q4 >> 1) ^ ni) + 32 * ((lane >> 4) & 1) + 8 * (lane & 3);
  const int pr = hg + (mi & 1) * 8;
  const bool ga = w < 4;
  const u32 cfoff = ga ? WS_F + xcd * FRS + (128 + (pr >> 1) * 8 + (lane & 7)) * 128 : OOB;
  asm volatile("" : "+v"(ad.x0[0]), "+v"(ad.x0[1]), "+v"(ad.x0[2]), "+v"(ad.x0[3]), "+v"(ad.x1[0]), "+v"(ad.x1[1]), "+v"(ad.x1[2]), "+v"(ad.x1[3]), "+v"(ad.wa)::"memory");
  const u32 tag = tag_wait<6>(ev);
  bar();
  const AMisc ms = {rws, cfoff, tag, lane == 0 ? WS_F + w * FRS + (KV ? 128 + hg * 8 + rc : 192 + qj * 8 + rc) * 128 : OOB, w < 4 ? WS_KV + ((hg * 2 + (w >> 1)) * 8 + rc) * 2048 + (w & 1) * 1024 : WS_Z + ((hg * 2 + (w & 1)) * 8 + rc) * 128, lane};
  u32x4 x0[33], f[2][3]; f32x16 c[4]; u32 cf;
  c[0] = c[1] = splat(KV ? 1.f : 1.000001f); c[2] = c[3] = splat(KV ? 0.f : 1.000001f);
  x0[0] = lds128(ad.x0[0]); f[0][0] = lds128(ad.x1[0]); f[0][1] = trd2(ad.wa, ad.wa + 512); f[0][2] = trd2(ad.wa + 8192, ad.wa + 8192 + 512);
  a_steps<KV, 0>(sa, s2, ad, x0, f, c, cf, ms);
  __builtin_amdgcn_s_setprio(0);
  asm volatile("" : "+v"(c[0]), "+v"(c[1]), "+v"(c[2]), "+v"(c[3]));
  const u32 pfl = WS_F + xcd * FRS + (512 + rc * 16 + 8 + qj) * 128;
  if constexpr (!KV) {
#pragma unroll
    for (int j = 0; j < 4; ++j)
#pragma unroll
      for (int h = 0; h < 2; ++h) { st128c(phi8<true>(c[j], 8 * h), lane * 16 + qj * 8192 + ni * 2048, rws, WS_P + (rc * 8 + (w ^ (j & 1))) * 16384 + ((j & 2) * 2 + h) * 1024); SB(); }
    asm volatile("s_barrier" ::: "memory");
    w_issue<1>(s2, 2);
    vmbar<4>();
    if (w == 0) st32c(tag, lane == 0 ? 0 : OOB, rws, pfl);
    if (blockIdx.x == 255 && w == 0) {
#pragma unroll 1
      for (int i = 0; i < 4; ++i) poll(WS_F + 7 * FRS + (i * 64 + lane) * 128, rws, tag);
      st32c(tag, lane == 0 ? 0 : OOB, rws, WS_E);
    }
    vmbar<0>();
    w_stage(w_addr(wa0), wr, 0, 12);
    bar();
    w_flag<3>(s2, w, tag, OOB, rws, 0);
    return tag;
  }
  const u32 S = L + 65536, KT = S + 14336, ZT = KT + 2048;
  if (w >= 4) w_issue<1>(s2, 2);
  const u32 on = ((lane >> 4) ^ (lane >> 2)) & 1 ? 0 : 0x3f803f80u;
  const u32x4 cs = {on, on, on, on};
  f32x16 d1 = splat(0.f); f32x4 d2 = {64e-6f, 64e-6f, 64e-6f, 64e-6f}, d3 = {0.f, 0.f, 0.f, 0.f};
  u32x4 p[4];
#pragma unroll
  for (int i = 0; i < 4; ++i) {
    const f32x16& k = c[i >> 1]; const int o = 8 * (i & 1);
    p[i][0] = phi2<false>(k[o], k[o + 1]); SB();
    if (i) d2 = mfma16(cs, p[i - 1], d2);
    const u32x4 v = pack8(c[2 + (i >> 1)], o);
    p[i][1] = phi2<false>(k[o + 2], k[o + 3]); SB();
    d3 = mfma16(cs, v, d3);
    p[i][2] = phi2<false>(k[o + 4], k[o + 5]); p[i][3] = phi2<false>(k[o + 6], k[o + 7]); SB();
    d1 = mfma(p[i], v, d1);
    SB(); if (w < 4) w_one<1>(s2, 2, i); SB();
  }
  d2 = mfma16(cs, p[3], d2);
  const bool lo = col < 16;
  f32x4 k0 = {lo ? d1[0] : d1[8], lo ? d1[1] : d1[9], lo ? d1[2] : d1[10], lo ? d1[3] : d1[11]};
  f32x4 k1 = {lo ? d1[4] : d1[12], lo ? d1[5] : d1[13], lo ? d1[6] : d1[14], lo ? d1[7] : d1[15]};
  const float ev6 = d3[0] * 1e-6f;
  k0 = add4(k0, f32x4{ev6, ev6, ev6, ev6}); k1 = add4(k1, f32x4{ev6, ev6, ev6, ev6});
  const float zp = d2[0];
  if (w >= 2) { *(LAS f32x4*)(S + (w - 2) * 2048 + lane * 16) = k0; *(LAS f32x4*)(S + (w - 2) * 2048 + 1024 + lane * 16) = k1; *(LAS float*)(S + 12288 + (w - 2) * 256 + lane * 4) = zp; }
  vmbar<0>();
  if (w < 2) {
    const u32 a = S + w * 2048 + lane * 16, z = S + 12288 + w * 256 + lane * 4;
    k0 = add4(add4(k0, ldsf4(a)), add4(ldsf4(a + 4096), ldsf4(a + 8192))); k1 = add4(add4(k1, ldsf4(a + 1024)), add4(ldsf4(a + 5120), ldsf4(a + 9216)));
    const float t = add1(add1(zp, ldsf(z)), add1(ldsf(z + 512), ldsf(z + 1024)));
    u32x4 p0 = __builtin_bit_cast(u32x4, k0), p1 = __builtin_bit_cast(u32x4, k1);
#pragma unroll
    for (int i = 0; i < 4; ++i) { p0[i] = umin(p0[i], ~1u); p1[i] = umin(p1[i], ~1u); }
    const u32 o = (hg * 2 + w) * 8 + rc;
    st128c(p0, lane * 16, rws, WS_KV + o * 2048); st128c(p1, lane * 16, rws, WS_KV + o * 2048 + 1024); st32c(umin(__float_as_uint(t), ~1u), col * 4, rws, WS_Z + o * 128);
  }
  u32x4 pq[2][2], t[8]; u32x2 z; u32 pf;
  u32 qa = L + 98304 + (8 * hh + q4) * 64 + 32 * ((lane >> 4) & 1) + 8 * (lane & 3);
  asm volatile("" : "+v"(qa), "+v"(cf));
  u32x4 qf[4];
#pragma unroll
  for (int i = 0; i < 3; ++i) qf[i] = trd2(qa + i * 1024, qa + i * 1024 + 256);
  f32x16 cq = splat(1.000001f);
#pragma unroll
  for (int i = 0; i < 32; ++i) {
    SB();
    if (i < 29) qf[(i + 3) & 3] = trd2(qa + (i + 3) * 1024, qa + (i + 3) * 1024 + 256);
    SB();
    cq = mfma(qf[i & 3], x0[i], cq);
  }
  SB();
  auto sweep = [&]() {
#pragma unroll
    for (int r = 0; r < 8; ++r) ld128c<0>(t[r], lane * 16, rws, WS_KV + (pr * 8 + r) * 2048 + ni * 1024);
    asm volatile("buffer_load_dwordx2 %0, %1, %2, %3 offen sc1" : "=v"(z) : "v"((lane >> 3) * 128 + (lane & 7) * 8), "s"(rws), "s"(WS_Z + pr * 1024 + ni * 64) : "memory");
  };
  asm volatile("" : "=v"(t[0]), "=v"(t[1]), "=v"(t[2]), "=v"(t[3]), "=v"(t[4]), "=v"(t[5]), "=v"(t[6]), "=v"(t[7]), "=v"(z));
  if (ga && __builtin_amdgcn_ballot_w64(cf != tag) != 0) poll(cfoff, rws, tag);
  ld32c(pf, pfl, rws, 0);
  if (mi == 0) sweep();
  pq[0][0] = phi8<true>(cq, 0); pq[0][1] = phi8<true>(cq, 8);
  asm volatile("" : "+v"(pq[0][0]), "+v"(pq[0][1]));
  w_stage(w_addr(wa0), wr, 0, 12);
  SB();
  if (mi == 1) sweep();
  if (!ga) vmwait<0>();
  asm volatile("s_waitcnt vmcnt(9)" : "+v"(pf)::"memory");
  if (__builtin_amdgcn_ballot_w64(pf != tag) != 0) poll(pfl, rws, tag);
  ld128c<0>(pq[1][0], lane * 16, rws, WS_P + (rc * 8 + w) * 16384 + hg * 2048); ld128c<1024>(pq[1][1], lane * 16, rws, WS_P + (rc * 8 + w) * 16384 + hg * 2048);
#define GW(N) asm volatile("s_waitcnt vmcnt(%9)" : "+v"(t[0]), "+v"(t[1]), "+v"(t[2]), "+v"(t[3]), "+v"(t[4]), "+v"(t[5]), "+v"(t[6]), "+v"(t[7]), "+v"(z) : "i"(N) : "memory")
  GW(2);
  if (ga) {
    for (int it = 0; it < SPIN_MAX; ++it) {
      u32 m = umax(z[0], z[1]);
#pragma unroll
      for (int r = 0; r < 8; ++r) m = umax(umax(umax(m, t[r][0]), t[r][1]), umax(t[r][2], t[r][3]));
      if (__builtin_amdgcn_ballot_w64(m == ~0u) == 0) break;
      sweep();
      GW(0);
    }
    f32x4 a = __builtin_bit_cast(f32x4, t[0]);
#pragma unroll
    for (int r = 1; r < 8; ++r) a += __builtin_bit_cast(f32x4, t[r]);
    float zs = fold32(__uint_as_float(z[0]), __uint_as_float(z[1]));
    zs = dppadd<0x128>(fold16(zs, zs));
    const int pl = mi, zf = 2 * (lane & 7) + hh;
    *(LAS u32x2*)(KT + pl * 1024 + lane * 16 + ni * 8) = u32x2{pk(a[0], a[1]), pk(a[2], a[3])};
    *(LAS unsigned short*)(ZT + pl * 64 + ni * 32 + ((zf >> 2) & 1) * 16 + ((zf & 3) + 4 * (zf >> 3)) * 2) = (unsigned short)pk(zs, zs);
  }
  bar();
  f32x16 dd[2][2];
#pragma unroll
  for (int j = 0; j < 2; ++j) {
    if (j) asm volatile("s_waitcnt vmcnt(0)" : "+v"(pq[1][0]), "+v"(pq[1][1])::"memory");
#pragma unroll
    for (int h = 0; h < 2; ++h) dd[j][h] = mfma(lds128(((col < 16) == (h == 0)) ? KT + j * 1024 + lane * 16 : ZT + j * 64 + h * 32 + hh * 16), pq[j][h], splat(0.f));
  }
  SB();
  w_issue<1>(s2, 3);
  SB();
#pragma unroll
  for (int j = 0; j < 2; ++j)
#pragma unroll
    for (int h = 0; h < 2; ++h) {
      const int o = 8 * h;
      const f32x16& e = dd[j][h];
      const float rd = __builtin_amdgcn_rcpf(e[8 - o] + 1e-6f);
      const u32x4 ov = {pk(e[o] * rd, e[o + 1] * rd), pk(e[o + 2] * rd, e[o + 3] * rd), pk(e[o + 4] * rd, e[o + 5] * rd), pk(e[o + 6] * rd, e[o + 7] * rd)};
      st128c(ov, lane * 16, rws, WS_O + (rc * 8 + w) * 32768 + ((hg + 8 * j) * 2 + h) * 1024);
    }
  bar();
  w_flag<4>(s2, w, tag, lane < 2 ? ((rc & 6) + lane) * FRS : OOB, rws, WS_F + (512 + rc * 16 + hg) * 128);
  return tag;
}

__global__ __launch_bounds__(512, 2) void fused_kernel(const char* __restrict__ x, const char* __restrict__ wqkv, const char* __restrict__ wo,
                                                        const char* __restrict__ w1, const char* __restrict__ b1, const char* __restrict__ w2,
                                                        const char* __restrict__ b2, const char* __restrict__ gw, char* __restrict__ out, char* ws) {
  __shared__ __attribute__((aligned(1024))) char sm[163840];
  u32 ev;
  {
    unsigned long long pc; u32 cn;
    asm volatile("s_getpc_b64 %0\n.Lfk_pc:\n\ts_mov_b32 %1, .Lfk_end-.Lfk_pc" : "=s"(pc), "=s"(cn));
    const u32 vo = (blockIdx.x >> 3 & 15) == 0 ? threadIdx.x * 128 : OOB;
    asm volatile("buffer_load_dword %0, %1, %2, 0 offen" : "=v"(ev) : "v"(vo), "s"(srd((const void*)pc, cn)) : "memory");
    SB();
  }
  asm volatile("" : "+s"(x), "+s"(wqkv), "+s"(wo), "+s"(w1), "+s"(b1), "+s"(w2), "+s"(b2), "+s"(gw), "+s"(out), "+s"(ws));
  const u32 L = (u32)(size_t)(LAS char*)sm;
  const int lane = threadIdx.x & 63, w = __builtin_amdgcn_readfirstlane(threadIdx.x >> 6);
  int xcd = blockIdx.x & 7, slot = blockIdx.x >> 3;
  const int hh = lane >> 5;
  const u32x4 rws = srd(ws, WS_END);
  u32x4 xr[2], gr;
  asm volatile("s_nop 4" : "=v"(xr[0]), "=v"(xr[1]), "=v"(gr)::"memory");
  ev_load(ev, rws);
  if (slot < 22) {
    int ft, dt1;
    const WSrc s1 = w1_src(w1, L + w * 4096, lane, w, xcd, slot, ft, dt1);
    if (slot < 16) {
      const int r = (xcd * 16 + slot) * 16 + w;
      const u32x4 rx = srd(x, 2048 * 1024);
      ld128(xr[0], lane * 16, rx, r * 1024); ld128(xr[1], lane * 16, rx, (r + 8) * 1024);
      ld128(gr, lane * 16, srd(gw, 1024), 0);
      asm volatile("s_barrier" ::: "memory");
    }
    w_issue<0>(s1, 0);
  } else {
    const ASrc sa = a_src(x, wqkv, L, lane, w, xcd, slot);
#pragma unroll
    for (int j = 0; j < 6; ++j) a_one(sa, 0, j);
    asm volatile("s_barrier" ::: "memory");
#pragma unroll
    for (int j = 0; j < 6; ++j) a_one(sa, 1, j);
  }
  asm volatile("" : "+s"(slot), "+s"(xcd));
  const int g = w >> 1, hf = w & 1, q4 = (lane & 15) >> 2;
  const u32 wch = (lane & 7) ^ ((lane >> 2) & 4);
  const u32 wa0 = L + g * 8192 + (4 * hh + q4) * 128 + 64 * (q4 >> 1) + 32 * ((lane >> 4) & 1) + 8 * (lane & 3);

  const int dt = (xcd & 1) * 4 + (slot & 3), stl = (xcd >> 1) * 8 + (slot >> 2);
  WSrc s2;
  s2.rs = g < 3 ? srd(w2, 1376 * 4096) : srd(wo, 512 * 1024); s2.stride = g < 3 ? 4096 : 1024; s2.lds = L + g * 8192 + hf * 4096;
  s2.voff = (lane >> 3) * s2.stride + wch * 16;
  s2.sbase = g < 3 ? (g * 512 + hf * 32) * 4096 + stl * 128 : hf * 32 * 1024 + dt * 128;
  s2.rot = 2 * ((slot + (slot >> 2)) & 3) + (xcd & 1);
  u32 tag;
  u32x4 wr[16][2];

  if (slot >= 30) tag = attn<false>(ev, x, wqkv, rws, s2, L, lane, w, xcd, slot, wa0, wr);
  else if (slot >= 22) tag = attn<true>(ev, x, wqkv, rws, s2, L, lane, w, xcd, slot, wa0, wr);
  else {
    const int gi = slot < 16 ? xcd * 16 + slot : 112 + xcd * 6 + slot;
    int ft, dt1;
    const WSrc s1 = w1_src(w1, s2.lds, lane, w, xcd, slot, ft, dt1);
    if (slot < 16) {
      const int sb = xcd * 16 + slot;
      tag = tag_wait<4>(ev);
      asm volatile("" : "+v"(xr[0]), "+v"(xr[1]), "+v"(gr)::"memory");
      f32x2 v[2][4], t; float s[2], gd[2], q[2];
#pragma unroll
      for (int j = 0; j < 2; ++j) {
#pragma unroll
        for (int i = 0; i < 4; ++i) v[j][i] = f32x2{blo(xr[j][i]), bhi(xr[j][i])};
        t = (v[j][0] + v[j][1]) + (v[j][2] + v[j][3]); s[j] = t[0] + t[1];
      }
      const float t0 = fold32(s[0], s[1]), t1 = rowsum(fold16(t0, t0)) * (1.f / 512.f);
#pragma unroll
      for (int j = 0; j < 2; ++j) {
        t = f32x2{0.f, 0.f};
#pragma unroll
        for (int i = 0; i < 4; ++i) t = __builtin_elementwise_fma(v[j][i], f32x2{blo(gr[i]), bhi(gr[i])}, t);
        gd[j] = t[0] + t[1];
        const float m = rdl(t1, 32 * j);
        t = f32x2{0.f, 0.f};
#pragma unroll
        for (int i = 0; i < 4; ++i) { v[j][i] -= m; t = __builtin_elementwise_fma(v[j][i], v[j][i], t); }
        q[j] = t[0] + t[1];
      }
      const float t2 = rowsum(fold16(fold32(q[0], q[1]), fold32(gd[0], gd[1])));
      const float rs = __builtin_amdgcn_rsqf(t2 * (1.f / 512.f) + 1e-5f);
#pragma unroll
      for (int j = 0; j < 2; ++j) {
        const float r = rdl(rs, 32 * j);
#pragma unroll
        for (int i = 0; i < 4; ++i) v[j][i] *= r;
        *(LAS u32x4*)(L + (w + 8 * j) * 1088 + lane * 16) = u32x4{pk(v[j][0][0], v[j][0][1]), pk(v[j][1][0], v[j][1][1]), pk(v[j][2][0], v[j][2][1]), pk(v[j][3][0], v[j][3][1])};
      }
      const float gt = __builtin_amdgcn_rcpf(1.f + __builtin_amdgcn_exp2f(t2 * -1.4426950408889634f));
      bar();
      const u32 ta = L + (4 * hh + q4) * 1088 + w * 128 + 32 * ((lane >> 4) & 1) + 8 * (lane & 3);
      const u32x4 h0 = trd2(ta, ta + 8 * 1088), h1 = trd2(ta + 64, ta + 64 + 8 * 1088);
      st32c(__float_as_uint(gt), (lane & 31) == 16 ? lane & 32 : OOB, rws, WS_G + (sb * 16 + w) * 4);
      st128c(h0, lane * 16, rws, WS_H + w * 262144 + sb * 1024);
      st128c(h1, lane * 16, rws, WS_H + w * 262144 + 131072 + sb * 1024);
      asm volatile("s_barrier" ::: "memory");
      w_issue<0>(s1, 1);
      vmbar<4>();
    } else {
      u32 tl;
      const int k = slot - 16, nl = w == 7 ? 44 : w == 6 ? (k < 4 ? 22 : 10) : w == 3 || w == 4 ? 6 : 64;
      asm volatile("buffer_load_dword %0, %1, %2, %3 offen sc1" : "=v"(tl) : "v"(lane >= nl ? OOB : w == 7 ? (lane >> 1) * 3072 + (lane & 1) * 128 : lane << (nl == 64 ? 18 : 7)),
                   "s"(w == 0 ? srd(w2, 1376 * 4096) : w == 1 ? srd(wo, 512 * 1024) : w == 2 ? srd(out, 2048 * 1024) : w == 3 ? srd(b1, 2752) : w == 4 ? srd(b2, 4096) : w == 7 ? srd(wqkv, 512 * 3072) : rws),
                   "s"(w == 7 ? ((xcd >> 1) * 6 + k) * (22 * 3072) + (xcd & 1) * 256 : w == 6 ? WS_F + xcd * FRS + (k < 4 ? 256 + ((xcd & 1) * 4 + k) * 32 : 448 + ((xcd & 6) + k) * 16) * 128 : k * 768) : "memory");
      w_issue<0>(s1, 1);
      tag = tag_wait<4>(ev);
      asm volatile("" ::"v"(tl));
      bar();
    }
    const WA a0 = w_addr(wa0);
    if (w == 0) st32c(tag, lane < 8 ? lane * FRS : OOB, rws, WS_F + (slot < 16 ? gi : gi + 80) * 128);
    w_round<0, 0, 2>(s1, a0, wr); w_issue<0>(s1, 3);
    vmbar0<8>(w);
    w_round<0, 1, 4>(s1, a0, wr);
    vmbar<8>();
    w_round<0, 2, 5>(s1, a0, wr);
    vmbar<8>();
    const u32 hfl = hf ? WS_F + xcd * FRS + (g * 32 + (lane & 31)) * 128 : OOB;
    u32 pf; u32x2 bb0, bb1;
    ld32c(pf, hfl, rws, 0);
    asm volatile("buffer_load_dwordx2 %0, %2, %3, %4 offen\n\tbuffer_load_dwordx2 %1, %2, %3, %4 offen offset:16" : "=&v"(bb0), "=&v"(bb1) : "v"(hh * 8), "s"(srd(b1, 2752)), "s"(ft * 128 + g * 32) : "memory");
    w_round<0, 3, 6>(s1, a0, wr); w_issue<0>(s1, 7);
    asm volatile("s_waitcnt vmcnt(8)" : "+v"(pf), "+v"(bb0), "+v"(bb1)::"memory");
    if (hf && __builtin_amdgcn_ballot_w64(pf != tag) != 0) poll(hfl, rws, tag);
    bar();
    f32x16 c0, c1;
    wr_gemm<0>(wr, a0, DSrc{rws, (u32)lane * 16, WS_H + (dt1 * 2 + hf) * 131072 + g * 32768, s1.rot}, s2, 0, c0, c1);
    const u32 R = L + 65536 + hf * 32768 + lane * 16;
    f32x4 lo = {blo(bb0[0]), bhi(bb0[0]), blo(bb0[1]), bhi(bb0[1])}, hi = {blo(bb1[0]), bhi(bb1[0]), blo(bb1[1]), bhi(bb1[1])};
#pragma unroll
    for (int q = 0; q < 4; ++q) {
      const f32x16& c = q < 2 ? c0 : c1; const int o = (q & 1) * 8;
      const f32x4 a = {c[o], c[o + 1], c[o + 2], c[o + 3]}, b = {c[o + 4], c[o + 5], c[o + 6], c[o + 7]};
      if (q == g) { lo += a; hi += b; }
      else { const u32 t = R + q * 8192 + ((g - q) & 3) * 2048; *(LAS f32x4*)t = a; *(LAS f32x4*)(t + 1024) = b; }
    }
    vmbar<0>();
#pragma unroll
    for (int k = 1; k < 4; ++k) { lo += ldsf4(R + g * 8192 + k * 2048); hi += ldsf4(R + g * 8192 + k * 2048 + 1024); }
    const u32x4 uv = {pk(gelu(lo[0]), gelu(lo[1])), pk(gelu(lo[2]), gelu(lo[3])), pk(gelu(hi[0]), gelu(hi[1])), pk(gelu(hi[2]), gelu(hi[3]))};
    st128c(uv, lane * 16, rws, WS_U + (dt1 * 2 + hf) * UDB + (ft * 4 + g) * 1024);
    w_stage(a0, wr, 0, 12);
    bar();
    w_flag<3>(s2, w, tag, lane < 4 ? (lane * 2 + (dt1 >> 2)) * FRS : OOB, rws, WS_F + (256 + dt1 * 32 + ft) * 128);
  }

  {
    asm volatile("" : "+s"(s2.lds), "+s"(s2.rot), "+s"(s2.sbase));
    const int sr = w * 8 + (lane >> 3), cq = lane & 7;
    u32 b2v;
    asm volatile("buffer_load_ushort %0, %1, %2, %3 offen" : "=v"(b2v) : "v"(sr * 2), "s"(srd(b2, 4096)), "s"(stl * 128) : "memory");
    const u32 GT = L + 131072, SG = GT + 256;
    const WA a0 = w_addr(wa0);
    f32x16 c0, c1;
    const int nf = g == 2 ? 6 : 8;
    const u32 fo = WS_F + xcd * FRS + ((g < 3 ? 256 + dt * 32 + g * 8 : 512 + (stl >> 2) * 16) + (lane < nf ? lane : 0)) * 128;
    w_issue<1>(s2, 6);
    w_issue<1>(s2, 7);
    vmbar0<17>(w);
    w_round<1, 3, 8>(s2, a0, wr);
    bar();
    *(volatile LAS u32*)(SG + w * 4) = 0;
    poll_bar(fo, rws, tag);
    asm volatile("" : "+v"(b2v));
    u32 gv;
    ld32c(gv, w == 0 ? lane * 4 : OOB, rws, WS_G + stl * 256);
    const u32 ub = WS_U + (dt * 2 + hf) * UDB;
    wr_gemm<1>(wr, a0, DSrc{srd(ws, g < 3 ? ub + 86 * 1024 : WS_END), (u32)lane * 16, g < 3 ? ub + g * 32768 : WS_O + (stl * 2 + hf) * 32768, s2.rot}, s2, SG + w * 4, c0, c1);
    asm volatile("" : "+v"(gv));
    if (w == 0) *(LAS u32*)(GT + lane * 4) = gv;
    for (int it = 0; it < SPIN_MAX * 16 && *(volatile LAS u32*)(SG + (w ^ 1) * 4) == 0; ++it) {}
    const u32 pb = L + g * 8192 + hf * 32768;
    if (g < 3) {
      asm volatile("s_mov_b32 m0, %0\n\ts_nop 15\n\ts_nop 7" ::"s"(pb + hf * 128) : "memory");
      put16<0, 0>(c0);
      asm volatile("s_mov_b32 m0, %0\n\ts_nop 0" ::"s"(pb + hf * 128 + 6144) : "memory");
      put16<65536 - 6144, 0>(c1);
    } else red_write<RSL>(pb + lane * 16, 65536, c0, c1);
    bar();
    const int sl = lane >> 3, i = (sl & 3) + 4 * (w & 3);
    const u32 ya = L + (cq >> 2) * (32768 + 128) + (w >> 2) * 65536 + (i >> 1) * 768 + (i & 1) * 272 + (sl >> 2) * 128 + (cq & 3) * 32;
    const f32x4 y0 = (ldsf4(ya) + ldsf4(ya + 8192)) + ldsf4(ya + 16384), y1 = (ldsf4(ya + 16) + ldsf4(ya + 8208)) + ldsf4(ya + 16400);
    const u32 aa = L + 24576 + (w >> 2) * 32768 + (cq >> 2) * 65536 + (cq & 3) * RSL + (sr & 31) * 16;
    const f32x4 a0v = ldsf4(aa), a1v = ldsf4(aa + 512);
    const float gt = ldsf(GT + sr * 4), bb = blo(b2v), og = 1.f - gt;
    u32x4 ov;
    ov[0] = pk(gt * a0v[0] + og * (y0[0] + bb), gt * a0v[1] + og * (y0[1] + bb));
    ov[1] = pk(gt * a0v[2] + og * (y0[2] + bb), gt * a0v[3] + og * (y0[3] + bb));
    ov[2] = pk(gt * a1v[0] + og * (y1[0] + bb), gt * a1v[1] + og * (y1[1] + bb));
    ov[3] = pk(gt * a1v[2] + og * (y1[2] + bb), gt * a1v[3] + og * (y1[3] + bb));
    asm volatile("buffer_store_dwordx4 %0, %1, %2, %3 offen nt" ::"v"(ov), "v"(sr * 1024 + cq * 16), "s"(srd(out, 2048 * 1024)), "s"(stl * 65536 + dt * 128) : "memory");
    asm volatile(".Lfk_end:");
  }
}

extern "C" void kernel_launch(void* const* d_in, const int* in_sizes, int n_in, void* d_out, int out_size, void* d_ws, size_t ws_size, hipStream_t stream) {
  fused_kernel<<<256, 512, 0, stream>>>((const char*)d_in[0], (const char*)d_in[1], (const char*)d_in[2], (const char*)d_in[4], (const char*)d_in[5],
                                        (const char*)d_in[6], (const char*)d_in[7], (const char*)d_in[8], (char*)d_out, (char*)d_ws);
}
```

```cpp
#include <hip/hip_runtime.h>
#include <hip/hip_bf16.h>


#pragma clang diagnostic ignored "-Wint-to-pointer-cast"
#define DI __device__ __forceinline__
#define LAS __attribute__((address_space(3)))
#define SB() __builtin_amdgcn_sched_barrier(0)
typedef unsigned u32;
typedef u32 u32x4 __attribute__((ext_vector_type(4)));
typedef u32 u32x2 __attribute__((ext_vector_type(2)));
typedef float f32x2 __attribute__((ext_vector_type(2)));
typedef float f32x4 __attribute__((ext_vector_type(4)));
typedef float f32x16 __attribute__((ext_vector_type(16)));
typedef __bf16 bf16x8 __attribute__((ext_vector_type(8)));
typedef __bf16 bf16x2 __attribute__((ext_vector_type(2)));
typedef short s16x4 __attribute__((ext_vector_type(4)));

constexpr u32 WS_E = 0, WS_G = 32768;
constexpr u32 WS_F = 7u << 20, FRS = 641 * 128;
constexpr u32 WS_KV = 65536, WS_Z = WS_KV + 262144, WS_H = 1u << 20, WS_U = 3u << 20, WS_O = 5u << 20, WS_P = 8u << 20, WS_END = 9u << 20, OOB = 1u << 30;
constexpr u32 UDB = 88 * 1024;
constexpr int SPIN_MAX = 20000;
constexpr int WIN = 10;

DI u32x4 srd(const void* p, u32 bytes) { const unsigned long long a = (unsigned long long)p; return u32x4{(u32)a, (u32)(a >> 32), bytes, 0x00020000u}; }
DI f32x16 mfma(u32x4 a, u32x4 b, f32x16 c) { return __builtin_amdgcn_mfma_f32_32x32x16_bf16(__builtin_bit_cast(bf16x8, a), __builtin_bit_cast(bf16x8, b), c, 0, 0, 0); }
DI f32x4 mfma16(u32x4 a, u32x4 b, f32x4 c) { return __builtin_amdgcn_mfma_f32_16x16x32_bf16(__builtin_bit_cast(bf16x8, a), __builtin_bit_cast(bf16x8, b), c, 0, 0, 0); }
DI u32x2 trd(u32 a) { return __builtin_bit_cast(u32x2, __builtin_amdgcn_ds_read_tr16_b64_v4i16((LAS s16x4*)a)); }
DI u32x4 trd2(u32 a, u32 b) { const u32x2 x = trd(a), y = trd(b); return u32x4{x[0], x[1], y[0], y[1]}; }
DI u32x4 lds128(u32 a) { return *(LAS u32x4*)a; }
DI f32x4 ldsf4(u32 a) { return *(LAS f32x4*)a; }
DI float ldsf(u32 a) { return *(LAS float*)a; }
DI u32 pk(float a, float b) { return __builtin_bit_cast(u32, __builtin_convertvector(f32x2{a, b}, bf16x2)); }
DI float blo(u32 v) { return __uint_as_float(v << 16); }
DI float bhi(u32 v) { return __uint_as_float(v & 0xffff0000u); }
DI u32x4 pack8(const f32x16& x, int o) { return u32x4{pk(x[o], x[o + 1]), pk(x[o + 2], x[o + 3]), pk(x[o + 4], x[o + 5]), pk(x[o + 6], x[o + 7])}; }
DI f32x16 splat(float v) { return f32x16{v, v, v, v, v, v, v, v, v, v, v, v, v, v, v, v}; }
DI float add1(float a, float b) { float r; asm("v_add_f32 %0, %1, %2" : "=v"(r) : "v"(a), "v"(b)); return r; }
DI f32x4 add4(f32x4 a, f32x4 b) { return f32x4{add1(a[0], b[0]), add1(a[1], b[1]), add1(a[2], b[2]), add1(a[3], b[3])}; }
DI u32 umin(u32 a, u32 b) { return a < b ? a : b; }
DI u32 umax(u32 a, u32 b) { return a > b ? a : b; }

DI void glds(u32 lds, u32 voff, u32x4 rs, u32 soff) {
  asm volatile("s_mov_b32 m0, %0\n\ts_nop 0\n\tbuffer_load_dwordx4 %1, %2, %3 offen lds" ::"s"(lds), "v"(voff), "s"(rs), "s"(soff) : "memory");
}
template <int OFF> DI void ld128c(u32x4& d, u32 voff, u32x4 rs, u32 soff) {
  asm volatile("buffer_load_dwordx4 %0, %1, %2, %3 offen offset:%4 sc1" : "=v"(d) : "v"(voff), "s"(rs), "s"(soff), "i"(OFF) : "memory");
}
DI void ld128(u32x4& d, u32 voff, u32x4 rs, u32 soff) { asm volatile("buffer_load_dwordx4 %0, %1, %2, %3 offen" : "=v"(d) : "v"(voff), "s"(rs), "s"(soff) : "memory"); }
template <int OFF = 0> DI void ld32c(u32& d, u32 voff, u32x4 rs, u32 soff) { asm volatile("buffer_load_dword %0, %1, %2, %3 offen offset:%4 sc1" : "=v"(d) : "v"(voff), "s"(rs), "s"(soff), "i"(OFF) : "memory"); }
DI void st128c(u32x4 v, u32 voff, u32x4 rs, u32 soff) { asm volatile("buffer_store_dwordx4 %0, %1, %2, %3 offen sc1\n\ts_nop 1" ::"v"(v), "v"(voff), "s"(rs), "s"(soff) : "memory"); }
DI void st32c(u32 v, u32 voff, u32x4 rs, u32 soff) { asm volatile("buffer_store_dword %0, %1, %2, %3 offen sc1" ::"v"(v), "v"(voff), "s"(rs), "s"(soff) : "memory"); }
template <int N> DI void vmwait() { asm volatile("s_waitcnt vmcnt(%0)" ::"i"(N) : "memory"); }
template <int N> DI void vmwait(u32x4& a) { asm volatile("s_waitcnt vmcnt(%1)" : "+v"(a) : "i"(N) : "memory"); }
DI void bar() { asm volatile("s_waitcnt lgkmcnt(0)\n\ts_barrier" ::: "memory"); }
template <int N> DI void vmbar() { asm volatile("s_waitcnt vmcnt(%0) lgkmcnt(0)\n\ts_barrier" ::"i"(N) : "memory"); }

DI void ev_load(u32& e, u32x4 ws) { asm volatile("buffer_load_dword %0, off, %1, 0" : "+v"(e) : "s"(ws) : "memory"); }
template <int N> DI u32 tag_wait(u32 e) {
  u32 t;
  asm volatile("s_waitcnt vmcnt(%2)\n\tv_readfirstlane_b32 %0, %1\n\ts_nop 1" : "=s"(t) : "v"(e), "i"(N) : "memory");
  return t * 1664525u + 1013904223u;
}
DI void poll(u32 voff, u32x4 ws, u32 tag) {
  for (int it = 0; it < SPIN_MAX; ++it) {
    u32 f;
    asm volatile("buffer_load_dword %0, %1, %2, 0 offen sc1\n\ts_waitcnt vmcnt(0)" : "=&v"(f) : "v"(voff), "s"(ws) : "memory");
    if (__builtin_amdgcn_ballot_w64(f != tag) == 0) break;
    __builtin_amdgcn_s_sleep(4);
  }
}
DI void poll_bar(u32 voff, u32x4 ws, u32 tag) {
  u32 f;
  asm volatile("buffer_load_dword %0, %1, %2, 0 offen sc1\n\ts_waitcnt vmcnt(0) lgkmcnt(0)\n\ts_barrier" : "=&v"(f) : "v"(voff), "s"(ws) : "memory");
  if (__builtin_amdgcn_ballot_w64(f != tag) != 0) poll(voff, ws, tag);
}
template <int C> DI float dppadd(float v) { return v + __int_as_float(__builtin_amdgcn_update_dpp(0, __float_as_int(v), C, 0xf, 0xf, true)); }
DI float rowsum(float v) { v = dppadd<0xB1>(v); v = dppadd<0x4E>(v); v = dppadd<0x141>(v); return dppadd<0x140>(v); }
DI float rdl(float v, int l) { return __int_as_float(__builtin_amdgcn_readlane(__float_as_int(v), l)); }
DI float fold32(float a, float b) { const auto r = __builtin_amdgcn_permlane32_swap(__float_as_uint(a), __float_as_uint(b), false, false); return __uint_as_float(r[0]) + __uint_as_float(r[1]); }
DI float fold16(float a, float b) { const auto r = __builtin_amdgcn_permlane16_swap(__float_as_uint(a), __float_as_uint(b), false, false); return __uint_as_float(r[0]) + __uint_as_float(r[1]); }
template <bool E> DI u32 phi2(float a, float b) {
  constexpr float l2e = 1.4426950408889634f, c = -l2e * (E ? 1.000001f : 1.f);
  float e0 = __builtin_amdgcn_fmed3f(__builtin_amdgcn_exp2f(__builtin_fmaf(a, l2e, c)), 0.f, 1.f), e1 = __builtin_amdgcn_fmed3f(__builtin_amdgcn_exp2f(__builtin_fmaf(b, l2e, c)), 0.f, 1.f);
  if (E) { e0 += 1e-6f; asm("" : "+v"(e0)); e1 += 1e-6f; }
  return pk(__builtin_amdgcn_fmed3f(a, e0, 3.4028234664e38f), __builtin_amdgcn_fmed3f(b, e1, 3.4028234664e38f));
}
template <bool E> DI u32x4 phi8(const f32x16& x, int o) { return u32x4{phi2<E>(x[o], x[o + 1]), phi2<E>(x[o + 2], x[o + 3]), phi2<E>(x[o + 4], x[o + 5]), phi2<E>(x[o + 6], x[o + 7])}; }
DI float gelu(float v) {
  const float t = v * (-2.3022081981f - 0.1029432397f * v * v);
  return v * __builtin_amdgcn_rcpf(1.f + __builtin_amdgcn_exp2f(t));
}

constexpr int wslot(int P, int c) { return (c + (P && c < 3 ? 4 : 1)) % 5; }
struct WSrc { u32x4 rs; u32 voff, sbase, stride, lds, rot; };
template <int P> DI void w_one(const WSrc& s, int c, int i) { glds(s.lds + wslot(P, c) * 32768 + i * 1024, s.voff, s.rs, s.sbase + (64 * ((c + s.rot) & 7) + 8 * i) * s.stride); }
template <int P> DI void w_issue(const WSrc& s, int c) {
#pragma unroll
  for (int i = 0; i < 4; ++i) w_one<P>(s, c, i);
}
struct WA { u32 a[3][2]; };
DI WA w_addr(u32 a0) { WA r;
#pragma unroll
  for (int i = 0; i < 3; ++i) { r.a[i][0] = a0 + i * 65536; r.a[i][1] = (a0 ^ 64) + i * 65536; asm volatile("" : "+v"(r.a[i][0]), "+v"(r.a[i][1])); }
  return r; }
template <int P> DI void w_frag(const WA& r, u32x4 (&f)[2], int c, int ks) {
#pragma unroll
  for (int b = 0; b < 2; ++b) { const u32 a = r.a[wslot(P, c) >> 1][b] + (wslot(P, c) & 1) * 32768 + ks * 2048; f[b] = trd2(a, a + 1024); }
}
template <int P, int C, int F> DI void w_round(const WSrc& s, const WA& a0, u32x4 (&wr)[16][2]) {
#pragma unroll
  for (int k = 0; k < 4; ++k) { w_frag<P>(a0, wr[4 * C + k], C, k); SB(); if constexpr (F < 8) w_one<P>(s, F, k); SB(); }
}
DI void w_stage(const WA& a0, u32x4 (&wr)[16][2], int k0, int k1) {
#pragma unroll
  for (int k = k0; k < k1; ++k) w_frag<1>(a0, wr[k], k >> 2, k & 3);
}
template <int C0> DI void w_flag(const WSrc& s, int w, u32 tag, u32 fv, u32x4 rws, u32 flag) {
  if constexpr (C0 < 6) w_issue<1>(s, C0);
  vmbar<(C0 < 6 ? 4 : 0)>();
  if (w == 0) st32c(tag, fv, rws, flag);
#pragma unroll
  for (int c = C0 + 1; c < 6; ++c) w_issue<1>(s, c);
}
template <int N> DI void vmbar0(int w) { if (w == 0) vmwait<N + 1>(); else vmwait<N>(); bar(); }
struct DSrc { u32x4 rs; u32 voff, sb, rot; };
template <int I> DI void d_issue(u32x4 (&d)[32], const DSrc& s) { ld128c<(I & 3) * 1024>(d[I], s.voff, s.rs, s.sb + (((I >> 2) + s.rot) & 7) * 4096); }
constexpr int KB = 32 - WIN;
template <int P, int K> DI void wr_steps(u32x4 (&d)[32], u32x4 (&wr)[16][2], const WA& a0, const DSrc& s, const WSrc& nx, u32 sig, f32x16& c0, f32x16& c1) {
  if constexpr (K < 32) {
    if constexpr (K == KB) {
      if constexpr (P == 0) bar();
      else { asm volatile("s_waitcnt lgkmcnt(0)" ::: "memory"); *(volatile LAS u32*)sig = 1; }
    }
    if constexpr (K % 8 == 0) __builtin_amdgcn_s_setprio(3 - K / 8);
    vmwait<(P == 0 || K < KB ? WIN - 1 : 31 - K)>(d[K]);
    c0 = mfma(wr[K & 15][0], d[K], c0); c1 = mfma(wr[K & 15][1], d[K], c1);
    asm volatile("" : "+v"(c0), "+v"(c1));
    if constexpr (K + WIN < 32) d_issue<K + WIN>(d, s);
    if constexpr (P == 0 && K == 1) bar();
    if constexpr (P == 0 && K >= KB) w_one<1>(nx, (K - KB) >> 2, (K - KB) & 3);
    if constexpr (K >= 1 && K <= 16) w_frag<P>(a0, wr[K - 1], 4 + ((K - 1) >> 2), (K - 1) & 3);
    wr_steps<P, K + 1>(d, wr, a0, s, nx, sig, c0, c1);
  }
}
template <int I> DI void d_first(u32x4 (&d)[32], const DSrc& s) { if constexpr (I < WIN) { d_issue<I>(d, s); d_first<I + 1>(d, s); } }
template <int P> DI void wr_gemm(u32x4 (&wr)[16][2], const WA& a0, const DSrc& s, const WSrc& nx, u32 sig, f32x16& c0, f32x16& c1) {
  u32x4 d[32];
  d_first<0>(d, s);
  c0 = splat(0.f); c1 = splat(0.f);
  wr_steps<P, 0>(d, wr, a0, s, nx, sig, c0, c1);
  if constexpr (P == 0) {
#pragma unroll
    for (int i = WIN; i < 12; ++i) w_one<1>(nx, i >> 2, i & 3);
  }
}
constexpr u32 RSL = 1088;
template <u32 RS> DI void red_write(u32 a, u32 bs, const f32x16& c0, const f32x16& c1) {
#pragma unroll
  for (int q = 0; q < 4; ++q) {
    *(LAS f32x4*)(a + q * RS) = f32x4{c0[4 * q], c0[4 * q + 1], c0[4 * q + 2], c0[4 * q + 3]};
    *(LAS f32x4*)(a + bs + q * RS) = f32x4{c1[4 * q], c1[4 * q + 1], c1[4 * q + 2], c1[4 * q + 3]};
  }
}

template <int O, int I> DI void put16(const f32x16& c) {
  if constexpr (I < 16) { asm volatile("ds_write_addtid_b32 %0 offset:%1" ::"v"(c[I]), "i"(O + (I >> 1) * 768 + (I & 1) * 272) : "memory"); put16<O, I + 1>(c); }
}

constexpr u32 AST = 49152;
struct ASrc { u32x4 rx, rw; u32 vx0, vx1, vw, vq, sx, s0, s1, sq, rot, L; int w; };
DI void a_one(const ASrc& s, int c, int j) {
  const u32 st = s.L + (c % 3) * AST, cc = (c + s.rot) & 7;
  if (j < 4) glds(st + s.w * 4096 + j * 1024, (j & 1) ? s.vx1 : s.vx0, s.rx, s.sx + j * 8192 + cc * 128);
  else glds(st + 32768 + (j - 4) * 8192 + s.w * 1024, s.vw, s.rw, (j == 4 ? s.s0 : s.s1) + cc * (64 * 3072));
}
template <bool KV, int C, int J> DI void a_pref(const ASrc& s, const WSrc& nx) {
  if constexpr (J < 0) {}
  else if constexpr (C < 8) a_one(s, C, J);
  else if constexpr (C == 8) { if constexpr (KV && J < 4) glds(s.L + 98304 + s.w * 4096 + J * 1024, s.vq, s.rw, s.sq + J * (16 * 3072)); }
  else w_one<1>(nx, (6 * (C - 9) + J) >> 2, (6 * (C - 9) + J) & 3);
}
constexpr int a_gap(int C, int g) {
  const int t0[6] = {5, 6, 9, 10, 13, 14}, t1[6] = {1, 3, 6, 9, 11, 14}, t2[6] = {1, 2, -1, -1, -1, -1};
  for (int j = 0; j < 6; ++j) if ((C == 2 ? t0[j] : C == 10 ? t2[j] : t1[j]) == g) return j;
  return -1;
}
struct AMisc { u32x4 rws; u32 cfoff, tag, rdy, clr; int lane; };
struct AAd { u32 x0[4], x1[4], wa; };
template <bool KV, int T> DI void a_steps(const ASrc& s, const WSrc& nx, AAd& ad, u32x4 (&x0)[33], u32x4 (&f)[2][3], f32x16 (&c)[4], u32& cf, const AMisc& ms) {
  if constexpr (T < 32) {
    constexpr int C = T >> 2, K = T & 3, P = T & 1, N = (T + 1) & 3;
    if constexpr (K == 3) {
      if constexpr (C < 6) vmbar<6 + ((KV && C == 1) || C == 3)>(); else if constexpr (C == 6) vmbar<KV ? 5 : 0>(); else vmbar<6>();
      if constexpr (C < 7) {
        constexpr u32 dl = (C + 1) % 3 ? AST : 0u - 2 * AST;
#pragma unroll
        for (int k = 0; k < 4; ++k) { ad.x0[k] += dl; ad.x1[k] += dl; }
        ad.wa += dl;
        asm volatile("" : "+v"(ad.x0[0]), "+v"(ad.x0[1]), "+v"(ad.x0[2]), "+v"(ad.x0[3]), "+v"(ad.x1[0]), "+v"(ad.x1[1]), "+v"(ad.x1[2]), "+v"(ad.x1[3]), "+v"(ad.wa));
      }
      if constexpr (C == 0 && KV) { if (s.w < 4) st128c(u32x4{~0u, ~0u, ~0u, ~0u}, ms.lane * 16, ms.rws, ms.clr); else st32c(~0u, (ms.lane & 31) * 4, ms.rws, ms.clr); }
      if constexpr (C == 2) { st32c(ms.tag, ms.rdy, ms.rws, 0); }
      if constexpr (C == 5 && KV) ld32c(cf, ms.cfoff, ms.rws, 0);
    }
    constexpr int PC = K == 3 ? C + 3 : C + 2, G0 = 4 * N;
    __builtin_amdgcn_s_setprio(3 - N);
    if constexpr (T < 31) { x0[T + 1] = lds128(ad.x0[N]); f[P ^ 1][1] = trd2(ad.wa + N * 2048, ad.wa + N * 2048 + 512); }
    SB();
    if constexpr (KV) c[0] = mfma(x0[T], f[P][1], c[0]); else c[0] = mfma(f[P][1], x0[T], c[0]);
    SB();
    if constexpr (T < 31) { f[P ^ 1][0] = lds128(ad.x1[N]); f[P ^ 1][2] = trd2(ad.wa + 8192 + N * 2048, ad.wa + 8192 + N * 2048 + 512); }
    a_pref<KV, PC, a_gap(PC, G0)>(s, nx);
    SB();
    if constexpr (KV) c[1] = mfma(f[P][0], f[P][1], c[1]); else c[1] = mfma(f[P][1], f[P][0], c[1]);
    SB();
    a_pref<KV, PC, a_gap(PC, G0 + 1)>(s, nx);
    SB();
    if constexpr (KV) c[2] = mfma(x0[T], f[P][2], c[2]); else c[2] = mfma(f[P][2], x0[T], c[2]);
    SB();
    a_pref<KV, PC, a_gap(PC, G0 + 2)>(s, nx);
    SB();
    if constexpr (KV) c[3] = mfma(f[P][0], f[P][2], c[3]); else c[3] = mfma(f[P][2], f[P][0], c[3]);
    SB();
    a_pref<KV, PC, a_gap(PC, G0 + 3)>(s, nx);
    SB();
    a_steps<KV, T + 1>(s, nx, ad, x0, f, c, cf, ms);
  }
}

DI ASrc a_src(const char* x, const char* wqkv, u32 L, int lane, int w, int xcd, int slot) {
  const bool kv = slot < 30;
  const int ai = slot - 22, rc = (xcd & 6) + (kv ? ai >> 2 : slot - 30), hg = (xcd & 1) * 4 + (ai & 3), qj = xcd & 1;
  ASrc sa;
  sa.rx = srd(x, 2048 * 1024); sa.rw = srd(wqkv, 512 * 3072); sa.L = L; sa.w = w;
  sa.rot = xcd & 6;
  sa.vx0 = (lane >> 3) * 1024 + ((lane & 7) ^ (lane >> 4)) * 16; sa.vx1 = sa.vx0 ^ 64;
  sa.vw = (lane >> 3) * 3072 + ((lane & 7) ^ ((lane >> 2) & 4)) * 16;
  sa.vq = (lane >> 2) * 3072 + (lane & 3) * 16;
  sa.sx = (rc * 256 + w * 32) * 1024;
  sa.s0 = w * 8 * 3072 + (kv ? 1024 + hg * 128 : 512 + qj * 256);
  sa.s1 = sa.s0 + (kv ? 1024 : 128);
  sa.sq = ((w + sa.rot) & 7) * (64 * 3072) + hg * 64;
  return sa;
}
DI WSrc w1_src(const char* w1, u32 lds, int lane, int w, int xcd, int slot, int& ft, int& dt1) {
  const int gj = slot < 16 ? (xcd >> 1) * 16 + slot : 48 + (xcd >> 1) * 6 + slot;
  const u32 wch = (lane & 7) ^ ((lane >> 2) & 4);
  ft = gj >> 2; dt1 = (xcd & 1) * 4 + (gj & 3);
  WSrc s1;
  s1.rs = srd(w1, 2048 * 2752); s1.stride = 2752; s1.lds = lds;
  s1.voff = (lane >> 3) * 2752 + (ft == 21 ? (wch & 3) : wch) * 16;
  s1.sbase = ((w >> 1) * 512 + (w & 1) * 32) * 2752 + ft * 128;
  s1.rot = (2 * (dt1 + ft)) & 7;
  return s1;
}
template <bool KV> DI u32 attn(u32 ev, const char* x, const char* wqkv, u32x4 rws, const WSrc& s2, u32 L, int lane, int w, int xcd, int slot, u32 wa0, u32x4 (&wr)[16][2]) {
  const int hh = lane >> 5, col = lane & 31, q4 = (lane & 15) >> 2, mi = w >> 1, ni = w & 1;
  const int ai = slot - 22, rc = (xcd & 6) + (KV ? ai >> 2 : slot - 30), hg = (xcd & 1) * 4 + (ai & 3), qj = xcd & 1;
  const ASrc sa = a_src(x, wqkv, L, lane, w, xcd, slot);
  AAd ad;
#pragma unroll
  for (int k = 0; k < 4; ++k) { ad.x0[k] = L + (mi * 64 + ni * 32 + col) * 128 + ((2 * k + hh) ^ ((col >> 1) & 7)) * 16; ad.x1[k] = ad.x0[k] ^ 4096; }
  ad.wa = L + 32768 + (8 * hh + q4) * 128 + 64 * ((q4 >> 1) ^ ni) + 32 * ((lane >> 4) & 1) + 8 * (lane & 3);
  const int pr = hg + (mi & 1) * 8;
  const bool ga = w < 4;
  const u32 cfoff = ga ? WS_F + xcd * FRS + (128 + (pr >> 1) * 8 + (lane & 7)) * 128 : OOB;
  asm volatile("" : "+v"(ad.x0[0]), "+v"(ad.x0[1]), "+v"(ad.x0[2]), "+v"(ad.x0[3]), "+v"(ad.x1[0]), "+v"(ad.x1[1]), "+v"(ad.x1[2]), "+v"(ad.x1[3]), "+v"(ad.wa)::"memory");
  const u32 tag = tag_wait<6>(ev);
  bar();
  const AMisc ms = {rws, cfoff, tag, lane == 0 ? WS_F + w * FRS + (KV ? 128 + hg * 8 + rc : 192 + qj * 8 + rc) * 128 : OOB, w < 4 ? WS_KV + ((hg * 2 + (w >> 1)) * 8 + rc) * 2048 + (w & 1) * 1024 : WS_Z + ((hg * 2 + (w & 1)) * 8 + rc) * 128, lane};
  u32x4 x0[33], f[2][3]; f32x16 c[4]; u32 cf;
  c[0] = c[1] = splat(KV ? 1.f : 1.000001f); c[2] = c[3] = splat(KV ? 0.f : 1.000001f);
  x0[0] = lds128(ad.x0[0]); f[0][0] = lds128(ad.x1[0]); f[0][1] = trd2(ad.wa, ad.wa + 512); f[0][2] = trd2(ad.wa + 8192, ad.wa + 8192 + 512);
  a_steps<KV, 0>(sa, s2, ad, x0, f, c, cf, ms);
  __builtin_amdgcn_s_setprio(0);
  asm volatile("" : "+v"(c[0]), "+v"(c[1]), "+v"(c[2]), "+v"(c[3]));
  const u32 pfl = WS_F + xcd * FRS + (512 + rc * 16 + 8 + qj) * 128;
  if constexpr (!KV) {
#pragma unroll
    for (int j = 0; j < 4; ++j)
#pragma unroll
      for (int h = 0; h < 2; ++h) { st128c(phi8<true>(c[j], 8 * h), lane * 16 + qj * 8192 + ni * 2048, rws, WS_P + (rc * 8 + (w ^ (j & 1))) * 16384 + ((j & 2) * 2 + h) * 1024); SB(); }
    asm volatile("s_barrier" ::: "memory");
    w_issue<1>(s2, 2);
    vmbar<4>();
    if (w == 0) st32c(tag, lane == 0 ? 0 : OOB, rws, pfl);
    if (blockIdx.x == 255 && w == 0) {
#pragma unroll 1
      for (int i = 0; i < 4; ++i) poll(WS_F + 7 * FRS + (i * 64 + lane) * 128, rws, tag);
      st32c(tag, lane == 0 ? 0 : OOB, rws, WS_E);
    }
    vmbar<0>();
    w_stage(w_addr(wa0), wr, 0, 12);
    bar();
    w_flag<3>(s2, w, tag, OOB, rws, 0);
    return tag;
  }
  const u32 S = L + 65536, KT = S + 14336, ZT = KT + 2048;
  if (w >= 4) w_issue<1>(s2, 2);
  const u32 on = ((lane >> 4) ^ (lane >> 2)) & 1 ? 0 : 0x3f803f80u;
  const u32x4 cs = {on, on, on, on};
  f32x16 d1 = splat(0.f); f32x4 d2 = {64e-6f, 64e-6f, 64e-6f, 64e-6f}, d3 = {0.f, 0.f, 0.f, 0.f};
  u32x4 p[4];
#pragma unroll
  for (int i = 0; i < 4; ++i) {
    const f32x16& k = c[i >> 1]; const int o = 8 * (i & 1);
    p[i][0] = phi2<false>(k[o], k[o + 1]); SB();
    if (i) d2 = mfma16(cs, p[i - 1], d2);
    const u32x4 v = pack8(c[2 + (i >> 1)], o);
    p[i][1] = phi2<false>(k[o + 2], k[o + 3]); SB();
    d3 = mfma16(cs, v, d3);
    p[i][2] = phi2<false>(k[o + 4], k[o + 5]); p[i][3] = phi2<false>(k[o + 6], k[o + 7]); SB();
    d1 = mfma(p[i], v, d1);
    SB(); if (w < 4) w_one<1>(s2, 2, i); SB();
  }
  d2 = mfma16(cs, p[3], d2);
  const bool lo = col < 16;
  f32x4 k0 = {lo ? d1[0] : d1[8], lo ? d1[1] : d1[9], lo ? d1[2] : d1[10], lo ? d1[3] : d1[11]};
  f32x4 k1 = {lo ? d1[4] : d1[12], lo ? d1[5] : d1[13], lo ? d1[6] : d1[14], lo ? d1[7] : d1[15]};
  const float ev6 = d3[0] * 1e-6f;
  k0 = add4(k0, f32x4{ev6, ev6, ev6, ev6}); k1 = add4(k1, f32x4{ev6, ev6, ev6, ev6});
  const float zp = d2[0];
  if (w >= 2) { *(LAS f32x4*)(S + (w - 2) * 2048 + lane * 16) = k0; *(LAS f32x4*)(S + (w - 2) * 2048 + 1024 + lane * 16) = k1; *(LAS float*)(S + 12288 + (w - 2) * 256 + lane * 4) = zp; }
  vmbar<0>();
  if (w < 2) {
    const u32 a = S + w * 2048 + lane * 16, z = S + 12288 + w * 256 + lane * 4;
    k0 = add4(add4(k0, ldsf4(a)), add4(ldsf4(a + 4096), ldsf4(a + 8192))); k1 = add4(add4(k1, ldsf4(a + 1024)), add4(ldsf4(a + 5120), ldsf4(a + 9216)));
    const float t = add1(add1(zp, ldsf(z)), add1(ldsf(z + 512), ldsf(z + 1024)));
    u32x4 p0 = __builtin_bit_cast(u32x4, k0), p1 = __builtin_bit_cast(u32x4, k1);
#pragma unroll
    for (int i = 0; i < 4; ++i) { p0[i] = umin(p0[i], ~1u); p1[i] = umin(p1[i], ~1u); }
    const u32 o = (hg * 2 + w) * 8 + rc;
    st128c(p0, lane * 16, rws, WS_KV + o * 2048); st128c(p1, lane * 16, rws, WS_KV + o * 2048 + 1024); st32c(umin(__float_as_uint(t), ~1u), col * 4, rws, WS_Z + o * 128);
  }
  u32x4 pq[2][2], t[8]; u32x2 z; u32 pf;
  u32 qa = L + 98304 + (8 * hh + q4) * 64 + 32 * ((lane >> 4) & 1) + 8 * (lane & 3);
  asm volatile("" : "+v"(qa), "+v"(cf));
  u32x4 qf[4];
#pragma unroll
  for (int i = 0; i < 3; ++i) qf[i] = trd2(qa + i * 1024, qa + i * 1024 + 256);
  f32x16 cq = splat(1.000001f);
#pragma unroll
  for (int i = 0; i < 32; ++i) {
    SB();
    if (i < 29) qf[(i + 3) & 3] = trd2(qa + (i + 3) * 1024, qa + (i + 3) * 1024 + 256);
    SB();
    cq = mfma(qf[i & 3], x0[i], cq);
  }
  SB();
  auto sweep = [&]() {
#pragma unroll
    for (int r = 0; r < 8; ++r) ld128c<0>(t[r], lane * 16, rws, WS_KV + (pr * 8 + r) * 2048 + ni * 1024);
    asm volatile("buffer_load_dwordx2 %0, %1, %2, %3 offen sc1" : "=v"(z) : "v"((lane >> 3) * 128 + (lane & 7) * 8), "s"(rws), "s"(WS_Z + pr * 1024 + ni * 64) : "memory");
  };
  asm volatile("" : "=v"(t[0]), "=v"(t[1]), "=v"(t[2]), "=v"(t[3]), "=v"(t[4]), "=v"(t[5]), "=v"(t[6]), "=v"(t[7]), "=v"(z));
  if (ga && __builtin_amdgcn_ballot_w64(cf != tag) != 0) poll(cfoff, rws, tag);
  ld32c(pf, pfl, rws, 0);
  if (mi == 0) sweep();
  pq[0][0] = phi8<true>(cq, 0); pq[0][1] = phi8<true>(cq, 8);
  asm volatile("" : "+v"(pq[0][0]), "+v"(pq[0][1]));
  w_stage(w_addr(wa0), wr, 0, 12);
  SB();
  if (mi == 1) sweep();
  if (!ga) vmwait<0>();
  asm volatile("s_waitcnt vmcnt(9)" : "+v"(pf)::"memory");
  if (__builtin_amdgcn_ballot_w64(pf != tag) != 0) poll(pfl, rws, tag);
  ld128c<0>(pq[1][0], lane * 16, rws, WS_P + (rc * 8 + w) * 16384 + hg * 2048); ld128c<1024>(pq[1][1], lane * 16, rws, WS_P + (rc * 8 + w) * 16384 + hg * 2048);
#define GW(N) asm volatile("s_waitcnt vmcnt(%9)" : "+v"(t[0]), "+v"(t[1]), "+v"(t[2]), "+v"(t[3]), "+v"(t[4]), "+v"(t[5]), "+v"(t[6]), "+v"(t[7]), "+v"(z) : "i"(N) : "memory")
  GW(2);
  if (ga) {
    for (int it = 0; it < SPIN_MAX; ++it) {
      u32 m = umax(z[0], z[1]);
#pragma unroll
      for (int r = 0; r < 8; ++r) m = umax(umax(umax(m, t[r][0]), t[r][1]), umax(t[r][2], t[r][3]));
      if (__builtin_amdgcn_ballot_w64(m == ~0u) == 0) break;
      sweep();
      GW(0);
    }
    f32x4 a = __builtin_bit_cast(f32x4, t[0]);
#pragma unroll
    for (int r = 1; r < 8; ++r) a += __builtin_bit_cast(f32x4, t[r]);
    float zs = fold32(__uint_as_float(z[0]), __uint_as_float(z[1]));
    zs = dppadd<0x128>(fold16(zs, zs));
    const int pl = mi, zf = 2 * (lane & 7) + hh;
    *(LAS u32x2*)(KT + pl * 1024 + lane * 16 + ni * 8) = u32x2{pk(a[0], a[1]), pk(a[2], a[3])};
    *(LAS unsigned short*)(ZT + pl * 64 + ni * 32 + ((zf >> 2) & 1) * 16 + ((zf & 3) + 4 * (zf >> 3)) * 2) = (unsigned short)pk(zs, zs);
  }
  bar();
  f32x16 dd[2][2];
#pragma unroll
  for (int j = 0; j < 2; ++j) {
    if (j) asm volatile("s_waitcnt vmcnt(0)" : "+v"(pq[1][0]), "+v"(pq[1][1])::"memory");
#pragma unroll
    for (int h = 0; h < 2; ++h) dd[j][h] = mfma(lds128(((col < 16) == (h == 0)) ? KT + j * 1024 + lane * 16 : ZT + j * 64 + h * 32 + hh * 16), pq[j][h], splat(0.f));
  }
  SB();
  w_issue<1>(s2, 3);
  SB();
#pragma unroll
  for (int j = 0; j < 2; ++j)
#pragma unroll
    for (int h = 0; h < 2; ++h) {
      const int o = 8 * h;
      const f32x16& e = dd[j][h];
      const float rd = __builtin_amdgcn_rcpf(e[8 - o] + 1e-6f);
      const u32x4 ov = {pk(e[o] * rd, e[o + 1] * rd), pk(e[o + 2] * rd, e[o + 3] * rd), pk(e[o + 4] * rd, e[o + 5] * rd), pk(e[o + 6] * rd, e[o + 7] * rd)};
      st128c(ov, lane * 16, rws, WS_O + (rc * 8 + w) * 32768 + ((hg + 8 * j) * 2 + h) * 1024);
    }
  bar();
  w_flag<4>(s2, w, tag, lane < 2 ? ((rc & 6) + lane) * FRS : OOB, rws, WS_F + (512 + rc * 16 + hg) * 128);
  return tag;
}

__global__ __launch_bounds__(512, 2) void fused_kernel(const char* __restrict__ x, const char* __restrict__ wqkv, const char* __restrict__ wo,
                                                        const char* __restrict__ w1, const char* __restrict__ b1, const char* __restrict__ w2,
                                                        const char* __restrict__ b2, const char* __restrict__ gw, char* __restrict__ out, char* ws) {
  __shared__ __attribute__((aligned(1024))) char sm[163840];
  u32 ev;
  {
    unsigned long long pc; u32 cn;
    asm volatile("s_getpc_b64 %0\n.Lfk_pc:\n\ts_mov_b32 %1, .Lfk_end-.Lfk_pc" : "=s"(pc), "=s"(cn));
    const u32 vo = (blockIdx.x >> 3 & 15) == 0 ? threadIdx.x * 128 : OOB;
    asm volatile("buffer_load_dword %0, %1, %2, 0 offen" : "=v"(ev) : "v"(vo), "s"(srd((const void*)pc, cn)) : "memory");
    SB();
  }
  asm volatile("" : "+s"(x), "+s"(wqkv), "+s"(wo), "+s"(w1), "+s"(b1), "+s"(w2), "+s"(b2), "+s"(gw), "+s"(out), "+s"(ws));
  const u32 L = (u32)(size_t)(LAS char*)sm;
  const int lane = threadIdx.x & 63, w = __builtin_amdgcn_readfirstlane(threadIdx.x >> 6);
  int xcd = blockIdx.x & 7, slot = blockIdx.x >> 3;
  const int hh = lane >> 5;
  const u32x4 rws = srd(ws, WS_END);
  u32x4 xr[2], gr;
  asm volatile("s_nop 4" : "=v"(xr[0]), "=v"(xr[1]), "=v"(gr)::"memory");
  ev_load(ev, rws);
  if (slot < 22) {
    int ft, dt1;
    const WSrc s1 = w1_src(w1, L + w * 4096, lane, w, xcd, slot, ft, dt1);
    if (slot < 16) {
      const int r = (xcd * 16 + slot) * 16 + w;
      const u32x4 rx = srd(x, 2048 * 1024);
      ld128(xr[0], lane * 16, rx, r * 1024); ld128(xr[1], lane * 16, rx, (r + 8) * 1024);
      ld128(gr, lane * 16, srd(gw, 1024), 0);
      asm volatile("s_barrier" ::: "memory");
    }
    w_issue<0>(s1, 0);
  } else {
    const ASrc sa = a_src(x, wqkv, L, lane, w, xcd, slot);
#pragma unroll
    for (int j = 0; j < 6; ++j) a_one(sa, 0, j);
    asm volatile("s_barrier" ::: "memory");
#pragma unroll
    for (int j = 0; j < 6; ++j) a_one(sa, 1, j);
  }
  asm volatile("" : "+s"(slot), "+s"(xcd));
  const int g = w >> 1, hf = w & 1, q4 = (lane & 15) >> 2;
  const u32 wch = (lane & 7) ^ ((lane >> 2) & 4);
  const u32 wa0 = L + g * 8192 + (4 * hh + q4) * 128 + 64 * (q4 >> 1) + 32 * ((lane >> 4) & 1) + 8 * (lane & 3);

  const int dt = (xcd & 1) * 4 + (slot & 3), stl = (xcd >> 1) * 8 + (slot >> 2);
  WSrc s2;
  s2.rs = g < 3 ? srd(w2, 1376 * 4096) : srd(wo, 512 * 1024); s2.stride = g < 3 ? 4096 : 1024; s2.lds = L + g * 8192 + hf * 4096;
  s2.voff = (lane >> 3) * s2.stride + wch * 16;
  s2.sbase = g < 3 ? (g * 512 + hf * 32) * 4096 + stl * 128 : hf * 32 * 1024 + dt * 128;
  s2.rot = 2 * ((slot + (slot >> 2)) & 3) + (xcd & 1);
  u32 tag;
  u32x4 wr[16][2];

  if (slot >= 30) tag = attn<false>(ev, x, wqkv, rws, s2, L, lane, w, xcd, slot, wa0, wr);
  else if (slot >= 22) tag = attn<true>(ev, x, wqkv, rws, s2, L, lane, w, xcd, slot, wa0, wr);
  else {
    const int gi = slot < 16 ? xcd * 16 + slot : 112 + xcd * 6 + slot;
    int ft, dt1;
    const WSrc s1 = w1_src(w1, s2.lds, lane, w, xcd, slot, ft, dt1);
    if (slot < 16) {
      const int sb = xcd * 16 + slot;
      tag = tag_wait<4>(ev);
      asm volatile("" : "+v"(xr[0]), "+v"(xr[1]), "+v"(gr)::"memory");
      f32x2 v[2][4], t; float s[2], gd[2], q[2];
#pragma unroll
      for (int j = 0; j < 2; ++j) {
#pragma unroll
        for (int i = 0; i < 4; ++i) v[j][i] = f32x2{blo(xr[j][i]), bhi(xr[j][i])};
        t = (v[j][0] + v[j][1]) + (v[j][2] + v[j][3]); s[j] = t[0] + t[1];
      }
      const float t0 = fold32(s[0], s[1]), t1 = rowsum(fold16(t0, t0)) * (1.f / 512.f);
#pragma unroll
      for (int j = 0; j < 2; ++j) {
        t = f32x2{0.f, 0.f};
#pragma unroll
        for (int i = 0; i < 4; ++i) t = __builtin_elementwise_fma(v[j][i], f32x2{blo(gr[i]), bhi(gr[i])}, t);
        gd[j] = t[0] + t[1];
        const float m = rdl(t1, 32 * j);
        t = f32x2{0.f, 0.f};
#pragma unroll
        for (int i = 0; i < 4; ++i) { v[j][i] -= m; t = __builtin_elementwise_fma(v[j][i], v[j][i], t); }
        q[j] = t[0] + t[1];
      }
      const float t2 = rowsum(fold16(fold32(q[0], q[1]), fold32(gd[0], gd[1])));
      const float rs = __builtin_amdgcn_rsqf(t2 * (1.f / 512.f) + 1e-5f);
#pragma unroll
      for (int j = 0; j < 2; ++j) {
        const float r = rdl(rs, 32 * j);
#pragma unroll
        for (int i = 0; i < 4; ++i) v[j][i] *= r;
        *(LAS u32x4*)(L + (w + 8 * j) * 1088 + lane * 16) = u32x4{pk(v[j][0][0], v[j][0][1]), pk(v[j][1][0], v[j][1][1]), pk(v[j][2][0], v[j][2][1]), pk(v[j][3][0], v[j][3][1])};
      }
      const float gt = __builtin_amdgcn_rcpf(1.f + __builtin_amdgcn_exp2f(t2 * -1.4426950408889634f));
      bar();
      const u32 ta = L + (4 * hh + q4) * 1088 + w * 128 + 32 * ((lane >> 4) & 1) + 8 * (lane & 3);
      const u32x4 h0 = trd2(ta, ta + 8 * 1088), h1 = trd2(ta + 64, ta + 64 + 8 * 1088);
      st32c(__float_as_uint(gt), (lane & 31) == 16 ? lane & 32 : OOB, rws, WS_G + (sb * 16 + w) * 4);
      st128c(h0, lane * 16, rws, WS_H + w * 262144 + sb * 1024);
      st128c(h1, lane * 16, rws, WS_H + w * 262144 + 131072 + sb * 1024);
      asm volatile("s_barrier" ::: "memory");
      w_issue<0>(s1, 1);
      vmbar<4>();
    } else {
      u32 tl;
      const int k = slot - 16, nl = w == 7 ? 44 : w == 6 ? (k < 4 ? 22 : 10) : w == 3 || w == 4 ? 6 : k == w ? 64 : 0;
      asm volatile("buffer_load_dword %0, %1, %2, %3 offen sc1" : "=v"(tl) : "v"(lane >= nl ? OOB : w == 7 ? (lane >> 1) * 3072 + (lane & 1) * 128 : lane << (nl == 64 ? 18 : 7)),
                   "s"(w == 0 ? srd(w2, 1376 * 4096) : w == 1 ? srd(wo, 512 * 1024) : w == 2 ? srd(out, 2048 * 1024) : w == 3 ? srd(b1, 2752) : w == 4 ? srd(b2, 4096) : w == 7 ? srd(wqkv, 512 * 3072) : rws),
                   "s"(w == 7 ? ((xcd >> 1) * 6 + k) * (22 * 3072) + (xcd & 1) * 256 : w == 6 ? WS_F + xcd * FRS + (k < 4 ? 256 + ((xcd & 1) * 4 + k) * 32 : 448 + ((xcd & 6) + k) * 16) * 128 : k * 768) : "memory");
      w_issue<0>(s1, 1);
      tag = tag_wait<4>(ev);
      asm volatile("" ::"v"(tl));
      bar();
    }
    const WA a0 = w_addr(wa0);
    if (w == 0) st32c(tag, lane < 8 ? lane * FRS : OOB, rws, WS_F + (slot < 16 ? gi : gi + 80) * 128);
    w_round<0, 0, 2>(s1, a0, wr); w_issue<0>(s1, 3);
    vmbar0<8>(w);
    w_round<0, 1, 4>(s1, a0, wr);
    vmbar<8>();
    w_round<0, 2, 5>(s1, a0, wr);
    vmbar<8>();
    const u32 hfl = hf ? WS_F + xcd * FRS + (g * 32 + (lane & 31)) * 128 : OOB;
    u32 pf; u32x2 bb0, bb1;
    ld32c(pf, hfl, rws, 0);
    asm volatile("buffer_load_dwordx2 %0, %2, %3, %4 offen\n\tbuffer_load_dwordx2 %1, %2, %3, %4 offen offset:16" : "=&v"(bb0), "=&v"(bb1) : "v"(hh * 8), "s"(srd(b1, 2752)), "s"(ft * 128 + g * 32) : "memory");
    w_round<0, 3, 6>(s1, a0, wr); w_issue<0>(s1, 7);
    asm volatile("s_waitcnt vmcnt(8)" : "+v"(pf), "+v"(bb0), "+v"(bb1)::"memory");
    if (hf && __builtin_amdgcn_ballot_w64(pf != tag) != 0) poll(hfl, rws, tag);
    bar();
    f32x16 c0, c1;
    wr_gemm<0>(wr, a0, DSrc{rws, (u32)lane * 16, WS_H + (dt1 * 2 + hf) * 131072 + g * 32768, s1.rot}, s2, 0, c0, c1);
    const u32 R = L + 65536 + hf * 32768 + lane * 16;
    f32x4 lo = {blo(bb0[0]), bhi(bb0[0]), blo(bb0[1]), bhi(bb0[1])}, hi = {blo(bb1[0]), bhi(bb1[0]), blo(bb1[1]), bhi(bb1[1])};
#pragma unroll
    for (int q = 0; q < 4; ++q) {
      const f32x16& c = q < 2 ? c0 : c1; const int o = (q & 1) * 8;
      const f32x4 a = {c[o], c[o + 1], c[o + 2], c[o + 3]}, b = {c[o + 4], c[o + 5], c[o + 6], c[o + 7]};
      if (q == g) { lo += a; hi += b; }
      else { const u32 t = R + q * 8192 + ((g - q) & 3) * 2048; *(LAS f32x4*)t = a; *(LAS f32x4*)(t + 1024) = b; }
    }
    bar();
#pragma unroll
    for (int k = 1; k < 4; ++k) { lo += ldsf4(R + g * 8192 + k * 2048); hi += ldsf4(R + g * 8192 + k * 2048 + 1024); }
    const u32x4 uv = {pk(gelu(lo[0]), gelu(lo[1])), pk(gelu(lo[2]), gelu(lo[3])), pk(gelu(hi[0]), gelu(hi[1])), pk(gelu(hi[2]), gelu(hi[3]))};
    st128c(uv, lane * 16, rws, WS_U + (dt1 * 2 + hf) * UDB + (ft * 4 + g) * 1024);
    vmbar<0>();
    if (w == 0) st32c(tag, lane < 4 ? (lane * 2 + (dt1 >> 2)) * FRS : OOB, rws, WS_F + (256 + dt1 * 32 + ft) * 128);
    w_stage(a0, wr, 0, 12);
    bar();
    w_issue<1>(s2, 3); w_issue<1>(s2, 4); w_issue<1>(s2, 5);
  }

  {
    asm volatile("" : "+s"(s2.lds), "+s"(s2.rot), "+s"(s2.sbase));
    const int sr = w * 8 + (lane >> 3), cq = lane & 7;
    u32 b2v;
    asm volatile("buffer_load_ushort %0, %1, %2, %3 offen" : "=v"(b2v) : "v"(sr * 2), "s"(srd(b2, 4096)), "s"(stl * 128) : "memory");
    const u32 GT = L + 131072, SG = GT + 256;
    const WA a0 = w_addr(wa0);
    f32x16 c0, c1;
    const int nf = g == 2 ? 6 : 8;
    const u32 fo = WS_F + xcd * FRS + ((g < 3 ? 256 + dt * 32 + g * 8 : 512 + (stl >> 2) * 16) + (lane < nf ? lane : 0)) * 128;
    w_issue<1>(s2, 6);
    w_issue<1>(s2, 7);
    vmbar<17>();
    w_round<1, 3, 8>(s2, a0, wr);
    bar();
    *(volatile LAS u32*)(SG + w * 4) = 0;
    poll_bar(fo, rws, tag);
    asm volatile("" : "+v"(b2v));
    u32 gv;
    ld32c(gv, w == 0 ? lane * 4 : OOB, rws, WS_G + stl * 256);
    const u32 ub = WS_U + (dt * 2 + hf) * UDB;
    wr_gemm<1>(wr, a0, DSrc{srd(ws, g < 3 ? ub + 86 * 1024 : WS_END), (u32)lane * 16, g < 3 ? ub + g * 32768 : WS_O + (stl * 2 + hf) * 32768, s2.rot}, s2, SG + w * 4, c0, c1);
    asm volatile("" : "+v"(gv));
    if (w == 0) *(LAS u32*)(GT + lane * 4) = gv;
    for (int it = 0; it < SPIN_MAX * 16 && *(volatile LAS u32*)(SG + (w ^ 1) * 4) == 0; ++it) {}
    const u32 pb = L + g * 8192 + hf * 32768;
    if (g < 3) {
      asm volatile("s_mov_b32 m0, %0\n\ts_nop 15\n\ts_nop 7" ::"s"(pb + hf * 128) : "memory");
      put16<0, 0>(c0);
      asm volatile("s_mov_b32 m0, %0\n\ts_nop 0" ::"s"(pb + hf * 128 + 6144) : "memory");
      put16<65536 - 6144, 0>(c1);
    } else red_write<RSL>(pb + lane * 16, 65536, c0, c1);
    bar();
    const int sl = lane >> 3, i = (sl & 3) + 4 * (w & 3);
    const u32 ya = L + (cq >> 2) * (32768 + 128) + (w >> 2) * 65536 + (i >> 1) * 768 + (i & 1) * 272 + (sl >> 2) * 128 + (cq & 3) * 32;
    const f32x4 y0 = (ldsf4(ya) + ldsf4(ya + 8192)) + ldsf4(ya + 16384), y1 = (ldsf4(ya + 16) + ldsf4(ya + 8208)) + ldsf4(ya + 16400);
    const u32 aa = L + 24576 + (w >> 2) * 32768 + (cq >> 2) * 65536 + (cq & 3) * RSL + (sr & 31) * 16;
    const f32x4 a0v = ldsf4(aa), a1v = ldsf4(aa + 512);
    const float gt = ldsf(GT + sr * 4), bb = blo(b2v), og = 1.f - gt;
    u32x4 ov;
    ov[0] = pk(gt * a0v[0] + og * (y0[0] + bb), gt * a0v[1] + og * (y0[1] + bb));
    ov[1] = pk(gt * a0v[2] + og * (y0[2] + bb), gt * a0v[3] + og * (y0[3] + bb));
    ov[2] = pk(gt * a1v[0] + og * (y1[0] + bb), gt * a1v[1] + og * (y1[1] + bb));
    ov[3] = pk(gt * a1v[2] + og * (y1[2] + bb), gt * a1v[3] + og * (y1[3] + bb));
    asm volatile("buffer_store_dwordx4 %0, %1, %2, %3 offen nt" ::"v"(ov), "v"(sr * 1024 + cq * 16), "s"(srd(out, 2048 * 1024)), "s"(stl * 65536 + dt * 128) : "memory");
    asm volatile(".Lfk_end:");
  }
}

extern "C" void kernel_launch(void* const* d_in, const int* in_sizes, int n_in, void* d_out, int out_size, void* d_ws, size_t ws_size, hipStream_t stream) {
  fused_kernel<<<256, 512, 0, stream>>>((const char*)d_in[0], (const char*)d_in[1], (const char*)d_in[2], (const char*)d_in[4], (const char*)d_in[5],
                                        (const char*)d_in[6], (const char*)d_in[7], (const char*)d_in[8], (char*)d_out, (char*)d_ws);
}
```

```cpp
#include <hip/hip_runtime.h>
#include <hip/hip_bf16.h>


#pragma clang diagnostic ignored "-Wint-to-pointer-cast"
#define DI __device__ __forceinline__
#define LAS __attribute__((address_space(3)))
#define SB() __builtin_amdgcn_sched_barrier(0)
typedef unsigned u32;
typedef u32 u32x4 __attribute__((ext_vector_type(4)));
typedef u32 u32x2 __attribute__((ext_vector_type(2)));
typedef float f32x2 __attribute__((ext_vector_type(2)));
typedef float f32x4 __attribute__((ext_vector_type(4)));
typedef float f32x16 __attribute__((ext_vector_type(16)));
typedef __bf16 bf16x8 __attribute__((ext_vector_type(8)));
typedef __bf16 bf16x2 __attribute__((ext_vector_type(2)));
typedef short s16x4 __attribute__((ext_vector_type(4)));

constexpr u32 WS_E = 0, WS_G = 32768;
constexpr u32 WS_F = 7u << 20, FRS = 641 * 128;
constexpr u32 WS_KV = 65536, WS_Z = WS_KV + 262144, WS_H = 1u << 20, WS_U = 3u << 20, WS_O = 5u << 20, WS_P = 8u << 20, WS_END = 9u << 20, OOB = 1u << 30;
constexpr u32 UDB = 88 * 1024;
constexpr int SPIN_MAX = 20000;
constexpr int WIN = 10;

DI u32x4 srd(const void* p, u32 bytes) { const unsigned long long a = (unsigned long long)p; return u32x4{(u32)a, (u32)(a >> 32), bytes, 0x00020000u}; }
DI f32x16 mfma(u32x4 a, u32x4 b, f32x16 c) { return __builtin_amdgcn_mfma_f32_32x32x16_bf16(__builtin_bit_cast(bf16x8, a), __builtin_bit_cast(bf16x8, b), c, 0, 0, 0); }
DI f32x4 mfma16(u32x4 a, u32x4 b, f32x4 c) { return __builtin_amdgcn_mfma_f32_16x16x32_bf16(__builtin_bit_cast(bf16x8, a), __builtin_bit_cast(bf16x8, b), c, 0, 0, 0); }
DI u32x2 trd(u32 a) { return __builtin_bit_cast(u32x2, __builtin_amdgcn_ds_read_tr16_b64_v4i16((LAS s16x4*)a)); }
DI u32x4 trd2(u32 a, u32 b) { const u32x2 x = trd(a), y = trd(b); return u32x4{x[0], x[1], y[0], y[1]}; }
DI u32x4 lds128(u32 a) { return *(LAS u32x4*)a; }
DI f32x4 ldsf4(u32 a) { return *(LAS f32x4*)a; }
DI float ldsf(u32 a) { return *(LAS float*)a; }
DI u32 pk(float a, float b) { return __builtin_bit_cast(u32, __builtin_convertvector(f32x2{a, b}, bf16x2)); }
DI float blo(u32 v) { return __uint_as_float(v << 16); }
DI float bhi(u32 v) { return __uint_as_float(v & 0xffff0000u); }
DI u32x4 pack8(const f32x16& x, int o) { return u32x4{pk(x[o], x[o + 1]), pk(x[o + 2], x[o + 3]), pk(x[o + 4], x[o + 5]), pk(x[o + 6], x[o + 7])}; }
DI f32x16 splat(float v) { return f32x16{v, v, v, v, v, v, v, v, v, v, v, v, v, v, v, v}; }
DI float add1(float a, float b) { float r; asm("v_add_f32 %0, %1, %2" : "=v"(r) : "v"(a), "v"(b)); return r; }
DI f32x4 add4(f32x4 a, f32x4 b) { return f32x4{add1(a[0], b[0]), add1(a[1], b[1]), add1(a[2], b[2]), add1(a[3], b[3])}; }
DI u32 umin(u32 a, u32 b) { return a < b ? a : b; }
DI u32 umax(u32 a, u32 b) { return a > b ? a : b; }

DI void glds(u32 lds, u32 voff, u32x4 rs, u32 soff) {
  asm volatile("s_mov_b32 m0, %0\n\ts_nop 0\n\tbuffer_load_dwordx4 %1, %2, %3 offen lds" ::"s"(lds), "v"(voff), "s"(rs), "s"(soff) : "memory");
}
template <int OFF> DI void ld128c(u32x4& d, u32 voff, u32x4 rs, u32 soff) {
  asm volatile("buffer_load_dwordx4 %0, %1, %2, %3 offen offset:%4 sc1" : "=v"(d) : "v"(voff), "s"(rs), "s"(soff), "i"(OFF) : "memory");
}
DI void ld128(u32x4& d, u32 voff, u32x4 rs, u32 soff) { asm volatile("buffer_load_dwordx4 %0, %1, %2, %3 offen" : "=v"(d) : "v"(voff), "s"(rs), "s"(soff) : "memory"); }
template <int OFF = 0> DI void ld32c(u32& d, u32 voff, u32x4 rs, u32 soff) { asm volatile("buffer_load_dword %0, %1, %2, %3 offen offset:%4 sc1" : "=v"(d) : "v"(voff), "s"(rs), "s"(soff), "i"(OFF) : "memory"); }
DI void st128c(u32x4 v, u32 voff, u32x4 rs, u32 soff) { asm volatile("buffer_store_dwordx4 %0, %1, %2, %3 offen sc1\n\ts_nop 1" ::"v"(v), "v"(voff), "s"(rs), "s"(soff) : "memory"); }
DI void st32c(u32 v, u32 voff, u32x4 rs, u32 soff) { asm volatile("buffer_store_dword %0, %1, %2, %3 offen sc1" ::"v"(v), "v"(voff), "s"(rs), "s"(soff) : "memory"); }
template <int N> DI void vmwait() { asm volatile("s_waitcnt vmcnt(%0)" ::"i"(N) : "memory"); }
template <int N> DI void vmwait(u32x4& a) { asm volatile("s_waitcnt vmcnt(%1)" : "+v"(a) : "i"(N) : "memory"); }
DI void bar() { asm volatile("s_waitcnt lgkmcnt(0)\n\ts_barrier" ::: "memory"); }
template <int N> DI void vmbar() { asm volatile("s_waitcnt vmcnt(%0) lgkmcnt(0)\n\ts_barrier" ::"i"(N) : "memory"); }

DI void ev_load(u32& e, u32x4 ws) { asm volatile("buffer_load_dword %0, off, %1, 0" : "+v"(e) : "s"(ws) : "memory"); }
template <int N> DI u32 tag_wait(u32 e) {
  u32 t;
  asm volatile("s_waitcnt vmcnt(%2)\n\tv_readfirstlane_b32 %0, %1\n\ts_nop 1" : "=s"(t) : "v"(e), "i"(N) : "memory");
  return t * 1664525u + 1013904223u;
}
DI void poll(u32 voff, u32x4 ws, u32 tag) {
  for (int it = 0; it < SPIN_MAX; ++it) {
    u32 f;
    asm volatile("buffer_load_dword %0, %1, %2, 0 offen sc1\n\ts_waitcnt vmcnt(0)" : "=&v"(f) : "v"(voff), "s"(ws) : "memory");
    if (__builtin_amdgcn_ballot_w64(f != tag) == 0) break;
    __builtin_amdgcn_s_sleep(4);
  }
}
DI void poll_bar(u32 voff, u32x4 ws, u32 tag) {
  u32 f;
  asm volatile("buffer_load_dword %0, %1, %2, 0 offen sc1\n\ts_waitcnt vmcnt(0) lgkmcnt(0)\n\ts_barrier" : "=&v"(f) : "v"(voff), "s"(ws) : "memory");
  if (__builtin_amdgcn_ballot_w64(f != tag) != 0) poll(voff, ws, tag);
}
template <int C> DI float dppadd(float v) { return v + __int_as_float(__builtin_amdgcn_update_dpp(0, __float_as_int(v), C, 0xf, 0xf, true)); }
DI float rowsum(float v) { v = dppadd<0xB1>(v); v = dppadd<0x4E>(v); v = dppadd<0x141>(v); return dppadd<0x140>(v); }
DI float rdl(float v, int l) { return __int_as_float(__builtin_amdgcn_readlane(__float_as_int(v), l)); }
DI float fold32(float a, float b) { const auto r = __builtin_amdgcn_permlane32_swap(__float_as_uint(a), __float_as_uint(b), false, false); return __uint_as_float(r[0]) + __uint_as_float(r[1]); }
DI float fold16(float a, float b) { const auto r = __builtin_amdgcn_permlane16_swap(__float_as_uint(a), __float_as_uint(b), false, false); return __uint_as_float(r[0]) + __uint_as_float(r[1]); }
template <bool E> DI u32 phi2(float a, float b) {
  constexpr float l2e = 1.4426950408889634f, c = -l2e * (E ? 1.000001f : 1.f);
  float e0 = __builtin_amdgcn_fmed3f(__builtin_amdgcn_exp2f(__builtin_fmaf(a, l2e, c)), 0.f, 1.f), e1 = __builtin_amdgcn_fmed3f(__builtin_amdgcn_exp2f(__builtin_fmaf(b, l2e, c)), 0.f, 1.f);
  if (E) { e0 += 1e-6f; asm("" : "+v"(e0)); e1 += 1e-6f; }
  return pk(__builtin_amdgcn_fmed3f(a, e0, 3.4028234664e38f), __builtin_amdgcn_fmed3f(b, e1, 3.4028234664e38f));
}
template <bool E> DI u32x4 phi8(const f32x16& x, int o) { return u32x4{phi2<E>(x[o], x[o + 1]), phi2<E>(x[o + 2], x[o + 3]), phi2<E>(x[o + 4], x[o + 5]), phi2<E>(x[o + 6], x[o + 7])}; }
DI float gelu(float v) {
  const float t = v * (-2.3022081981f - 0.1029432397f * v * v);
  return v * __builtin_amdgcn_rcpf(1.f + __builtin_amdgcn_exp2f(t));
}

constexpr int wslot(int P, int c) { return (c + (P && c < 3 ? 4 : 1)) % 5; }
struct WSrc { u32x4 rs; u32 voff, sbase, stride, lds, rot; };
template <int P> DI void w_one(const WSrc& s, int c, int i) { glds(s.lds + wslot(P, c) * 32768 + i * 1024, s.voff, s.rs, s.sbase + (64 * ((c + s.rot) & 7) + 8 * i) * s.stride); }
template <int P> DI void w_issue(const WSrc& s, int c) {
#pragma unroll
  for (int i = 0; i < 4; ++i) w_one<P>(s, c, i);
}
struct WA { u32 a[3][2]; };
DI WA w_addr(u32 a0) { WA r;
#pragma unroll
  for (int i = 0; i < 3; ++i) { r.a[i][0] = a0 + i * 65536; r.a[i][1] = (a0 ^ 64) + i * 65536; asm volatile("" : "+v"(r.a[i][0]), "+v"(r.a[i][1])); }
  return r; }
template <int P> DI void w_frag(const WA& r, u32x4 (&f)[2], int c, int ks) {
#pragma unroll
  for (int b = 0; b < 2; ++b) { const u32 a = r.a[wslot(P, c) >> 1][b] + (wslot(P, c) & 1) * 32768 + ks * 2048; f[b] = trd2(a, a + 1024); }
}
template <int P, int C, int F> DI void w_round(const WSrc& s, const WA& a0, u32x4 (&wr)[16][2]) {
#pragma unroll
  for (int k = 0; k < 4; ++k) { w_frag<P>(a0, wr[4 * C + k], C, k); SB(); if constexpr (F < 8) w_one<P>(s, F, k); SB(); }
}
DI void w_stage(const WA& a0, u32x4 (&wr)[16][2], int k0, int k1) {
#pragma unroll
  for (int k = k0; k < k1; ++k) w_frag<1>(a0, wr[k], k >> 2, k & 3);
}
template <int N> DI void vmbar0(int w) { if (w == 0) vmwait<N + 1>(); else vmwait<N>(); bar(); }
struct DSrc { u32x4 rs; u32 voff, sb, rot; };
template <int I> DI void d_issue(u32x4 (&d)[32], const DSrc& s) { ld128c<(I & 3) * 1024>(d[I], s.voff, s.rs, s.sb + (((I >> 2) + s.rot) & 7) * 4096); }
constexpr int KB = 32 - WIN;
template <int P, int K> DI void wr_steps(u32x4 (&d)[32], u32x4 (&wr)[16][2], const WA& a0, const DSrc& s, const WSrc& nx, u32 sig, f32x16& c0, f32x16& c1) {
  if constexpr (K < 32) {
    if constexpr (K == KB) {
      if constexpr (P == 0) bar();
      else { asm volatile("s_waitcnt lgkmcnt(0)" ::: "memory"); *(volatile LAS u32*)sig = 1; }
    }
    if constexpr (K % 8 == 0) __builtin_amdgcn_s_setprio(3 - K / 8);
    vmwait<(P == 0 || K < KB ? WIN - 1 : 31 - K)>(d[K]);
    c0 = mfma(wr[K & 15][0], d[K], c0); c1 = mfma(wr[K & 15][1], d[K], c1);
    asm volatile("" : "+v"(c0), "+v"(c1));
    if constexpr (K + WIN < 32) d_issue<K + WIN>(d, s);
    if constexpr (P == 0 && K == 1) bar();
    if constexpr (P == 0 && K >= KB) w_one<1>(nx, (K - KB) >> 2, (K - KB) & 3);
    if constexpr (K >= 1 && K <= 16) w_frag<P>(a0, wr[K - 1], 4 + ((K - 1) >> 2), (K - 1) & 3);
    wr_steps<P, K + 1>(d, wr, a0, s, nx, sig, c0, c1);
  }
}
template <int I> DI void d_first(u32x4 (&d)[32], const DSrc& s) { if constexpr (I < WIN) { d_issue<I>(d, s); d_first<I + 1>(d, s); } }
template <int P> DI void wr_gemm(u32x4 (&wr)[16][2], const WA& a0, const DSrc& s, const WSrc& nx, u32 sig, f32x16& c0, f32x16& c1) {
  u32x4 d[32];
  d_first<0>(d, s);
  c0 = splat(0.f); c1 = splat(0.f);
  wr_steps<P, 0>(d, wr, a0, s, nx, sig, c0, c1);
  if constexpr (P == 0) {
#pragma unroll
    for (int i = WIN; i < 12; ++i) w_one<1>(nx, i >> 2, i & 3);
  }
}
constexpr u32 RSL = 1088;
template <u32 RS> DI void red_write(u32 a, u32 bs, const f32x16& c0, const f32x16& c1) {
#pragma unroll
  for (int q = 0; q < 4; ++q) {
    *(LAS f32x4*)(a + q * RS) = f32x4{c0[4 * q], c0[4 * q + 1], c0[4 * q + 2], c0[4 * q + 3]};
    *(LAS f32x4*)(a + bs + q * RS) = f32x4{c1[4 * q], c1[4 * q + 1], c1[4 * q + 2], c1[4 * q + 3]};
  }
}

template <int O, int I> DI void put16(const f32x16& c) {
  if constexpr (I < 16) { asm volatile("ds_write_addtid_b32 %0 offset:%1" ::"v"(c[I]), "i"(O + (I >> 1) * 768 + (I & 1) * 272) : "memory"); put16<O, I + 1>(c); }
}

constexpr u32 AST = 49152;
struct ASrc { u32x4 rx, rw; u32 vx0, vx1, vw, vq, sx, s0, s1, sq, rot, L; int w; };
DI void a_one(const ASrc& s, int c, int j) {
  const u32 st = s.L + (c % 3) * AST, cc = (c + s.rot) & 7;
  if (j < 4) glds(st + s.w * 4096 + j * 1024, (j & 1) ? s.vx1 : s.vx0, s.rx, s.sx + j * 8192 + cc * 128);
  else glds(st + 32768 + (j - 4) * 8192 + s.w * 1024, s.vw, s.rw, (j == 4 ? s.s0 : s.s1) + cc * (64 * 3072));
}
template <bool KV, int C, int J> DI void a_pref(const ASrc& s, const WSrc& nx) {
  if constexpr (J < 0) {}
  else if constexpr (C < 8) a_one(s, C, J);
  else if constexpr (C == 8) { if constexpr (KV && J < 4) glds(s.L + 98304 + s.w * 4096 + J * 1024, s.vq, s.rw, s.sq + J * (16 * 3072)); }
  else w_one<1>(nx, (6 * (C - 9) + J) >> 2, (6 * (C - 9) + J) & 3);
}
constexpr int a_gap(int C, int g) {
  const int t0[6] = {5, 6, 9, 10, 13, 14}, t1[6] = {1, 3, 6, 9, 11, 14}, t2[6] = {1, 2, -1, -1, -1, -1};
  for (int j = 0; j < 6; ++j) if ((C == 2 ? t0[j] : C == 10 ? t2[j] : t1[j]) == g) return j;
  return -1;
}
struct AMisc { u32x4 rws; u32 cfoff, tag, rdy, clr; int lane; };
struct AAd { u32 x0[4], x1[4], wa; };
template <bool KV, int T> DI void a_steps(const ASrc& s, const WSrc& nx, AAd& ad, u32x4 (&x0)[33], u32x4 (&f)[2][3], f32x16 (&c)[4], u32& cf, const AMisc& ms) {
  if constexpr (T < 32) {
    constexpr int C = T >> 2, K = T & 3, P = T & 1, N = (T + 1) & 3;
    if constexpr (K == 3) {
      if constexpr (C < 6) vmbar<6 + ((KV && C == 1) || C == 3)>(); else if constexpr (C == 6) vmbar<KV ? 5 : 0>(); else vmbar<6>();
      if constexpr (C < 7) {
        constexpr u32 dl = (C + 1) % 3 ? AST : 0u - 2 * AST;
#pragma unroll
        for (int k = 0; k < 4; ++k) { ad.x0[k] += dl; ad.x1[k] += dl; }
        ad.wa += dl;
        asm volatile("" : "+v"(ad.x0[0]), "+v"(ad.x0[1]), "+v"(ad.x0[2]), "+v"(ad.x0[3]), "+v"(ad.x1[0]), "+v"(ad.x1[1]), "+v"(ad.x1[2]), "+v"(ad.x1[3]), "+v"(ad.wa));
      }
      if constexpr (C == 0 && KV) { if (s.w < 4) st128c(u32x4{~0u, ~0u, ~0u, ~0u}, ms.lane * 16, ms.rws, ms.clr); else st32c(~0u, (ms.lane & 31) * 4, ms.rws, ms.clr); }
      if constexpr (C == 2) { st32c(ms.tag, ms.rdy, ms.rws, 0); }
      if constexpr (C == 5 && KV) ld32c(cf, ms.cfoff, ms.rws, 0);
    }
    constexpr int PC = K == 3 ? C + 3 : C + 2, G0 = 4 * N;
    __builtin_amdgcn_s_setprio(3 - N);
    if constexpr (T < 31) { x0[T + 1] = lds128(ad.x0[N]); f[P ^ 1][1] = trd2(ad.wa + N * 2048, ad.wa + N * 2048 + 512); }
    SB();
    if constexpr (KV) c[0] = mfma(x0[T], f[P][1], c[0]); else c[0] = mfma(f[P][1], x0[T], c[0]);
    SB();
    if constexpr (T < 31) { f[P ^ 1][0] = lds128(ad.x1[N]); f[P ^ 1][2] = trd2(ad.wa + 8192 + N * 2048, ad.wa + 8192 + N * 2048 + 512); }
    a_pref<KV, PC, a_gap(PC, G0)>(s, nx);
    SB();
    if constexpr (KV) c[1] = mfma(f[P][0], f[P][1], c[1]); else c[1] = mfma(f[P][1], f[P][0], c[1]);
    SB();
    a_pref<KV, PC, a_gap(PC, G0 + 1)>(s, nx);
    SB();
    if constexpr (KV) c[2] = mfma(x0[T], f[P][2], c[2]); else c[2] = mfma(f[P][2], x0[T], c[2]);
    SB();
    a_pref<KV, PC, a_gap(PC, G0 + 2)>(s, nx);
    SB();
    if constexpr (KV) c[3] = mfma(f[P][0], f[P][2], c[3]); else c[3] = mfma(f[P][2], f[P][0], c[3]);
    SB();
    a_pref<KV, PC, a_gap(PC, G0 + 3)>(s, nx);
    SB();
    a_steps<KV, T + 1>(s, nx, ad, x0, f, c, cf, ms);
  }
}

DI ASrc a_src(const char* x, const char* wqkv, u32 L, int lane, int w, int xcd, int slot) {
  const bool kv = slot < 30;
  const int ai = slot - 22, rc = (xcd & 6) + (kv ? ai >> 2 : slot - 30), hg = (xcd & 1) * 4 + (ai & 3), qj = xcd & 1;
  ASrc sa;
  sa.rx = srd(x, 2048 * 1024); sa.rw = srd(wqkv, 512 * 3072); sa.L = L; sa.w = w;
  sa.rot = xcd & 6;
  sa.vx0 = (lane >> 3) * 1024 + ((lane & 7) ^ (lane >> 4)) * 16; sa.vx1 = sa.vx0 ^ 64;
  sa.vw = (lane >> 3) * 3072 + ((lane & 7) ^ ((lane >> 2) & 4)) * 16;
  sa.vq = (lane >> 2) * 3072 + (lane & 3) * 16;
  sa.sx = (rc * 256 + w * 32) * 1024;
  sa.s0 = w * 8 * 3072 + (kv ? 1024 + hg * 128 : 512 + qj * 256);
  sa.s1 = sa.s0 + (kv ? 1024 : 128);
  sa.sq = ((w + sa.rot) & 7) * (64 * 3072) + hg * 64;
  return sa;
}
DI WSrc w1_src(const char* w1, u32 lds, int lane, int w, int xcd, int slot, int& ft, int& dt1) {
  const int gj = slot < 16 ? (xcd >> 1) * 16 + slot : 48 + (xcd >> 1) * 6 + slot;
  const u32 wch = (lane & 7) ^ ((lane >> 2) & 4);
  ft = gj >> 2; dt1 = (xcd & 1) * 4 + (gj & 3);
  WSrc s1;
  s1.rs = srd(w1, 2048 * 2752); s1.stride = 2752; s1.lds = lds;
  s1.voff = (lane >> 3) * 2752 + (ft == 21 ? (wch & 3) : wch) * 16;
  s1.sbase = ((w >> 1) * 512 + (w & 1) * 32) * 2752 + ft * 128;
  s1.rot = (2 * (dt1 + ft)) & 7;
  return s1;
}
template <bool KV> DI u32 attn(u32 ev, const char* x, const char* wqkv, u32x4 rws, const WSrc& s2, u32 L, int lane, int w, int xcd, int slot, u32 wa0, u32x4 (&wr)[16][2]) {
  const int hh = lane >> 5, col = lane & 31, q4 = (lane & 15) >> 2, mi = w >> 1, ni = w & 1;
  const int ai = slot - 22, rc = (xcd & 6) + (KV ? ai >> 2 : slot - 30), hg = (xcd & 1) * 4 + (ai & 3), qj = xcd & 1;
  const ASrc sa = a_src(x, wqkv, L, lane, w, xcd, slot);
  AAd ad;
#pragma unroll
  for (int k = 0; k < 4; ++k) { ad.x0[k] = L + (mi * 64 + ni * 32 + col) * 128 + ((2 * k + hh) ^ ((col >> 1) & 7)) * 16; ad.x1[k] = ad.x0[k] ^ 4096; }
  ad.wa = L + 32768 + (8 * hh + q4) * 128 + 64 * ((q4 >> 1) ^ ni) + 32 * ((lane >> 4) & 1) + 8 * (lane & 3);
  const int pr = hg + (mi & 1) * 8;
  const bool ga = w < 4;
  const u32 cfoff = ga ? WS_F + xcd * FRS + (128 + (pr >> 1) * 8 + (lane & 7)) * 128 : OOB;
  asm volatile("" : "+v"(ad.x0[0]), "+v"(ad.x0[1]), "+v"(ad.x0[2]), "+v"(ad.x0[3]), "+v"(ad.x1[0]), "+v"(ad.x1[1]), "+v"(ad.x1[2]), "+v"(ad.x1[3]), "+v"(ad.wa)::"memory");
  const u32 tag = tag_wait<6>(ev);
  bar();
  const AMisc ms = {rws, cfoff, tag, lane == 0 ? WS_F + w * FRS + (KV ? 128 + hg * 8 + rc : 192 + qj * 8 + rc) * 128 : OOB, w < 4 ? WS_KV + ((hg * 2 + (w >> 1)) * 8 + rc) * 2048 + (w & 1) * 1024 : WS_Z + ((hg * 2 + (w & 1)) * 8 + rc) * 128, lane};
  u32x4 x0[33], f[2][3]; f32x16 c[4]; u32 cf;
  c[0] = c[1] = splat(KV ? 1.f : 1.000001f); c[2] = c[3] = splat(KV ? 0.f : 1.000001f);
  x0[0] = lds128(ad.x0[0]); f[0][0] = lds128(ad.x1[0]); f[0][1] = trd2(ad.wa, ad.wa + 512); f[0][2] = trd2(ad.wa + 8192, ad.wa + 8192 + 512);
  a_steps<KV, 0>(sa, s2, ad, x0, f, c, cf, ms);
  __builtin_amdgcn_s_setprio(0);
  asm volatile("" : "+v"(c[0]), "+v"(c[1]), "+v"(c[2]), "+v"(c[3]));
  const u32 pfl = WS_F + xcd * FRS + (512 + rc * 16 + 8 + qj) * 128;
  if constexpr (!KV) {
#pragma unroll
    for (int j = 0; j < 4; ++j)
#pragma unroll
      for (int h = 0; h < 2; ++h) { st128c(phi8<true>(c[j], 8 * h), lane * 16 + qj * 8192 + ni * 2048, rws, WS_P + (rc * 8 + (w ^ (j & 1))) * 16384 + ((j & 2) * 2 + h) * 1024); SB(); }
    asm volatile("s_barrier" ::: "memory");
    w_issue<1>(s2, 2);
    vmbar<4>();
    if (w == 0) st32c(tag, lane == 0 ? 0 : OOB, rws, pfl);
    if (blockIdx.x == 255 && w == 0) {
#pragma unroll 1
      for (int i = 0; i < 4; ++i) poll(WS_F + 7 * FRS + (i * 64 + lane) * 128, rws, tag);
      st32c(tag, lane == 0 ? 0 : OOB, rws, WS_E);
    }
    vmbar<0>();
    w_stage(w_addr(wa0), wr, 0, 12);
    bar();
    w_issue<1>(s2, 3); w_issue<1>(s2, 4); w_issue<1>(s2, 5);
    return tag;
  }
  const u32 S = L + 65536, KT = S + 14336, ZT = KT + 2048;
  if (w >= 4) w_issue<1>(s2, 2);
  const u32 on = ((lane >> 4) ^ (lane >> 2)) & 1 ? 0 : 0x3f803f80u;
  const u32x4 cs = {on, on, on, on};
  f32x16 d1 = splat(0.f); f32x4 d2 = {64e-6f, 64e-6f, 64e-6f, 64e-6f}, d3 = {0.f, 0.f, 0.f, 0.f};
  u32x4 p[4];
#pragma unroll
  for (int i = 0; i < 4; ++i) {
    const f32x16& k = c[i >> 1]; const int o = 8 * (i & 1);
    p[i][0] = phi2<false>(k[o], k[o + 1]); SB();
    if (i) d2 = mfma16(cs, p[i - 1], d2);
    const u32x4 v = pack8(c[2 + (i >> 1)], o);
    p[i][1] = phi2<false>(k[o + 2], k[o + 3]); SB();
    d3 = mfma16(cs, v, d3);
    p[i][2] = phi2<false>(k[o + 4], k[o + 5]); p[i][3] = phi2<false>(k[o + 6], k[o + 7]); SB();
    d1 = mfma(p[i], v, d1);
    SB(); if (w < 4) w_one<1>(s2, 2, i); SB();
  }
  d2 = mfma16(cs, p[3], d2);
  const bool lo = col < 16;
  f32x4 k0 = {lo ? d1[0] : d1[8], lo ? d1[1] : d1[9], lo ? d1[2] : d1[10], lo ? d1[3] : d1[11]};
  f32x4 k1 = {lo ? d1[4] : d1[12], lo ? d1[5] : d1[13], lo ? d1[6] : d1[14], lo ? d1[7] : d1[15]};
  const float ev6 = d3[0] * 1e-6f;
  k0 = add4(k0, f32x4{ev6, ev6, ev6, ev6}); k1 = add4(k1, f32x4{ev6, ev6, ev6, ev6});
  const float zp = d2[0];
  if (w >= 2) { *(LAS f32x4*)(S + (w - 2) * 2048 + lane * 16) = k0; *(LAS f32x4*)(S + (w - 2) * 2048 + 1024 + lane * 16) = k1; *(LAS float*)(S + 12288 + (w - 2) * 256 + lane * 4) = zp; }
  vmbar<0>();
  if (w < 2) {
    const u32 a = S + w * 2048 + lane * 16, z = S + 12288 + w * 256 + lane * 4;
    k0 = add4(add4(k0, ldsf4(a)), add4(ldsf4(a + 4096), ldsf4(a + 8192))); k1 = add4(add4(k1, ldsf4(a + 1024)), add4(ldsf4(a + 5120), ldsf4(a + 9216)));
    const float t = add1(add1(zp, ldsf(z)), add1(ldsf(z + 512), ldsf(z + 1024)));
    u32x4 p0 = __builtin_bit_cast(u32x4, k0), p1 = __builtin_bit_cast(u32x4, k1);
#pragma unroll
    for (int i = 0; i < 4; ++i) { p0[i] = umin(p0[i], ~1u); p1[i] = umin(p1[i], ~1u); }
    const u32 o = (hg * 2 + w) * 8 + rc;
    st128c(p0, lane * 16, rws, WS_KV + o * 2048); st128c(p1, lane * 16, rws, WS_KV + o * 2048 + 1024); st32c(umin(__float_as_uint(t), ~1u), col * 4, rws, WS_Z + o * 128);
  }
  u32x4 pq[2][2], t[8]; u32x2 z; u32 pf;
  u32 qa = L + 98304 + (8 * hh + q4) * 64 + 32 * ((lane >> 4) & 1) + 8 * (lane & 3);
  asm volatile("" : "+v"(qa), "+v"(cf));
  u32x4 qf[4];
#pragma unroll
  for (int i = 0; i < 3; ++i) qf[i] = trd2(qa + i * 1024, qa + i * 1024 + 256);
  f32x16 cq = splat(1.000001f);
#pragma unroll
  for (int i = 0; i < 32; ++i) {
    SB();
    if (i < 29) qf[(i + 3) & 3] = trd2(qa + (i + 3) * 1024, qa + (i + 3) * 1024 + 256);
    SB();
    cq = mfma(qf[i & 3], x0[i], cq);
  }
  SB();
  auto sweep = [&]() {
#pragma unroll
    for (int r = 0; r < 8; ++r) ld128c<0>(t[r], lane * 16, rws, WS_KV + (pr * 8 + r) * 2048 + ni * 1024);
    asm volatile("buffer_load_dwordx2 %0, %1, %2, %3 offen sc1" : "=v"(z) : "v"((lane >> 3) * 128 + (lane & 7) * 8), "s"(rws), "s"(WS_Z + pr * 1024 + ni * 64) : "memory");
  };
  asm volatile("" : "=v"(t[0]), "=v"(t[1]), "=v"(t[2]), "=v"(t[3]), "=v"(t[4]), "=v"(t[5]), "=v"(t[6]), "=v"(t[7]), "=v"(z));
  if (ga && __builtin_amdgcn_ballot_w64(cf != tag) != 0) poll(cfoff, rws, tag);
  ld32c(pf, pfl, rws, 0);
  if (mi == 0) sweep();
  pq[0][0] = phi8<true>(cq, 0); pq[0][1] = phi8<true>(cq, 8);
  asm volatile("" : "+v"(pq[0][0]), "+v"(pq[0][1]));
  w_stage(w_addr(wa0), wr, 0, 12);
  SB();
  if (mi == 1) sweep();
  if (!ga) vmwait<0>();
  asm volatile("s_waitcnt vmcnt(9)" : "+v"(pf)::"memory");
  if (__builtin_amdgcn_ballot_w64(pf != tag) != 0) poll(pfl, rws, tag);
  ld128c<0>(pq[1][0], lane * 16, rws, WS_P + (rc * 8 + w) * 16384 + hg * 2048); ld128c<1024>(pq[1][1], lane * 16, rws, WS_P + (rc * 8 + w) * 16384 + hg * 2048);
#define GW(N) asm volatile("s_waitcnt vmcnt(%9)" : "+v"(t[0]), "+v"(t[1]), "+v"(t[2]), "+v"(t[3]), "+v"(t[4]), "+v"(t[5]), "+v"(t[6]), "+v"(t[7]), "+v"(z) : "i"(N) : "memory")
  GW(2);
  if (ga) {
    for (int it = 0; it < SPIN_MAX; ++it) {
      u32 m = umax(z[0], z[1]);
#pragma unroll
      for (int r = 0; r < 8; ++r) m = umax(umax(umax(m, t[r][0]), t[r][1]), umax(t[r][2], t[r][3]));
      if (__builtin_amdgcn_ballot_w64(m == ~0u) == 0) break;
      sweep();
      GW(0);
    }
    f32x4 a = __builtin_bit_cast(f32x4, t[0]);
#pragma unroll
    for (int r = 1; r < 8; ++r) a += __builtin_bit_cast(f32x4, t[r]);
    float zs = fold32(__uint_as_float(z[0]), __uint_as_float(z[1]));
    zs = dppadd<0x128>(fold16(zs, zs));
    const int pl = mi, zf = 2 * (lane & 7) + hh;
    *(LAS u32x2*)(KT + pl * 1024 + lane * 16 + ni * 8) = u32x2{pk(a[0], a[1]), pk(a[2], a[3])};
    *(LAS unsigned short*)(ZT + pl * 64 + ni * 32 + ((zf >> 2) & 1) * 16 + ((zf & 3) + 4 * (zf >> 3)) * 2) = (unsigned short)pk(zs, zs);
  }
  bar();
  f32x16 dd[2][2];
#pragma unroll
  for (int j = 0; j < 2; ++j) {
    if (j) asm volatile("s_waitcnt vmcnt(0)" : "+v"(pq[1][0]), "+v"(pq[1][1])::"memory");
#pragma unroll
    for (int h = 0; h < 2; ++h) dd[j][h] = mfma(lds128(((col < 16) == (h == 0)) ? KT + j * 1024 + lane * 16 : ZT + j * 64 + h * 32 + hh * 16), pq[j][h], splat(0.f));
  }
  SB();
  w_issue<1>(s2, 3);
  SB();
#pragma unroll
  for (int j = 0; j < 2; ++j)
#pragma unroll
    for (int h = 0; h < 2; ++h) {
      const int o = 8 * h;
      const f32x16& e = dd[j][h];
      const float rd = __builtin_amdgcn_rcpf(e[8 - o] + 1e-6f);
      const u32x4 ov = {pk(e[o] * rd, e[o + 1] * rd), pk(e[o + 2] * rd, e[o + 3] * rd), pk(e[o + 4] * rd, e[o + 5] * rd), pk(e[o + 6] * rd, e[o + 7] * rd)};
      st128c(ov, lane * 16, rws, WS_O + (rc * 8 + w) * 32768 + ((hg + 8 * j) * 2 + h) * 1024);
    }
  bar();
  w_issue<1>(s2, 4);
  vmbar<4>();
  if (w == 0) st32c(tag, lane < 2 ? ((rc & 6) + lane) * FRS : OOB, rws, WS_F + (512 + rc * 16 + hg) * 128);
  w_issue<1>(s2, 5);
  return tag;
}

__global__ __launch_bounds__(512, 2) void fused_kernel(const char* __restrict__ x, const char* __restrict__ wqkv, const char* __restrict__ wo,
                                                        const char* __restrict__ w1, const char* __restrict__ b1, const char* __restrict__ w2,
                                                        const char* __restrict__ b2, const char* __restrict__ gw, char* __restrict__ out, char* ws) {
  __shared__ __attribute__((aligned(1024))) char sm[163840];
  u32 ev;
  {
    unsigned long long pc; u32 cn;
    asm volatile("s_getpc_b64 %0\n.Lfk_pc:\n\ts_mov_b32 %1, .Lfk_end-.Lfk_pc" : "=s"(pc), "=s"(cn));
    const u32 vo = (blockIdx.x >> 3 & 15) == 0 ? threadIdx.x * 128 : OOB;
    asm volatile("buffer_load_dword %0, %1, %2, 0 offen" : "=v"(ev) : "v"(vo), "s"(srd((const void*)pc, cn)) : "memory");
    SB();
  }
  asm volatile("" : "+s"(x), "+s"(wqkv), "+s"(wo), "+s"(w1), "+s"(b1), "+s"(w2), "+s"(b2), "+s"(gw), "+s"(out), "+s"(ws));
  const u32 L = (u32)(size_t)(LAS char*)sm;
  const int lane = threadIdx.x & 63, w = __builtin_amdgcn_readfirstlane(threadIdx.x >> 6);
  int xcd = blockIdx.x & 7, slot = blockIdx.x >> 3;
  const int hh = lane >> 5;
  const u32x4 rws = srd(ws, WS_END);
  u32x4 xr[2], gr;
  asm volatile("s_nop 4" : "=v"(xr[0]), "=v"(xr[1]), "=v"(gr)::"memory");
  ev_load(ev, rws);
  if (slot < 22) {
    int ft, dt1;
    const WSrc s1 = w1_src(w1, L + w * 4096, lane, w, xcd, slot, ft, dt1);
    if (slot < 16) {
      const int r = (xcd * 16 + slot) * 16 + w;
      const u32x4 rx = srd(x, 2048 * 1024);
      ld128(xr[0], lane * 16, rx, r * 1024); ld128(xr[1], lane * 16, rx, (r + 8) * 1024);
      ld128(gr, lane * 16, srd(gw, 1024), 0);
      asm volatile("s_barrier" ::: "memory");
    }
    w_issue<0>(s1, 0);
  } else {
    const ASrc sa = a_src(x, wqkv, L, lane, w, xcd, slot);
#pragma unroll
    for (int j = 0; j < 6; ++j) a_one(sa, 0, j);
    asm volatile("s_barrier" ::: "memory");
#pragma unroll
    for (int j = 0; j < 6; ++j) a_one(sa, 1, j);
  }
  asm volatile("" : "+s"(slot), "+s"(xcd));
  const int g = w >> 1, hf = w & 1, q4 = (lane & 15) >> 2;
  const u32 wch = (lane & 7) ^ ((lane >> 2) & 4);
  const u32 wa0 = L + g * 8192 + (4 * hh + q4) * 128 + 64 * (q4 >> 1) + 32 * ((lane >> 4) & 1) + 8 * (lane & 3);

  const int dt = (xcd & 1) * 4 + (slot & 3), stl = (xcd >> 1) * 8 + (slot >> 2);
  WSrc s2;
  s2.rs = g < 3 ? srd(w2, 1376 * 4096) : srd(wo, 512 * 1024); s2.stride = g < 3 ? 4096 : 1024; s2.lds = L + g * 8192 + hf * 4096;
  s2.voff = (lane >> 3) * s2.stride + wch * 16;
  s2.sbase = g < 3 ? (g * 512 + hf * 32) * 4096 + stl * 128 : hf * 32 * 1024 + dt * 128;
  s2.rot = 2 * ((slot + (slot >> 2)) & 3) + (xcd & 1);
  u32 tag;
  u32x4 wr[16][2];

  if (slot >= 30) tag = attn<false>(ev, x, wqkv, rws, s2, L, lane, w, xcd, slot, wa0, wr);
  else if (slot >= 22) tag = attn<true>(ev, x, wqkv, rws, s2, L, lane, w, xcd, slot, wa0, wr);
  else {
    const int gi = slot < 16 ? xcd * 16 + slot : 112 + xcd * 6 + slot;
    int ft, dt1;
    const WSrc s1 = w1_src(w1, s2.lds, lane, w, xcd, slot, ft, dt1);
    if (slot < 16) {
      const int sb = xcd * 16 + slot;
      tag = tag_wait<4>(ev);
      asm volatile("" : "+v"(xr[0]), "+v"(xr[1]), "+v"(gr)::"memory");
      f32x2 v[2][4], t; float s[2], gd[2], q[2];
#pragma unroll
      for (int j = 0; j < 2; ++j) {
#pragma unroll
        for (int i = 0; i < 4; ++i) v[j][i] = f32x2{blo(xr[j][i]), bhi(xr[j][i])};
        t = (v[j][0] + v[j][1]) + (v[j][2] + v[j][3]); s[j] = t[0] + t[1];
      }
      const float t0 = fold32(s[0], s[1]), t1 = rowsum(fold16(t0, t0)) * (1.f / 512.f);
#pragma unroll
      for (int j = 0; j < 2; ++j) {
        t = f32x2{0.f, 0.f};
#pragma unroll
        for (int i = 0; i < 4; ++i) t = __builtin_elementwise_fma(v[j][i], f32x2{blo(gr[i]), bhi(gr[i])}, t);
        gd[j] = t[0] + t[1];
        const float m = rdl(t1, 32 * j);
        t = f32x2{0.f, 0.f};
#pragma unroll
        for (int i = 0; i < 4; ++i) { v[j][i] -= m; t = __builtin_elementwise_fma(v[j][i], v[j][i], t); }
        q[j] = t[0] + t[1];
      }
      const float t2 = rowsum(fold16(fold32(q[0], q[1]), fold32(gd[0], gd[1])));
      const float rs = __builtin_amdgcn_rsqf(t2 * (1.f / 512.f) + 1e-5f);
#pragma unroll
      for (int j = 0; j < 2; ++j) {
        const float r = rdl(rs, 32 * j);
#pragma unroll
        for (int i = 0; i < 4; ++i) v[j][i] *= r;
        *(LAS u32x4*)(L + (w + 8 * j) * 1088 + lane * 16) = u32x4{pk(v[j][0][0], v[j][0][1]), pk(v[j][1][0], v[j][1][1]), pk(v[j][2][0], v[j][2][1]), pk(v[j][3][0], v[j][3][1])};
      }
      const float gt = __builtin_amdgcn_rcpf(1.f + __builtin_amdgcn_exp2f(t2 * -1.4426950408889634f));
      bar();
      const u32 ta = L + (4 * hh + q4) * 1088 + w * 128 + 32 * ((lane >> 4) & 1) + 8 * (lane & 3);
      const u32x4 h0 = trd2(ta, ta + 8 * 1088), h1 = trd2(ta + 64, ta + 64 + 8 * 1088);
      st32c(__float_as_uint(gt), (lane & 31) == 16 ? lane & 32 : OOB, rws, WS_G + (sb * 16 + w) * 4);
      st128c(h0, lane * 16, rws, WS_H + w * 262144 + sb * 1024);
      st128c(h1, lane * 16, rws, WS_H + w * 262144 + 131072 + sb * 1024);
      asm volatile("s_barrier" ::: "memory");
      w_issue<0>(s1, 1);
      vmbar<4>();
    } else {
      u32 tl;
      const int k = slot - 16, nl = w == 7 ? 44 : w == 6 ? (k < 4 ? 22 : 10) : w == 3 || w == 4 ? 6 : k == w ? 64 : 0;
      asm volatile("buffer_load_dword %0, %1, %2, %3 offen sc1" : "=v"(tl) : "v"(lane >= nl ? OOB : w == 7 ? (lane >> 1) * 3072 + (lane & 1) * 128 : lane << (nl == 64 ? 18 : 7)),
                   "s"(w == 0 ? srd(w2, 1376 * 4096) : w == 1 ? srd(wo, 512 * 1024) : w == 2 ? srd(out, 2048 * 1024) : w == 3 ? srd(b1, 2752) : w == 4 ? srd(b2, 4096) : w == 7 ? srd(wqkv, 512 * 3072) : rws),
                   "s"(w == 7 ? ((xcd >> 1) * 6 + k) * (22 * 3072) + (xcd & 1) * 256 : w == 6 ? WS_F + xcd * FRS + (k < 4 ? 256 + ((xcd & 1) * 4 + k) * 32 : 448 + ((xcd & 6) + k) * 16) * 128 : k * 768) : "memory");
      w_issue<0>(s1, 1);
      tag = tag_wait<4>(ev);
      asm volatile("" ::"v"(tl));
      bar();
    }
    const WA a0 = w_addr(wa0);
    if (w == 0) st32c(tag, lane < 8 ? lane * FRS : OOB, rws, WS_F + (slot < 16 ? gi : gi + 80) * 128);
    w_round<0, 0, 2>(s1, a0, wr); w_issue<0>(s1, 3);
    vmbar0<8>(w);
    w_round<0, 1, 4>(s1, a0, wr);
    vmbar<8>();
    w_round<0, 2, 5>(s1, a0, wr);
    vmbar<8>();
    const u32 hfl = hf ? WS_F + xcd * FRS + (g * 32 + (lane & 31)) * 128 : OOB;
    u32 pf; u32x2 bb0, bb1;
    ld32c(pf, hfl, rws, 0);
    asm volatile("buffer_load_dwordx2 %0, %2, %3, %4 offen\n\tbuffer_load_dwordx2 %1, %2, %3, %4 offen offset:16" : "=&v"(bb0), "=&v"(bb1) : "v"(hh * 8), "s"(srd(b1, 2752)), "s"(ft * 128 + g * 32) : "memory");
    w_round<0, 3, 6>(s1, a0, wr); w_issue<0>(s1, 7);
    asm volatile("s_waitcnt vmcnt(8)" : "+v"(pf), "+v"(bb0), "+v"(bb1)::"memory");
    if (hf && __builtin_amdgcn_ballot_w64(pf != tag) != 0) poll(hfl, rws, tag);
    bar();
    f32x16 c0, c1;
    wr_gemm<0>(wr, a0, DSrc{rws, (u32)lane * 16, WS_H + (dt1 * 2 + hf) * 131072 + g * 32768, s1.rot}, s2, 0, c0, c1);
    const u32 R = L + 65536 + hf * 32768 + lane * 16;
    f32x4 lo = {blo(bb0[0]), bhi(bb0[0]), blo(bb0[1]), bhi(bb0[1])}, hi = {blo(bb1[0]), bhi(bb1[0]), blo(bb1[1]), bhi(bb1[1])};
#pragma unroll
    for (int q = 0; q < 4; ++q) {
      const f32x16& c = q < 2 ? c0 : c1; const int o = (q & 1) * 8;
      const f32x4 a = {c[o], c[o + 1], c[o + 2], c[o + 3]}, b = {c[o + 4], c[o + 5], c[o + 6], c[o + 7]};
      if (q == g) { lo += a; hi += b; }
      else { const u32 t = R + q * 8192 + ((g - q) & 3) * 2048; *(LAS f32x4*)t = a; *(LAS f32x4*)(t + 1024) = b; }
    }
    bar();
#pragma unroll
    for (int k = 1; k < 4; ++k) { lo += ldsf4(R + g * 8192 + k * 2048); hi += ldsf4(R + g * 8192 + k * 2048 + 1024); }
    const u32x4 uv = {pk(gelu(lo[0]), gelu(lo[1])), pk(gelu(lo[2]), gelu(lo[3])), pk(gelu(hi[0]), gelu(hi[1])), pk(gelu(hi[2]), gelu(hi[3]))};
    st128c(uv, lane * 16, rws, WS_U + (dt1 * 2 + hf) * UDB + (ft * 4 + g) * 1024);
    vmbar<0>();
    if (w == 0) st32c(tag, lane < 4 ? (lane * 2 + (dt1 >> 2)) * FRS : OOB, rws, WS_F + (256 + dt1 * 32 + ft) * 128);
    w_stage(a0, wr, 0, 12);
    bar();
    w_issue<1>(s2, 3); w_issue<1>(s2, 4); w_issue<1>(s2, 5);
  }

  {
    asm volatile("" : "+s"(s2.lds), "+s"(s2.rot), "+s"(s2.sbase));
    const int sr = w * 8 + (lane >> 3), cq = lane & 7;
    u32 b2v;
    asm volatile("buffer_load_ushort %0, %1, %2, %3 offen" : "=v"(b2v) : "v"(sr * 2), "s"(srd(b2, 4096)), "s"(stl * 128) : "memory");
    const u32 GT = L + 131072, SG = GT + 256;
    const WA a0 = w_addr(wa0);
    f32x16 c0, c1;
    const int nf = g == 2 ? 6 : 8;
    const u32 fo = WS_F + xcd * FRS + ((g < 3 ? 256 + dt * 32 + g * 8 : 512 + (stl >> 2) * 16) + (lane < nf ? lane : 0)) * 128;
    w_issue<1>(s2, 6);
    w_issue<1>(s2, 7);
    vmbar<17>();
    w_round<1, 3, 8>(s2, a0, wr);
    bar();
    *(volatile LAS u32*)(SG + w * 4) = 0;
    poll_bar(fo, rws, tag);
    asm volatile("" : "+v"(b2v));
    u32 gv;
    ld32c(gv, w == 0 ? lane * 4 : OOB, rws, WS_G + stl * 256);
    const u32 ub = WS_U + (dt * 2 + hf) * UDB;
    wr_gemm<1>(wr, a0, DSrc{srd(ws, g < 3 ? ub + 86 * 1024 : WS_END), (u32)lane * 16, g < 3 ? ub + g * 32768 : WS_O + (stl * 2 + hf) * 32768, s2.rot}, s2, SG + w * 4, c0, c1);
    asm volatile("" : "+v"(gv));
    if (w == 0) *(LAS u32*)(GT + lane * 4) = gv;
    for (int it = 0; it < SPIN_MAX * 16 && *(volatile LAS u32*)(SG + (w ^ 1) * 4) == 0; ++it) {}
    const u32 pb = L + g * 8192 + hf * 32768;
    if (g < 3) {
      asm volatile("s_mov_b32 m0, %0\n\ts_nop 15\n\ts_nop 7" ::"s"(pb + hf * 128) : "memory");
      put16<0, 0>(c0);
      asm volatile("s_mov_b32 m0, %0\n\ts_nop 0" ::"s"(pb + hf * 128 + 6144) : "memory");
      put16<65536 - 6144, 0>(c1);
    } else red_write<RSL>(pb + lane * 16, 65536, c0, c1);
    bar();
    const int sl = lane >> 3, i = (sl & 3) + 4 * (w & 3);
    const u32 ya = L + (cq >> 2) * (32768 + 128) + (w >> 2) * 65536 + (i >> 1) * 768 + (i & 1) * 272 + (sl >> 2) * 128 + (cq & 3) * 32;
    const f32x4 y0 = (ldsf4(ya) + ldsf4(ya + 8192)) + ldsf4(ya + 16384), y1 = (ldsf4(ya + 16) + ldsf4(ya + 8208)) + ldsf4(ya + 16400);
    const u32 aa = L + 24576 + (w >> 2) * 32768 + (cq >> 2) * 65536 + (cq & 3) * RSL + (sr & 31) * 16;
    const f32x4 a0v = ldsf4(aa), a1v = ldsf4(aa + 512);
    const float gt = ldsf(GT + sr * 4), bb = blo(b2v), og = 1.f - gt;
    u32x4 ov;
    ov[0] = pk(gt * a0v[0] + og * (y0[0] + bb), gt * a0v[1] + og * (y0[1] + bb));
    ov[1] = pk(gt * a0v[2] + og * (y0[2] + bb), gt * a0v[3] + og * (y0[3] + bb));
    ov[2] = pk(gt * a1v[0] + og * (y1[0] + bb), gt * a1v[1] + og * (y1[1] + bb));
    ov[3] = pk(gt * a1v[2] + og * (y1[2] + bb), gt * a1v[3] + og * (y1[3] + bb));
    asm volatile("buffer_store_dwordx4 %0, %1, %2, %3 offen nt" ::"v"(ov), "v"(sr * 1024 + cq * 16), "s"(srd(out, 2048 * 1024)), "s"(stl * 65536 + dt * 128) : "memory");
    asm volatile(".Lfk_end:");
  }
}

extern "C" void kernel_launch(void* const* d_in, const int* in_sizes, int n_in, void* d_out, int out_size, void* d_ws, size_t ws_size, hipStream_t stream) {
  fused_kernel<<<256, 512, 0, stream>>>((const char*)d_in[0], (const char*)d_in[1], (const char*)d_in[2], (const char*)d_in[4], (const char*)d_in[5],
                                        (const char*)d_in[6], (const char*)d_in[7], (const char*)d_in[8], (char*)d_out, (char*)d_ws);
}
```
